```python
import torch

def _softmax(x, axis=-1):
    return torch.softmax(x, dim=axis)
def _gelu(x):
    return torch.nn.functional.gelu(x, approximate="tanh")
def _silu(x):
    return torch.nn.functional.silu(x)
def _sigmoid(x):
    return torch.sigmoid(x)
def _layernorm(x, axis=-1, eps=1e-5):
    m = x.mean(dim=axis, keepdim=True)
    v = x.var(dim=axis, keepdim=True, unbiased=False)
    return (x - m) / torch.sqrt(v + eps)
def _scatter_add(tgt, idx, src, axis=0):
    return tgt.clone().index_add_(axis, idx, src)
def _segment_sum(src, seg, n):
    out = torch.zeros((n,) + src.shape[1:], dtype=src.dtype)
    return out.index_add_(0, seg, src)
def _pad(x, pw):
    flat = tuple(v for pair in reversed(pw) for v in pair)
    return torch.nn.functional.pad(x, flat)

kernel_name = 'recurrent_scan__gqa_block_cached__mixer_channel_mlp__decode'

def setup_inputs():

    _w = (lambda a: (lambda t: t.to(torch.bfloat16) if t.is_floating_point() else t)(torch.as_tensor(a)))
    def _A_setup():
        B, Sq, D = 2, 1, 1024
        Hq, Hkv, Sk, Dh = 32, 4, 16384, 128
        assert Hq % Hkv == 0, (Hq, Hkv)
        return {
            "x": _w((lambda *s: torch.randn(*s, dtype=torch.bfloat16) * 0.1)(B, Sq, D)),
            "k_cache": _w((lambda *s: torch.randn(*s, dtype=torch.bfloat16) * 0.8)(B, Hkv, Sk, Dh)),
            "v_cache": _w((lambda *s: torch.randn(*s, dtype=torch.bfloat16) * 3.2)(B, Hkv, Sk, Dh)),
            "wq": _w((lambda *s: torch.randn(*s, dtype=torch.bfloat16) * 0.8)(D, Hq * Dh) * (D ** -0.5)),
            "wo": _w((lambda *s: torch.randn(*s, dtype=torch.bfloat16) * 3.2)(Hq * Dh, D) * ((Hq * Dh) ** -0.5)),
        }
    _A_in = _A_setup()

    def _B_setup():
        B, S, D, F = 2, 1, 1024, 3584
        return {
            "x": _w((lambda *s: torch.randn(*s, dtype=torch.bfloat16) * 0.1)(B, S, D)),
            "w1": _w((lambda *s: torch.randn(*s, dtype=torch.bfloat16) * 3.2)(D, F) * (D ** -0.5)),
            "b1": _w((lambda *s: torch.randn(*s, dtype=torch.bfloat16) * 3.2)(F)),
            "w2": _w((lambda *s: torch.randn(*s, dtype=torch.bfloat16) * 3.2)(F, D) * (F ** -0.5)),
            "b2": _w((lambda *s: torch.randn(*s, dtype=torch.bfloat16) * 3.2)(D)),
        }
    _B_in = _B_setup()

    return {
        "A_x": _A_in["x"],
        "A_k_cache": _A_in["k_cache"],
        "A_v_cache": _A_in["v_cache"],
        "A_wq": _A_in["wq"],
        "A_wo": _A_in["wo"],
        "B_x": _B_in["x"],
        "B_w1": _B_in["w1"],
        "B_b1": _B_in["b1"],
        "B_w2": _B_in["w2"],
        "B_b2": _B_in["b2"],
    }

def reference(A_x, A_k_cache, A_v_cache, A_wq, A_wo, B_x, B_w1, B_b1, B_w2, B_b2):
    def _A_ref(x, k_cache, v_cache, wq, wo):
        B, Sq, D = x.shape
        Hq, Dh = 32, 128
        Hkv = k_cache.shape[1]
        rep = Hq // Hkv
        q = (lambda x, *d: x.permute(*d))((x @ wq).reshape(B, Sq, Hq, Dh), 0, 2, 1, 3)
        k = (lambda x, n, axis: torch.repeat_interleave(x, n, dim=axis))(k_cache, rep, axis=1)
        v = (lambda x, n, axis: torch.repeat_interleave(x, n, dim=axis))(v_cache, rep, axis=1)
        scores = torch.einsum("bhqd,bhkd->bhqk", q, k) * (Dh ** -0.5)
        p = _softmax(scores, axis=-1)
        o = torch.einsum("bhqk,bhkd->bhqd", p, v)
        return (lambda x, *d: x.permute(*d))(o, 0, 2, 1, 3).reshape(B, Sq, Hq * Dh) @ wo

    def _B_ref(x, w1, b1, w2, b2):
        y = _layernorm(x, axis=-1)
        h = _gelu(torch.einsum("bsd,df->bsf", y, w1) + b1)
        return torch.einsum("bsf,fd->bsd", h, w2) + b2

    x = A_x
    xn = _layernorm(x, axis=-1)
    s = _A_ref(xn, A_k_cache, A_v_cache, A_wq, A_wo)
    g = _silu(_B_ref(xn, B_w1, B_b1, B_w2, B_b2))
    return x + s * g
```

```cpp
#include <hip/hip_runtime.h>
#include <hip/hip_bf16.h>

namespace {

typedef unsigned v4u __attribute__((ext_vector_type(4)));
typedef unsigned v2u __attribute__((ext_vector_type(2)));
typedef float f4 __attribute__((ext_vector_type(4)));
typedef float f2 __attribute__((ext_vector_type(2)));
typedef __bf16 bf8 __attribute__((ext_vector_type(8)));
typedef __bf16 bf2 __attribute__((ext_vector_type(2)));
typedef short s4 __attribute__((ext_vector_type(4)));
typedef __amdgpu_buffer_rsrc_t rsrc_t;
#define LAS __attribute__((address_space(3)))

constexpr int NT = 512;
constexpr unsigned KVB = 2u * 4 * 16384 * 128 * 2, WQB = 1024u * 4096 * 2, W1B = 1024u * 3584 * 2;
constexpr unsigned WS_E = 0;
constexpr unsigned WS_QP = 4096;
constexpr unsigned WS_HP = WS_QP + 262144;
constexpr unsigned WS_ST = WS_HP + 229376;
constexpr unsigned WS_OP = WS_ST + 32768;
constexpr unsigned WS_FP = WS_OP + 1048576;
constexpr unsigned WS_MP = WS_FP + 524288;
constexpr unsigned WS_END = WS_MP + 524288;
constexpr int L_XS = 0, L_YS = 4096, L_RED = 8192, L_QS = 8448, L_ST = L_QS + 8 * 272, L_WT = L_ST + 512,
              L_R1 = L_WT + 8 * 256, L_R2 = L_R1 + 16384, L_RO = L_R2 + 16384, L_END = L_RO + 8 * 4608;
constexpr int LDS_BYTES = 84 * 1024;
static_assert(L_END <= LDS_BYTES && (L_QS % 16) == 0 && (L_ST % 16) == 0 && (L_WT % 16) == 0 && (L_R1 % 16) == 0, "lds map");
constexpr int AUX_NT = 2, AUX_SC1 = 16;
constexpr unsigned SPIN_CAP = 1u << 15;
constexpr float SM_C = 0.088388347648318440f * 1.4426950408889634f;

__device__ __forceinline__ rsrc_t mk(const void* p, unsigned bytes) { return __builtin_amdgcn_make_buffer_rsrc((void*)p, 0, bytes, 0x00020000); }
__device__ __forceinline__ v4u ldnt(rsrc_t r, int vo, int so) { return __builtin_amdgcn_raw_buffer_load_b128(r, vo, so, AUX_NT); }
__device__ __forceinline__ v4u ldc4(rsrc_t r, int vo, int so) { return __builtin_amdgcn_raw_buffer_load_b128(r, vo, so, AUX_SC1); }
__device__ __forceinline__ v2u ldc2(rsrc_t r, int vo, int so) { return __builtin_amdgcn_raw_buffer_load_b64(r, vo, so, AUX_SC1); }
__device__ __forceinline__ void stc4(rsrc_t r, int vo, int so, v4u v) { __builtin_amdgcn_raw_buffer_store_b128(v, r, vo, so, AUX_SC1); }
__device__ __forceinline__ float asf(unsigned u) { return __builtin_bit_cast(float, u); }
__device__ __forceinline__ unsigned asu(float f) { return __builtin_bit_cast(unsigned, f); }
__device__ __forceinline__ unsigned pk(float a, float b) { f2 v = {a, b}; return __builtin_bit_cast(unsigned, __builtin_convertvector(v, bf2)); }
__device__ __forceinline__ float blo(unsigned u) { return asf(u << 16); }
__device__ __forceinline__ float bhi(unsigned u) { return asf(u & 0xffff0000u); }
__device__ __forceinline__ float rb(float a) { return blo(pk(a, a)); }
__device__ __forceinline__ unsigned plo(unsigned a, unsigned b) { return __builtin_amdgcn_perm(b, a, 0x05040100u); }
__device__ __forceinline__ unsigned phi(unsigned a, unsigned b) { return __builtin_amdgcn_perm(b, a, 0x07060302u); }
__device__ __forceinline__ f4 mfma32(v4u a, v4u b, f4 c) { return __builtin_amdgcn_mfma_f32_16x16x32_bf16(__builtin_bit_cast(bf8, a), __builtin_bit_cast(bf8, b), c, 0, 0, 0); }
__device__ __forceinline__ f4 mfma16(v2u a, v2u b, f4 c) { return __builtin_amdgcn_mfma_f32_16x16x16bf16_1k(__builtin_bit_cast(s4, a), __builtin_bit_cast(s4, b), c, 0, 0, 0); }
template <int C> __device__ __forceinline__ float dpp(float x) { return asf(__builtin_amdgcn_mov_dpp(asu(x), C, 0xf, 0xf, true)); }
constexpr int XOR1 = 0xB1, XOR2 = 0x4E, ROR4 = 0x124, ROR8 = 0x128;
__device__ __forceinline__ float x16_sum(float x) { auto s = __builtin_amdgcn_permlane16_swap(asu(x), asu(x), false, false); return asf(s[0]) + asf(s[1]); }
__device__ __forceinline__ float x32_sum(float x) { auto s = __builtin_amdgcn_permlane32_swap(asu(x), asu(x), false, false); return asf(s[0]) + asf(s[1]); }
__device__ __forceinline__ float x16_max(float x) { auto s = __builtin_amdgcn_permlane16_swap(asu(x), asu(x), false, false); return fmaxf(asf(s[0]), asf(s[1])); }
__device__ __forceinline__ float x32_max(float x) { auto s = __builtin_amdgcn_permlane32_swap(asu(x), asu(x), false, false); return fmaxf(asf(s[0]), asf(s[1])); }
__device__ __forceinline__ float row_sum(float x) { x += dpp<XOR1>(x); x += dpp<XOR2>(x); x += dpp<ROR4>(x); x += dpp<ROR8>(x); return x; }
__device__ __forceinline__ float row_max(float x) { x = fmaxf(x, dpp<XOR1>(x)); x = fmaxf(x, dpp<XOR2>(x)); x = fmaxf(x, dpp<ROR4>(x)); x = fmaxf(x, dpp<ROR8>(x)); return x; }
__device__ __forceinline__ float wave_sum(float x) { return x32_sum(x16_sum(row_sum(x))); }
__device__ __forceinline__ float ex2(float x) { return __builtin_amdgcn_exp2f(x); }
__device__ __forceinline__ float rcp(float x) { return __builtin_amdgcn_rcpf(x); }

#define SB() __builtin_amdgcn_sched_barrier(0)
#define BAR() do { SB(); asm volatile("s_waitcnt lgkmcnt(0)" ::: "memory"); __builtin_amdgcn_s_barrier(); asm volatile("" ::: "memory"); SB(); } while (0)
#define PACE(n) do { SB(); asm volatile("s_waitcnt vmcnt(%0)" :: "i"(n) : "memory"); SB(); } while (0)
#define STAMP(i)
#define WIN 6

__device__ __forceinline__ void gemv16(const v4u (&wv)[4], v2u a, f4 (&d)[8]) {
#pragma unroll
  for (int dd = 0; dd < 8; ++dd) {
    v2u bfr;
    if (dd & 1) { bfr.x = phi(wv[0][dd >> 1], wv[1][dd >> 1]); bfr.y = phi(wv[2][dd >> 1], wv[3][dd >> 1]); }
    else { bfr.x = plo(wv[0][dd >> 1], wv[1][dd >> 1]); bfr.y = plo(wv[2][dd >> 1], wv[3][dd >> 1]); }
    d[dd] = mfma16(a, bfr, f4{0.f, 0.f, 0.f, 0.f});
  }
}

__global__ void __launch_bounds__(NT, 2)
fused_decode(const unsigned short* __restrict__ x, const void* __restrict__ kc, const void* __restrict__ vc, const void* __restrict__ wq,
             const void* __restrict__ wo, const void* __restrict__ w1, const void* __restrict__ b1, const void* __restrict__ w2,
             const void* __restrict__ b2, void* __restrict__ out, void* ws) {
  __shared__ __attribute__((aligned(16))) unsigned char smem[LDS_BYTES];
  LAS unsigned char* const L = (LAS unsigned char*)smem;
  const int tid = threadIdx.x, lane = tid & 63, w = __builtin_amdgcn_readfirstlane(tid >> 6), c = blockIdx.x;
  const int g = lane >> 4, n16 = lane & 15;
  STAMP(0);
  const int grp = c & 7, sp = c >> 3, ab = grp >> 2, ahkv = grp & 3;
  const int ksq = c >> 6, cgq = c & 63;
  const bool has1 = c < 224;
  const int ks1 = c / 56, cg1 = c % 56;
  const int idx = c >> 3, cgo = idx & 7;
  const int ho = (c & 7) * 4 + (idx >> 3);
  const int ks2 = (idx >> 3) * 8 + (c & 7);
  const bool has2 = ks2 < 28;

  const rsrc_t rws = mk(ws, WS_END), rx = mk(x, 4096);
  const rsrc_t rk = mk(kc, KVB), rv = mk(vc, KVB), rq = mk(wq, WQB), ro = mk(wo, WQB);
  const rsrc_t r1 = mk(w1, has1 ? W1B : 0u), r2 = mk(w2, has2 ? W1B : 0u);
  const rsrc_t rws1 = mk(ws, has1 ? WS_END : 0u), rws2 = mk(ws, has2 ? WS_END : 0u);

  const unsigned eraw = __builtin_amdgcn_raw_buffer_load_b32(rws, 0, WS_E, AUX_SC1);
  const v2u xr = __builtin_amdgcn_raw_buffer_load_b64(rx, tid * 8, 0, 0);
  const unsigned b1p = __builtin_amdgcn_raw_buffer_load_b32(mk(b1, has2 ? 7168u : 0u), (lane & 7) * 4, (ks2 * 128 + 16 * w) * 2, 0);
  const unsigned short xfin = __builtin_amdgcn_raw_buffer_load_b16(rx, ((lane >> 2) & 1) * 2048 + (lane & 3) * 2, c * 8, 0);
  const unsigned short b2fin = __builtin_amdgcn_raw_buffer_load_b16(mk(b2, 2048), (lane & 3) * 2, c * 8, 0);
  const float piv = blo(((const unsigned*)x)[(w >> 2) * 512]);
  SB();
  v4u Wq[4], W1[4], K[16], V[16], W2[4], Wo[4];
  {
    const int vo = (lane >> 3) * 4 * 8192 + (lane & 7) * 16, so = (ksq * 256 + 32 * w) * 8192 + cgq * 128;
#pragma unroll
    for (int j = 0; j < 4; ++j) { Wq[j] = ldnt(rq, vo, so + j * 8192); SB(); }
  }
  SB(); __builtin_amdgcn_s_barrier(); SB();
  {
    const int vo = (lane >> 3) * 4 * 7168 + (lane & 7) * 16, so = (ks1 * 256 + 32 * w) * 7168 + cg1 * 128;
#pragma unroll
    for (int j = 0; j < 4; ++j) { W1[j] = ldnt(r1, vo, so + j * 7168); SB(); }
  }
  const int kvb = (grp * 16384 + sp * 512) * 256 + w * 4096;
  const int kvo = (((n16 >> 1) & 1) * 4 + (n16 >> 2)) * 256 + g * 32 + (n16 & 1) * 16;
#define LOADK(i) do { K[i] = ldnt(rk, kvo + (((i) >> 1) & 1) * 2048 + ((i) & 1) * 128, kvb + ((i) >> 2) * 32768); SB(); } while (0)
#define LOADV(i) do { V[i] = ldnt(rv, lane * 16 + ((i) & 3) * 1024, kvb + ((i) >> 2) * 32768); SB(); } while (0)
#define PLOADK(i) do { PACE(WIN - 1); LOADK(i); } while (0)
#define PLOADV(i) do { PACE(WIN - 1); LOADV(i); } while (0)
  STAMP(1);

  {
    LAS f2* red = (LAS f2*)(L + L_RED);
    float xf[4] = {blo(xr.x), bhi(xr.x), blo(xr.y), bhi(xr.y)};
    float s1 = 0.f, s2 = 0.f;
#pragma unroll
    for (int i = 0; i < 4; ++i) { const float d = xf[i] - piv; s1 += d; s2 = fmaf(d, d, s2); }
    s1 = wave_sum(s1); s2 = wave_sum(s2);
    if (lane == 0) red[w] = f2{s1, s2};
    BAR();
    const f4 ra = *(LAS f4*)(red + (w & 4)), rbb = *(LAS f4*)(red + (w & 4) + 2);
    float a1 = ((ra.x + ra.z) + (rbb.x + rbb.z)) * (1.0f / 1024), a2 = ((ra.y + ra.w) + (rbb.y + rbb.w)) * (1.0f / 1024);
    float mean = rb(piv + a1), var = rb(fmaxf(a2 - a1 * a1, 0.f));
    float inv = 1.0f / rb(sqrtf(rb(var + 1e-5f)));
    float xn[4];
    s1 = 0.f; s2 = 0.f;
#pragma unroll
    for (int i = 0; i < 4; ++i) { xn[i] = rb(rb(xf[i] - mean) * inv); s1 += xn[i]; s2 = fmaf(xn[i], xn[i], s2); }
    *(LAS v2u*)(L + L_XS + tid * 8) = v2u{pk(xn[0], xn[1]), pk(xn[2], xn[3])};
    s1 = wave_sum(s1); s2 = wave_sum(s2);
    if (lane == 0) red[8 + w] = f2{s1, s2};
    BAR();
    const f4 rc = *(LAS f4*)(red + 8 + (w & 4)), rd = *(LAS f4*)(red + 8 + (w & 4) + 2);
    a1 = ((rc.x + rc.z) + (rd.x + rd.z)) * (1.0f / 1024); a2 = ((rc.y + rc.w) + (rd.y + rd.w)) * (1.0f / 1024);
    mean = rb(a1); var = rb(fmaxf(a2 - a1 * a1, 0.f));
    inv = 1.0f / rb(sqrtf(rb(var + 1e-5f)));
    *(LAS v2u*)(L + L_YS + tid * 8) = v2u{pk(rb(xn[0] - mean) * inv, rb(xn[1] - mean) * inv), pk(rb(xn[2] - mean) * inv, rb(xn[3] - mean) * inv)};
  }
  LOADK(0); LOADK(1);
  BAR();
  STAMP(2);
  const unsigned tag = __builtin_amdgcn_readfirstlane(eraw) * 0x9E3779B1u + 0x7F4A7C15u;
  LOADK(2); LOADK(3);

  {
    const int aoff = (n16 & 1) * 2048 + (32 * w + 4 * (2 * g + ((n16 >> 1) & 1))) * 2;
    const v2u aq = *(LAS v2u*)(L + L_XS + ksq * 512 + aoff), ah = *(LAS v2u*)(L + L_YS + (ks1 & 3) * 512 + aoff);
    const bool par = (n16 >> 3) != 0;
    f4 d[8];
    gemv16(Wq, aq, d);
    LAS f4* dst = (LAS f4*)(L + L_R1 + ((w * 2 + (n16 >> 3)) * 128 + 8 * (n16 & 7)) * 4);
    if (lane < 16) {
      dst[0] = f4{par ? d[0].z : d[0].x, par ? d[1].z : d[1].x, par ? d[2].z : d[2].x, par ? d[3].z : d[3].x};
      dst[1] = f4{par ? d[4].z : d[4].x, par ? d[5].z : d[5].x, par ? d[6].z : d[6].x, par ? d[7].z : d[7].x};
      dst[16] = f4{par ? d[0].w : d[0].y, par ? d[1].w : d[1].y, par ? d[2].w : d[2].y, par ? d[3].w : d[3].y};
      dst[17] = f4{par ? d[4].w : d[4].y, par ? d[5].w : d[5].y, par ? d[6].w : d[6].y, par ? d[7].w : d[7].y};
    }
    SB();
    PLOADK(4); PLOADK(5);
    gemv16(W1, ah, d);
    dst += 512;
    if (lane < 16) {
      dst[0] = f4{par ? d[0].z : d[0].x, par ? d[1].z : d[1].x, par ? d[2].z : d[2].x, par ? d[3].z : d[3].x};
      dst[1] = f4{par ? d[4].z : d[4].x, par ? d[5].z : d[5].x, par ? d[6].z : d[6].x, par ? d[7].z : d[7].x};
      dst[16] = f4{par ? d[0].w : d[0].y, par ? d[1].w : d[1].y, par ? d[2].w : d[2].y, par ? d[3].w : d[3].y};
      dst[17] = f4{par ? d[4].w : d[4].y, par ? d[5].w : d[5].y, par ? d[6].w : d[6].y, par ? d[7].w : d[7].y};
    }
    SB();
    PLOADK(6); PLOADK(7);
    BAR();
    if (w < 2) {
      const LAS f2* src = (const LAS f2*)(L + L_R1 + w * 8192) + lane;
      f2 s = src[0];
#pragma unroll
      for (int i = 1; i < 16; ++i) s += src[i * 64];
      const int b = lane >> 5, cp = lane & 31;
      const v4u gv = {asu(s.x), tag, asu(s.y), tag};
      if (w == 0) stc4(rws, b * 131072 + cp * 16, WS_QP + (((cgq >> 4) * 4 + ksq) * 1024 + (cgq & 15) * 64) * 8, gv);
      else stc4(rws1, b * 28672 + cp * 16, WS_HP + (ks1 * 2 * 3584 + cg1 * 64) * 8, gv);
    }
  }
  STAMP(3);
  PLOADK(8); PLOADK(9); PLOADK(10); PLOADK(11); PLOADK(12); PLOADK(13); PLOADK(14); PLOADK(15);
  STAMP(4);

  v4u QG[4], HG;
  const int qso = WS_QP + (grp * 4096 + w * 128) * 8;
  const int hvo = (g * 2 + ((lane >> 3) & 1)) * 28672 + (lane & 7) * 16, hso = WS_HP + (ks2 * 128 + 16 * w) * 8;
#pragma unroll
  for (int i = 0; i < 4; ++i) { QG[i] = ldc4(rws, lane * 16, qso + i * 8192); SB(); }
  HG = ldc4(rws2, hvo, hso); SB();
  LOADV(0); LOADV(1); LOADV(2); LOADV(3);
#define QBAD() (((HG.y ^ tag2) | (HG.w ^ tag2) | (QG[0].y ^ tag) | (QG[0].w ^ tag) | (QG[1].y ^ tag) | (QG[1].w ^ tag) | (QG[2].y ^ tag) | \
                 (QG[2].w ^ tag) | (QG[3].y ^ tag) | (QG[3].w ^ tag)) != 0u)
  const unsigned tag2 = has2 ? tag : 0u;
  if (__builtin_expect(__any(QBAD()), 0)) {
    for (unsigned spin = 0; spin < SPIN_CAP; ++spin) {
      __builtin_amdgcn_s_sleep(2);
#pragma unroll
      for (int i = 0; i < 4; ++i) QG[i] = ldc4(rws, lane * 16, qso + i * 8192);
      HG = ldc4(rws2, hvo, hso);
      if (!__any(QBAD())) break;
    }
  }
  SB();
  STAMP(5);
  *(LAS unsigned*)(L + L_QS + w * 272 + lane * 4) =
      pk((asf(QG[0].x) + asf(QG[1].x)) + (asf(QG[2].x) + asf(QG[3].x)), (asf(QG[0].z) + asf(QG[1].z)) + (asf(QG[2].z) + asf(QG[3].z)));
  PLOADV(4); PLOADV(5);
  v2u a2;
  {
    float h0 = rb(rb(x32_sum(x16_sum(asf(HG.x)))) + blo(b1p)), h1 = rb(rb(x32_sum(x16_sum(asf(HG.z)))) + bhi(b1p));
    constexpr float GC = -2.0f * 0.7978845608028654f * 1.4426950408889634f;
    h0 = h0 * rcp(1.0f + ex2(GC * (h0 + 0.044715f * h0 * h0 * h0)));
    h1 = h1 * rcp(1.0f + ex2(GC * (h1 + 0.044715f * h1 * h1 * h1)));
    const unsigned hp = pk(h0, h1);
    const int src = (18 * g + 8 * (lane & 1)) * 4;
    a2.x = __builtin_amdgcn_ds_bpermute(src, hp);
    a2.y = __builtin_amdgcn_ds_bpermute(src + 4, hp);
  }
  PLOADV(6); PLOADV(7);
  BAR();
  float p[16];
  float mw;
  {
    const LAS unsigned char* qb = L + L_QS + (n16 >> 1) * 272 + g * 32 + (n16 & 1) * 16;
    const v4u q0 = *(const LAS v4u*)qb, q1 = *(const LAS v4u*)(qb + 128);
    const bool par = (lane & 1) != 0;
#pragma unroll
    for (int t = 0; t < 8; ++t) {
      f4 a = mfma32(K[2 * t], q0, f4{0.f, 0.f, 0.f, 0.f});
      a = mfma32(K[2 * t + 1], q1, a);
      const float u0 = par ? a.y : a.x, u1 = par ? a.w : a.z;
      p[2 * t] = u0 + dpp<XOR1>(u0);
      p[2 * t + 1] = u1 + dpp<XOR1>(u1);
    }
    mw = p[0];
#pragma unroll
    for (int i = 1; i < 16; ++i) mw = fmaxf(mw, p[i]);
    mw = x32_max(x16_max(mw)) * SM_C;
    float lw = 0.f;
#pragma unroll
    for (int i = 0; i < 16; ++i) { p[i] = ex2(fmaf(p[i], SM_C, -mw)); lw += p[i]; }
    lw = x32_sum(x16_sum(lw));
    if (lane < 16 && !par) *(LAS f2*)(L + L_ST + (w * 8 + (lane >> 1)) * 8) = f2{mw, lw};
  }
  STAMP(6);
  PLOADV(8); PLOADV(9); PLOADV(10); PLOADV(11);
  BAR();
  v4u P0, P1;
  {
    const LAS f2* st = (const LAS f2*)(L + L_ST) + (n16 >> 1);
    f2 s[8];
#pragma unroll
    for (int i = 0; i < 8; ++i) s[i] = st[i * 8];
    float M = s[0].x;
#pragma unroll
    for (int i = 1; i < 8; ++i) M = fmaxf(M, s[i].x);
    if (w == 0) {
      float Ls = 0.f;
#pragma unroll
      for (int i = 0; i < 8; ++i) Ls = fmaf(s[i].y, ex2(s[i].x - M), Ls);
      if (lane < 16 && !(lane & 1)) stc4(rws, (lane >> 1) * 512, WS_ST + ((ab * 32 + ahkv * 8) * 32 + sp) * 16, v4u{asu(M), tag, asu(Ls), tag});
    }
    const float f = ex2(mw - M);
    const unsigned pm = (lane & 1) ? 0xffff0000u : 0u;
#pragma unroll
    for (int i = 0; i < 16; i += 2) {
      const float v0 = p[i] * f, v1 = p[i + 1] * f;
      const unsigned h = pk(v0, v1);
      p[i] = v0 - asf((h << 16) & pm); p[i + 1] = v1 - asf(h & pm);
    }
    P0 = v4u{pk(p[0], p[1]), pk(p[2], p[3]), pk(p[4], p[5]), pk(p[6], p[7])};
    P1 = v4u{pk(p[8], p[9]), pk(p[10], p[11]), pk(p[12], p[13]), pk(p[14], p[15])};
  }
  SB();
  PLOADV(12); PLOADV(13);
  f4 acc[8];
#pragma unroll
  for (int dd = 0; dd < 8; ++dd) {
    v4u bfr;
#pragma unroll
    for (int i = 0; i < 4; ++i) bfr[i] = (dd & 1) ? phi(V[2 * i][dd >> 1], V[2 * i + 1][dd >> 1]) : plo(V[2 * i][dd >> 1], V[2 * i + 1][dd >> 1]);
    acc[dd] = mfma32(P0, bfr, f4{0.f, 0.f, 0.f, 0.f});
  }
  SB();
  PLOADV(14); PLOADV(15);
  STAMP(7);
  {
    const int vo = g * 4 * 2048 + n16 * 16, so = (ks2 * 128 + 16 * w) * 2048 + cgo * 256;
#pragma unroll
    for (int j = 0; j < 4; ++j) { PACE(WIN - 1); W2[j] = ldnt(r2, vo, so + j * 2048); SB(); }
  }
  const int wovo = ((g & 1) * 8 + (g >> 1) * 4) * 2048 + n16 * 16, woso = (ho * 128 + 16 * w) * 2048 + cgo * 256;
  PACE(WIN - 1); Wo[0] = ldnt(ro, wovo, woso); SB();
  PACE(WIN - 1); Wo[1] = ldnt(ro, wovo, woso + 2048); SB();
#pragma unroll
  for (int dd = 0; dd < 8; ++dd) {
    v4u bfr;
#pragma unroll
    for (int i = 0; i < 4; ++i) bfr[i] = (dd & 1) ? phi(V[8 + 2 * i][dd >> 1], V[9 + 2 * i][dd >> 1]) : plo(V[8 + 2 * i][dd >> 1], V[9 + 2 * i][dd >> 1]);
    acc[dd] = mfma32(P1, bfr, acc[dd]);
  }
  SB();
  Wo[2] = ldnt(ro, wovo, woso + 2 * 2048); SB();
  Wo[3] = ldnt(ro, wovo, woso + 3 * 2048); SB();
  const int svo = (lane >> 5) * 16384 + (lane & 31) * 16, sso = WS_ST + ho * 512;
  v4u SG = ldc4(rws, svo, sso); SB();
  {
    LAS f4* dst = (LAS f4*)(L + L_RO + w * 4608 + lane * 16);
    dst[0] = f4{acc[0].x + acc[0].y, acc[1].x + acc[1].y, acc[2].x + acc[2].y, acc[3].x + acc[3].y};
    dst[72] = f4{acc[4].x + acc[4].y, acc[5].x + acc[5].y, acc[6].x + acc[6].y, acc[7].x + acc[7].y};
    dst[144] = f4{acc[0].z + acc[0].w, acc[1].z + acc[1].w, acc[2].z + acc[2].w, acc[3].z + acc[3].w};
    dst[216] = f4{acc[4].z + acc[4].w, acc[5].z + acc[5].w, acc[6].z + acc[6].w, acc[7].z + acc[7].w};
  }
  STAMP(8);
  BAR();
  if (w < 4) {
    const int hh = lane >> 5, cc = (lane >> 1) & 15, half = lane & 1;
    const LAS f4* src = (const LAS f4*)(L + L_RO + (2 * hh + half) * 1152 + (16 * w + cc) * 16);
    f4 s = src[0];
#pragma unroll
    for (int i = 1; i < 8; ++i) s += src[i * 288];
    stc4(rws, hh * 16384 + (cc >> 1) * 2048 + (cc & 1) * 32 + half * 16, WS_OP + ((ab * 32 + ahkv * 8 + 2 * w) * 256 + sp) * 64,
         v4u{pk(s.x, s.y), tag, pk(s.z, s.w), tag});
  }
  STAMP(9);
  {
    f4 d[8];
    gemv16(W2, a2, d);
    LAS f4* dst = (LAS f4*)(L + L_R2 + (w * 256 + 8 * n16) * 4);
    if (lane < 16) {
      dst[0] = f4{d[0].x, d[1].x, d[2].x, d[3].x}; dst[1] = f4{d[4].x, d[5].x, d[6].x, d[7].x};
      dst[32] = f4{d[0].y, d[1].y, d[2].y, d[3].y}; dst[33] = f4{d[4].y, d[5].y, d[6].y, d[7].y};
    }
    BAR();
    if (w >= 4 && w < 6) {
      const int nn = tid - 256;
      const LAS float* src = (const LAS float*)(L + L_R2) + nn;
      float s0 = src[0], s1 = src[128];
#pragma unroll
      for (int i = 1; i < 8; ++i) { s0 += src[i * 256]; s1 += src[i * 256 + 128]; }
      stc4(rws2, (nn >> 2) * 2048 + (nn & 3) * 16, WS_MP + (cgo * 32 * 32 + ks2) * 64, v4u{asu(s0), tag, asu(s1), tag});
    }
  }
  if (__builtin_expect(__any(((SG.y ^ tag) | (SG.w ^ tag)) != 0u), 0)) {
    for (unsigned spin = 0; spin < SPIN_CAP; ++spin) {
      __builtin_amdgcn_s_sleep(2);
      SG = ldc4(rws, svo, sso);
      if (!__any(((SG.y ^ tag) | (SG.w ^ tag)) != 0u)) break;
    }
  }
  v4u WF;
  {
    const float m = asf(SG.x), M = x16_max(row_max(m));
    const float e = ex2(m - M), Ls = x16_sum(row_sum(e * asf(SG.z)));
    const float wt = e * rcp(Ls), wh = rb(wt);
    LAS unsigned short* wl = (LAS unsigned short*)(L + L_WT + w * 256);
    wl[(lane >> 5) * 64 + (lane & 31)] = (unsigned short)(asu(wh) >> 16);
    wl[(lane >> 5) * 64 + 32 + (lane & 31)] = (unsigned short)pk(wt - wh, 0.f);
    asm volatile("" ::: "memory");
    WF = *(const LAS v4u*)(wl + (n16 & 3) * 32 + 8 * g);
  }
  v2u BF[8];
#pragma unroll
  for (int dd = 0; dd < 8; ++dd) {
    if (dd & 1) { BF[dd].x = phi(Wo[0][dd >> 1], Wo[1][dd >> 1]); BF[dd].y = phi(Wo[2][dd >> 1], Wo[3][dd >> 1]); }
    else { BF[dd].x = plo(Wo[0][dd >> 1], Wo[1][dd >> 1]); BF[dd].y = plo(Wo[2][dd >> 1], Wo[3][dd >> 1]); }
  }
  asm volatile("" : "+v"(BF[0]), "+v"(BF[1]), "+v"(BF[2]), "+v"(BF[3]), "+v"(BF[4]), "+v"(BF[5]), "+v"(BF[6]), "+v"(BF[7]), "+v"(WF));
  STAMP(10);
  {
    const int ovo = (n16 >> 3) * 524288 + g * 512 + (n16 & 7) * 8, oso = WS_OP + (ho * 8 + w) * 2048;
    v2u G[8];
    for (unsigned spin = 0;; ++spin) {
#pragma unroll
      for (int j = 0; j < 8; ++j) G[j] = ldc2(rws, ovo + j * 64, oso);
      unsigned bad = 0;
#pragma unroll
      for (int j = 0; j < 8; ++j) bad |= G[j].y ^ tag;
      if (!__any(bad != 0u) || spin > SPIN_CAP) break;
      __builtin_amdgcn_s_sleep(1);
    }
    STAMP(11);
    const f4 dl = mfma32(v4u{plo(G[0].x, G[1].x), plo(G[2].x, G[3].x), plo(G[4].x, G[5].x), plo(G[6].x, G[7].x)}, WF, f4{0.f, 0.f, 0.f, 0.f});
    const f4 dh = mfma32(v4u{phi(G[0].x, G[1].x), phi(G[2].x, G[3].x), phi(G[4].x, G[5].x), phi(G[6].x, G[7].x)}, WF, f4{0.f, 0.f, 0.f, 0.f});
    const auto s0 = __builtin_amdgcn_permlane32_swap(pk(dl.x, dh.x), pk(dl.z, dh.z), false, false);
    const auto s1 = __builtin_amdgcn_permlane32_swap(pk(dl.y, dh.y), pk(dl.w, dh.w), false, false);
    const bool b1s = (lane & 2) != 0;
    const v2u ao = {b1s ? s0[1] : s0[0], b1s ? s1[1] : s1[0]};
    f4 d[8];
#pragma unroll
    for (int dd = 0; dd < 8; ++dd) d[dd] = mfma16(ao, BF[dd], f4{0.f, 0.f, 0.f, 0.f});
    LAS f4* dst = (LAS f4*)(L + L_R2 + 8192 + (w * 256 + 8 * n16) * 4);
    if (lane < 16) {
      dst[0] = f4{d[0].x + d[0].y, d[1].x + d[1].y, d[2].x + d[2].y, d[3].x + d[3].y};
      dst[1] = f4{d[4].x + d[4].y, d[5].x + d[5].y, d[6].x + d[6].y, d[7].x + d[7].y};
      dst[32] = f4{d[0].z + d[0].w, d[1].z + d[1].w, d[2].z + d[2].w, d[3].z + d[3].w};
      dst[33] = f4{d[4].z + d[4].w, d[5].z + d[5].w, d[6].z + d[6].w, d[7].z + d[7].w};
    }
    BAR();
    if (w < 2) {
      const LAS float* src = (const LAS float*)(L + L_R2 + 8192) + tid;
      float t0 = src[0], t1 = src[128];
#pragma unroll
      for (int i = 1; i < 8; ++i) { t0 += src[i * 256]; t1 += src[i * 256 + 128]; }
      stc4(rws, (tid >> 2) * 2048 + (tid & 3) * 16, WS_FP + (cgo * 32 * 32 + ho) * 64, v4u{asu(t0), tag, asu(t1), tag});
    }
  }
  STAMP(12);
  if (w == 2) {
    v4u F0, F1, M0, M1;
    const unsigned mlive = lane < 48 ? 0xffffffffu : 0u;
    for (unsigned spin = 0;; ++spin) {
      M0 = ldc4(rws, lane * 16, WS_MP + c * 2048); M1 = ldc4(rws, lane * 16, WS_MP + c * 2048 + 1024);
      F0 = ldc4(rws, lane * 16, WS_FP + c * 2048); F1 = ldc4(rws, lane * 16, WS_FP + c * 2048 + 1024);
      const unsigned bad = (F0.y ^ tag) | (F0.w ^ tag) | (F1.y ^ tag) | (F1.w ^ tag) | (M0.y ^ tag) | (M0.w ^ tag) |
                           (((M1.y ^ tag) | (M1.w ^ tag)) & mlive);
      if (!__any(bad != 0u) || spin > SPIN_CAP) break;
      __builtin_amdgcn_s_sleep(1);
    }
    STAMP(13);
    float s0 = asf(F0.x) + asf(F1.x), s1 = asf(F0.z) + asf(F1.z);
    float m0 = asf(M0.x) + asf(M1.x & mlive), m1 = asf(M0.z) + asf(M1.z & mlive);
    s0 += dpp<ROR4>(s0); s1 += dpp<ROR4>(s1); m0 += dpp<ROR4>(m0); m1 += dpp<ROR4>(m1);
    s0 += dpp<ROR8>(s0); s1 += dpp<ROR8>(s1); m0 += dpp<ROR8>(m0); m1 += dpp<ROR8>(m1);
    const bool b = (lane & 4) != 0;
    const float s = rb(x32_sum(x16_sum(b ? s1 : s0)));
    const float m = rb(rb(x32_sum(x16_sum(b ? m1 : m0))) + blo(b2fin));
    const float gt = rb(m * rcp(1.0f + ex2(-1.4426950408889634f * m)));
    const float o = blo(xfin) + rb(s * gt);
    if (lane < 8) __builtin_amdgcn_raw_buffer_store_b16((unsigned short)pk(o, o), mk(out, 4096), (lane >> 2) * 2048 + (lane & 3) * 2, c * 8, AUX_SC1);
    if (c == 0 && lane == 0) __builtin_amdgcn_raw_buffer_store_b32(tag, rws, 0, WS_E, AUX_SC1);
    STAMP(14);
  }
}

}

extern "C" void kernel_launch(void* const* d_in, const int* in_sizes, int n_in, void* d_out, int out_size, void* d_ws, size_t ws_size,
                              hipStream_t stream) {
  (void)in_sizes; (void)n_in; (void)out_size; (void)ws_size;
  fused_decode<<<256, NT, 0, stream>>>((const unsigned short*)d_in[0], d_in[1], d_in[2], d_in[3], d_in[4], d_in[6], d_in[7], d_in[8], d_in[9],
                                       d_out, d_ws);
}
```

```cpp
#include <hip/hip_runtime.h>
#include <hip/hip_bf16.h>

namespace {

typedef unsigned v4u __attribute__((ext_vector_type(4)));
typedef unsigned v2u __attribute__((ext_vector_type(2)));
typedef float f4 __attribute__((ext_vector_type(4)));
typedef float f2 __attribute__((ext_vector_type(2)));
typedef __bf16 bf8 __attribute__((ext_vector_type(8)));
typedef __bf16 bf2 __attribute__((ext_vector_type(2)));
typedef short s4 __attribute__((ext_vector_type(4)));
typedef __amdgpu_buffer_rsrc_t rsrc_t;
#define LAS __attribute__((address_space(3)))

constexpr int NT = 576;
constexpr unsigned KVB = 2u * 4 * 16384 * 128 * 2, WQB = 1024u * 4096 * 2, W1B = 1024u * 3584 * 2;
constexpr unsigned WS_E = 0;
constexpr unsigned WS_QP = 4096;
constexpr unsigned WS_HP = WS_QP + 262144;
constexpr unsigned WS_QF = WS_HP + 229376;
constexpr unsigned WS_HF = WS_QF + 32768;
constexpr unsigned WS_ST = WS_HF + 28672;
constexpr unsigned WS_OP = WS_ST + 32768;
constexpr unsigned WS_FP = WS_OP + 1048576;
constexpr unsigned WS_MP = WS_FP + 524288;
constexpr unsigned WS_END = WS_MP + 524288;
constexpr int L_P = 131072, L_XS = L_P, L_YS = L_P + 4096, L_QS = L_P + 16384, L_HS = L_QS + 8 * 272, L_OS = L_HS + 2 * 272,
              L_ST = L_OS + 4 * 272, L_WT = L_ST + 512, L_FL = L_WT + 8 * 256, L_STAMP = L_FL + 256, L_END = L_STAMP + 9 * 256;
constexpr int LDS_BYTES = L_END;
static_assert(L_END <= 163840 && (L_HS % 16) == 0 && (L_OS % 16) == 0 && (L_ST % 16) == 0 && (L_WT % 16) == 0 && (L_FL % 16) == 0, "lds map");
enum { F_FULL = 0, F_LN = 1, F_Q = 2, F_P = 3, F_DQ = 4, F_DH = 5, F_TAG = 6, F_DK = 8, F_DV = 16, F_WORDS = 32 };
#define AUXL 16
#define AUXS 16
constexpr int AUX_SC1 = 16;
constexpr unsigned SPIN_CAP = 1u << 15, LSPIN_CAP = 1u << 20;
constexpr float SM_C = 0.088388347648318440f * 1.4426950408889634f;
#define LSLP 2
#define QGATE 10
#define WIN 24

__device__ __forceinline__ rsrc_t mk(const void* p, unsigned bytes) { return __builtin_amdgcn_make_buffer_rsrc((void*)p, 0, bytes, 0x00020000); }
__device__ __forceinline__ v4u ldc4(rsrc_t r, int vo, int so) { return __builtin_amdgcn_raw_buffer_load_b128(r, vo, so, AUXL); }
__device__ __forceinline__ v2u ldc2(rsrc_t r, int vo, int so) { return __builtin_amdgcn_raw_buffer_load_b64(r, vo, so, AUXL); }
__device__ __forceinline__ void stc4(rsrc_t r, int vo, int so, v4u v) { __builtin_amdgcn_raw_buffer_store_b128(v, r, vo, so, AUXS); }
__device__ __forceinline__ float asf(unsigned u) { return __builtin_bit_cast(float, u); }
__device__ __forceinline__ unsigned asu(float f) { return __builtin_bit_cast(unsigned, f); }
__device__ __forceinline__ unsigned pk(float a, float b) { f2 v = {a, b}; return __builtin_bit_cast(unsigned, __builtin_convertvector(v, bf2)); }
__device__ __forceinline__ float blo(unsigned u) { return asf(u << 16); }
__device__ __forceinline__ float bhi(unsigned u) { return asf(u & 0xffff0000u); }
__device__ __forceinline__ float rb(float a) { return blo(pk(a, a)); }
__device__ __forceinline__ unsigned plo(unsigned a, unsigned b) { return __builtin_amdgcn_perm(b, a, 0x05040100u); }
__device__ __forceinline__ unsigned phi(unsigned a, unsigned b) { return __builtin_amdgcn_perm(b, a, 0x07060302u); }
__device__ __forceinline__ f4 mfma32(v4u a, v4u b, f4 c) { return __builtin_amdgcn_mfma_f32_16x16x32_bf16(__builtin_bit_cast(bf8, a), __builtin_bit_cast(bf8, b), c, 0, 0, 0); }
template <int C> __device__ __forceinline__ float dpp(float x) { return asf(__builtin_amdgcn_mov_dpp(asu(x), C, 0xf, 0xf, true)); }
template <int C> __device__ __forceinline__ unsigned dppu(unsigned x) { return __builtin_amdgcn_mov_dpp(x, C, 0xf, 0xf, true); }
constexpr int XOR1 = 0xB1, XOR2 = 0x4E, ROR4 = 0x124, ROR8 = 0x128;
__device__ __forceinline__ float x16_sum(float x) { auto s = __builtin_amdgcn_permlane16_swap(asu(x), asu(x), false, false); return asf(s[0]) + asf(s[1]); }
__device__ __forceinline__ float x32_sum(float x) { auto s = __builtin_amdgcn_permlane32_swap(asu(x), asu(x), false, false); return asf(s[0]) + asf(s[1]); }
__device__ __forceinline__ float x16_max(float x) { auto s = __builtin_amdgcn_permlane16_swap(asu(x), asu(x), false, false); return fmaxf(asf(s[0]), asf(s[1])); }
__device__ __forceinline__ float x32_max(float x) { auto s = __builtin_amdgcn_permlane32_swap(asu(x), asu(x), false, false); return fmaxf(asf(s[0]), asf(s[1])); }
__device__ __forceinline__ float row_sum(float x) { x += dpp<XOR1>(x); x += dpp<XOR2>(x); x += dpp<ROR4>(x); x += dpp<ROR8>(x); return x; }
__device__ __forceinline__ float row_max(float x) { x = fmaxf(x, dpp<XOR1>(x)); x = fmaxf(x, dpp<XOR2>(x)); x = fmaxf(x, dpp<ROR4>(x)); x = fmaxf(x, dpp<ROR8>(x)); return x; }
__device__ __forceinline__ float wave_sum(float x) { return x32_sum(x16_sum(row_sum(x))); }
__device__ __forceinline__ float ex2(float x) { return __builtin_amdgcn_exp2f(x); }
__device__ __forceinline__ float rcp(float x) { return __builtin_amdgcn_rcpf(x); }
__device__ __forceinline__ v4u trfrag(const LAS unsigned char* a, int off0, int off1) {
  const v2u lo = __builtin_bit_cast(v2u, __builtin_amdgcn_ds_read_tr16_b64_v4i16((LAS s4*)(a + off0)));
  const v2u hi = __builtin_bit_cast(v2u, __builtin_amdgcn_ds_read_tr16_b64_v4i16((LAS s4*)(a + off1)));
  return v4u{lo.x, lo.y, hi.x, hi.y};
}

#define SB() __builtin_amdgcn_sched_barrier(0)
#define FENCE() do { SB(); asm volatile("" ::: "memory"); SB(); } while (0)
#define LGKM0() do { SB(); asm volatile("s_waitcnt lgkmcnt(0)" ::: "memory"); SB(); } while (0)
#define STAMP(i)

template <int SLEEP> __device__ __forceinline__ void lds_wait(LAS unsigned char* L, int word, unsigned want) {
  FENCE();
  const volatile LAS unsigned* p = (const volatile LAS unsigned*)(L + L_FL) + word;
  for (unsigned i = 0; i < LSPIN_CAP; ++i) {
    if ((unsigned)__builtin_amdgcn_readfirstlane(*p) >= want) break;
    if (SLEEP) __builtin_amdgcn_s_sleep(SLEEP);
  }
  FENCE();
}
__device__ __forceinline__ void lds_signal(LAS unsigned char* L, int word, int lane) {
  LGKM0();
  if (lane == 0) __hip_atomic_fetch_add((LAS unsigned*)(L + L_FL) + word, 1u, __ATOMIC_RELAXED, __HIP_MEMORY_SCOPE_WORKGROUP);
  FENCE();
}
template <int GS> __device__ __forceinline__ void dma4(rsrc_t r, int v0, int v1, int v2, int v3, int& so, unsigned& m0v) {
  asm volatile("s_mov_b32 m0, %1\n\ts_nop 0\n\t"
               "buffer_load_dwordx4 %2, %6, %0 offen nt lds\n\ts_add_u32 %0, %0, %7\n\t"
               "buffer_load_dwordx4 %3, %6, %0 offen offset:1024 nt lds\n\ts_add_u32 %0, %0, %7\n\t"
               "buffer_load_dwordx4 %4, %6, %0 offen offset:2048 nt lds\n\ts_add_u32 %0, %0, %7\n\t"
               "buffer_load_dwordx4 %5, %6, %0 offen offset:3072 nt lds\n\ts_add_u32 %0, %0, %8\n\ts_add_u32 %1, %1, 0x1000"
               : "+s"(so), "+s"(m0v) : "v"(v0), "v"(v1), "v"(v2), "v"(v3), "s"(r), "i"(GS - 1024), "i"(GS + 3072) : "memory", "scc");
}
template <int N> __device__ __forceinline__ void waitvm() { asm volatile("s_waitcnt vmcnt(%0)" :: "i"(N) : "memory"); }
template <int K> __device__ __forceinline__ void drain(LAS unsigned char* L, int wv) {
  if constexpr (K >= 0) {
    waitvm<K>();
    if constexpr (K % 16 == 0) { ((volatile LAS unsigned*)(L + L_FL))[F_FULL] = 24 - K / 16; STAMP(24 - K / 16); }
    drain<K - 4>(L, wv);
  }
}

__global__ void __launch_bounds__(NT)
fused_decode(const unsigned short* __restrict__ x, const void* __restrict__ kc, const void* __restrict__ vc, const void* __restrict__ wq,
             const void* __restrict__ wo, const void* __restrict__ w1, const void* __restrict__ b1, const void* __restrict__ w2,
             const void* __restrict__ b2, void* __restrict__ out, void* ws) {
  __shared__ __attribute__((aligned(16))) unsigned char smem[LDS_BYTES];
  LAS unsigned char* const L = (LAS unsigned char*)smem;
  const int tid = threadIdx.x, lane = tid & 63, wv = __builtin_amdgcn_readfirstlane(tid >> 6), c = blockIdx.x;
  const int g = lane >> 4, n16 = lane & 15;
  const int grp = c & 7, sp = c >> 3, ab = grp >> 2, ahkv = grp & 3;
  const int ksq = c >> 6, cgq = c & 63;
  const bool has1 = c < 224;
  const int ks1 = c / 56, cg1 = c % 56;
  const int cgo = sp & 7;
  const int ho = grp * 4 + (sp >> 3);
  const int ks2 = (sp >> 3) * 8 + grp;
  const bool has2 = ks2 < 28;

  if (wv == 0) {
    __builtin_amdgcn_s_setprio(3);
    STAMP(0);
    const rsrc_t rq = mk(wq, WQB);
    const int r8 = lane >> 3, c8 = lane & 7;
    const int f0 = r8 & 2;
    const int vq0 = r8 * 8192 + (c8 ^ f0) * 16, vq1 = r8 * 8192 + (c8 ^ f0 ^ 4) * 16;
    int so = ksq * 256 * 8192 + cgq * 128;
    unsigned m0v = (unsigned)(size_t)smem;
    int cnt = 0;
    const rsrc_t rk = mk(kc, KVB), rv = mk(vc, KVB), ro = mk(wo, WQB), r1 = mk(w1, has1 ? W1B : 0u), r2 = mk(w2, has2 ? W1B : 0u);
    const int v10 = r8 * 7168 + (c8 ^ f0) * 16, v11 = r8 * 7168 + (c8 ^ f0 ^ 4) * 16;
    const int vk0 = g * 256 + (n16 ^ g) * 16, vk1 = vk0 ^ 64, vk2 = vk0 ^ 128, vk3 = vk0 ^ 192;
    const int vv0 = g * 256 + (n16 ^ (2 * g)) * 16, vv1 = vv0 ^ 128;
    const int vw0 = g * 2048 + (n16 ^ (2 * g)) * 16, vw1 = vw0 ^ 128;
    const int kvb = (grp * 16384 + sp * 512) * 256;
    volatile LAS unsigned* fl = (volatile LAS unsigned*)(L + L_FL);
#pragma unroll
    for (int f = 0; f < 24; ++f) {
      if (f == 2) so = ks1 * 256 * 7168 + cg1 * 128;
      if (f >= 4 && f < 20) { int rr = sp; asm volatile("" : "+s"(rr)); so = kvb + ((f - 4 + rr) & 7) * 16384; }
      if (f == 20) so = ks2 * 128 * 2048 + cgo * 256;
      if (f == 22) so = ho * 128 * 2048 + cgo * 256;
      if ((f & 7) == 0) m0v = (unsigned)(size_t)smem;
      if (f == 8) lds_wait<LSLP>(L, F_DQ, 4);
      if (f == 10) lds_wait<LSLP>(L, F_DH, 4);
      if (f >= 12 && f < 20) lds_wait<LSLP>(L, F_DK + f - 12, 1);
      if (f >= 20) lds_wait<LSLP>(L, F_DV + f - 20, 8);
#pragma unroll
      for (int i = 0; i < 4; ++i) {
        if (f < 2) dma4<8 * 8192>(rq, vq0, vq1, vq0, vq1, so, m0v);
        else if (f < 4) dma4<8 * 7168>(r1, v10, v11, v10, v11, so, m0v);
        else if (f < 12) dma4<1024>(rk, vk0, vk1, vk2, vk3, so, m0v);
        else if (f < 20) dma4<1024>(rv, vv0, vv1, vv0, vv1, so, m0v);
        else if (f < 22) dma4<8192>(r2, vw0, vw0, vw1, vw1, so, m0v);
        else dma4<8192>(ro, vw0, vw0, vw1, vw1, so, m0v);
        cnt += 4;
        if (cnt == WIN) { SB(); __builtin_amdgcn_s_barrier(); SB(); }
        if (cnt >= WIN) {
          waitvm<WIN - 4>();
          const int landed = cnt - (WIN - 4);
          if (landed % 16 == 0) { fl[F_FULL] = landed / 16; STAMP(landed / 16); }
        }
      }
    }
    drain<WIN - 8>(L, wv);
    {
      const rsrc_t rws = mk(ws, WS_END);
      const unsigned short xfin = __builtin_amdgcn_raw_buffer_load_b16(mk(x, 4096), ((lane >> 2) & 1) * 2048 + (lane & 3) * 2, c * 8, 0);
      const unsigned short b2fin = __builtin_amdgcn_raw_buffer_load_b16(mk(b2, 2048), (lane & 3) * 2, c * 8, 0);
      SB();
      __builtin_amdgcn_s_barrier();
      FENCE();
      const unsigned tag = __builtin_amdgcn_readfirstlane(fl[F_TAG]);
      v4u F0, F1, M0, M1;
      const unsigned mlive = lane < 48 ? 0xffffffffu : 0u;
      for (unsigned spin = 0;; ++spin) {
        M0 = ldc4(rws, lane * 16, WS_MP + c * 2048); M1 = ldc4(rws, lane * 16, WS_MP + c * 2048 + 1024);
        F0 = ldc4(rws, lane * 16, WS_FP + c * 2048); F1 = ldc4(rws, lane * 16, WS_FP + c * 2048 + 1024);
        const unsigned bad = (F0.y ^ tag) | (F0.w ^ tag) | (F1.y ^ tag) | (F1.w ^ tag) | (M0.y ^ tag) | (M0.w ^ tag) |
                             (((M1.y ^ tag) | (M1.w ^ tag)) & mlive);
        if (!__any(bad != 0u) || spin > SPIN_CAP) break;
        __builtin_amdgcn_s_sleep(1);
      }
      STAMP(25);
      float s0 = asf(F0.x) + asf(F1.x), s1 = asf(F0.z) + asf(F1.z);
      float m0 = asf(M0.x) + asf(M1.x & mlive), m1 = asf(M0.z) + asf(M1.z & mlive);
      s0 += dpp<ROR4>(s0); s1 += dpp<ROR4>(s1); m0 += dpp<ROR4>(m0); m1 += dpp<ROR4>(m1);
      s0 += dpp<ROR8>(s0); s1 += dpp<ROR8>(s1); m0 += dpp<ROR8>(m0); m1 += dpp<ROR8>(m1);
      const bool b = (lane & 4) != 0;
      const float s = rb(x32_sum(x16_sum(b ? s1 : s0)));
      const float m = rb(rb(x32_sum(x16_sum(b ? m1 : m0))) + blo(b2fin));
      const float gt = rb(m * rcp(1.0f + ex2(-1.4426950408889634f * m)));
      const float o = blo(xfin) + rb(s * gt);
      if (lane < 8) __builtin_amdgcn_raw_buffer_store_b16((unsigned short)pk(o, o), mk(out, 4096), (lane >> 2) * 2048 + (lane & 3) * 2, c * 8, 0);
      if (c == 0 && lane == 0) __builtin_amdgcn_raw_buffer_store_b32(tag, rws, 0, WS_E, AUX_SC1);
      STAMP(26);
    }
  } else {
    const int w = wv - 1;
    STAMP(0);
    const rsrc_t rws = mk(ws, WS_END);
    const rsrc_t rws2 = mk(ws, has2 ? WS_END : 0u);
    const unsigned eraw = __builtin_amdgcn_raw_buffer_load_b32(rws, 0, WS_E, AUX_SC1);
    const rsrc_t rxl = mk(x, w < 2 ? 4096u : 0u);
    const v4u xa = __builtin_amdgcn_raw_buffer_load_b128(rxl, lane * 16, w * 2048, 0);
    const v4u xb = __builtin_amdgcn_raw_buffer_load_b128(rxl, lane * 16, w * 2048 + 1024, 0);
    const unsigned b1p = __builtin_amdgcn_raw_buffer_load_b32(mk(b1, (w == 6 && has1) ? 7168u : 0u), n16 * 4, (c % 112) * 64, 0);
    SB();
    if (w == 7 && lane < F_WORDS) ((volatile LAS unsigned*)(L + L_FL))[lane] = 0u;
    LGKM0();
    __builtin_amdgcn_s_barrier();
    FENCE();
    if (w < 2) {
      float xf[16];
#pragma unroll
      for (int i = 0; i < 4; ++i) { xf[2 * i] = blo(xa[i]); xf[2 * i + 1] = bhi(xa[i]); xf[8 + 2 * i] = blo(xb[i]); xf[9 + 2 * i] = bhi(xb[i]); }
      const float piv = asf(__builtin_amdgcn_readfirstlane(asu(xf[0])));
      float s1 = 0.f, s2 = 0.f;
#pragma unroll
      for (int i = 0; i < 16; ++i) { const float d = xf[i] - piv; s1 += d; s2 = fmaf(d, d, s2); }
      float a1 = wave_sum(s1) * (1.0f / 1024), a2 = wave_sum(s2) * (1.0f / 1024);
      float mean = rb(piv + a1), inv = rcp(rb(__builtin_amdgcn_sqrtf(rb(rb(fmaxf(a2 - a1 * a1, 0.f)) + 1e-5f))));
      v4u pa, pb;
      s1 = 0.f; s2 = 0.f;
#pragma unroll
      for (int i = 0; i < 8; ++i) {
        const unsigned t = pk(xf[2 * i] - mean, xf[2 * i + 1] - mean), u = pk(blo(t) * inv, bhi(t) * inv);
        if (i < 4) pa[i] = u; else pb[i - 4] = u;
        xf[2 * i] = blo(u); xf[2 * i + 1] = bhi(u);
        s1 += xf[2 * i] + xf[2 * i + 1]; s2 = fmaf(xf[2 * i], xf[2 * i], fmaf(xf[2 * i + 1], xf[2 * i + 1], s2));
      }
      *(LAS v4u*)(L + L_XS + w * 2048 + lane * 16) = pa;
      *(LAS v4u*)(L + L_XS + w * 2048 + 1024 + lane * 16) = pb;
      a1 = wave_sum(s1) * (1.0f / 1024); a2 = wave_sum(s2) * (1.0f / 1024);
      mean = rb(a1); inv = rcp(rb(__builtin_amdgcn_sqrtf(rb(rb(fmaxf(a2 - a1 * a1, 0.f)) + 1e-5f))));
#pragma unroll
      for (int i = 0; i < 8; ++i) {
        const unsigned t = pk(xf[2 * i] - mean, xf[2 * i + 1] - mean), u = pk(blo(t) * inv, bhi(t) * inv);
        if (i < 4) pa[i] = u; else pb[i - 4] = u;
      }
      *(LAS v4u*)(L + L_YS + w * 2048 + lane * 16) = pa;
      *(LAS v4u*)(L + L_YS + w * 2048 + 1024 + lane * 16) = pb;
      lds_signal(L, F_LN, lane);
    }
    const unsigned tag = __builtin_amdgcn_readfirstlane(eraw) * 0x9E3779B1u + 0x7F4A7C15u;
    if (w == 7) ((volatile LAS unsigned*)(L + L_FL))[F_TAG] = tag;
    const int q4 = n16 >> 2, p4 = lane & 3;
    STAMP(1);
    {
      const bool isq = w < 4;
      lds_wait<LSLP>(L, F_LN, 2);
      const LAS unsigned char* ap = L + (isq ? L_XS + ksq * 512 : L_YS + (ks1 & 3) * 512) + (lane & 1) * 2048 + g * 16;
      v4u A[8];
#pragma unroll
      for (int s = 0; s < 8; ++s) A[s] = *(const LAS v4u*)(ap + s * 64);
      lds_wait<LSLP>(L, F_FULL, isq ? 2 : 4);
      const LAS unsigned char* bp = L + (isq ? 0 : 32768) + (8 * g + q4) * 128 + (((w & 3) ^ ((q4 >> 1) | ((g & 1) << 1))) * 32) + p4 * 8;
      v4u Bf[8];
#pragma unroll
      for (int s = 0; s < 8; ++s) Bf[s] = trfrag(bp, s * 4096, s * 4096 + 512);
      lds_signal(L, isq ? F_DQ : F_DH, lane);
      f4 d0 = mfma32(A[0], Bf[0], f4{0.f, 0.f, 0.f, 0.f}), d1 = mfma32(A[1], Bf[1], f4{0.f, 0.f, 0.f, 0.f});
#pragma unroll
      for (int s = 2; s < 8; s += 2) { d0 = mfma32(A[s], Bf[s], d0); d1 = mfma32(A[s + 1], Bf[s + 1], d1); }
      const float e0 = d0.x + d1.x, e1 = d0.y + d1.y;
      const bool odd = (lane & 1) != 0;
      const float rcv = dpp<XOR1>(odd ? e0 : e1);
      const v4u gv = {asu(odd ? rcv : e0), tag, asu(odd ? e1 : rcv), tag};
      const rsrc_t rp = mk(ws, (isq || has1) ? WS_END : 0u);
      const int pso = (isq ? WS_QP + 2 * cgq * 1024 + ksq * 256 : WS_HP + 2 * cg1 * 1024 + ks1 * 256) + ((w >> 1) & 1) * 1024 + (w & 1) * 128;
      if (lane < 16) stc4(rp, (lane & 1) * (isq ? 131072 : 114688) + (lane >> 1) * 16, pso, gv);
    }
    STAMP(2);
    if (w >= 6 && (w == 7 || has1)) {
      const int rso = (w == 7 ? WS_QP : WS_HP) + c * 1024;
      v4u G;
      for (unsigned spin = 0;; ++spin) {
        G = ldc4(rws, lane * 16, rso);
        if (!__any(((G.y ^ tag) | (G.w ^ tag)) != 0u) || spin > SPIN_CAP) break;
        __builtin_amdgcn_s_sleep(8);
      }
      float h0 = x32_sum(x16_sum(asf(G.x))), h1 = x32_sum(x16_sum(asf(G.z)));
      if (w == 6) {
        h0 = rb(rb(h0) + blo(b1p)); h1 = rb(rb(h1) + bhi(b1p));
        constexpr float GC = -2.0f * 0.7978845608028654f * 1.4426950408889634f;
        h0 = h0 * rcp(1.0f + ex2(GC * (h0 + 0.044715f * h0 * h0 * h0)));
        h1 = h1 * rcp(1.0f + ex2(GC * (h1 + 0.044715f * h1 * h1 * h1)));
      }
      const unsigned gr = pk(h0, h1), gn = dppu<XOR1>(gr);
      if (lane < 16 && !(lane & 1)) stc4(rws, lane * 8, (w == 7 ? WS_QF : WS_HF) + c * 128, v4u{gr, tag, gn, tag});
    }
    STAMP(13);
    v4u K[16];
    const LAS unsigned char* kp = L + ((4 + w) & 7) * 16384 + n16 * 256;
#define READK() do { lds_wait<LSLP>(L, F_FULL, 5 + w); \
      _Pragma("unroll") for (int s = 0; s < 4; ++s) { const LAS unsigned char* kq = kp + (((4 * s + g) ^ n16) * 16); \
        _Pragma("unroll") for (int t = 0; t < 4; ++t) K[4 * t + s] = *(const LAS v4u*)(kq + t * 4096); } \
      lds_signal(L, F_DK + w, lane); } while (0)
    {
      if (w < 6) READK();
      lds_wait<LSLP>(L, F_FULL, QGATE);
      const rsrc_t rh = mk(ws, has2 ? WS_END : 0u);
      const int qso = WS_QF + grp * 4096 + w * 512;
      const int hvo = ((lane >> 3) & 1) * 14336 + (lane & 7) * 8, hso = WS_HF + ks2 * 512 + w * 64;
      const unsigned tag2 = has2 ? tag : 0u;
      v2u QG, HG;
      for (unsigned spin = 0;; ++spin) {
        QG = ldc2(rws, lane * 8, qso);
        HG = ldc2(rh, hvo, hso);
        if (!__any(((QG.y ^ tag) | (HG.y ^ tag2)) != 0u) || spin > SPIN_CAP) break;
        __builtin_amdgcn_s_sleep(8);
      }
      *(LAS unsigned*)(L + L_QS + w * 272 + lane * 4) = QG.x;
      if (lane < 16) *(LAS unsigned*)(L + L_HS + (lane >> 3) * 272 + w * 32 + (lane & 7) * 4) = HG.x;
      lds_signal(L, F_Q, lane);
      STAMP(3);
      if (w >= 6) READK();
    }
    lds_wait<LSLP>(L, F_Q, 8);
    {
      const LAS unsigned char* qb = L + L_QS + (n16 >> 1) * 272 + g * 16;
      v4u qf[4];
#pragma unroll
      for (int s = 0; s < 4; ++s) qf[s] = *(const LAS v4u*)(qb + s * 64);
      f4 sc[4];
#pragma unroll
      for (int t = 0; t < 4; ++t) sc[t] = mfma32(K[4 * t], qf[0], f4{0.f, 0.f, 0.f, 0.f});
#pragma unroll
      for (int s = 1; s < 4; ++s)
#pragma unroll
        for (int t = 0; t < 4; ++t) sc[t] = mfma32(K[4 * t + s], qf[s], sc[t]);
      float mw = fmaxf(fmaxf(sc[0].x, sc[0].y), fmaxf(sc[0].z, sc[0].w));
#pragma unroll
      for (int t = 1; t < 4; ++t) mw = fmaxf(mw, fmaxf(fmaxf(sc[t].x, sc[t].y), fmaxf(sc[t].z, sc[t].w)));
      mw = x32_max(x16_max(mw)) * SM_C;
      float lw = 0.f;
      const unsigned pm = (lane & 1) ? 0xffff0000u : 0u;
      unsigned P[8];
#pragma unroll
      for (int t = 0; t < 4; ++t)
#pragma unroll
        for (int r = 0; r < 4; r += 2) {
          const float v0 = ex2(fmaf(sc[t][r], SM_C, -mw)), v1 = ex2(fmaf(sc[t][r + 1], SM_C, -mw));
          lw += v0 + v1;
          const unsigned h = pk(v0, v1);
          P[2 * t + (r >> 1)] = pk(v0 - asf((h << 16) & pm), v1 - asf(h & pm));
        }
      *(LAS v4u*)(L + L_P + w * 2048 + lane * 16) = v4u{P[0], P[1], P[2], P[3]};
      *(LAS v4u*)(L + L_P + w * 2048 + 1024 + lane * 16) = v4u{P[4], P[5], P[6], P[7]};
      lw = x32_sum(x16_sum(lw));
      if (lane < 16 && !(lane & 1)) *(LAS f2*)(L + L_ST + (w * 8 + (lane >> 1)) * 8) = f2{mw, lw};
      lds_signal(L, F_P, lane);
    }
    STAMP(4);
    lds_wait<LSLP>(L, F_P, 8);
    float o0 = 0.f, o1 = 0.f;
    v4u SG;
    const int svo = (lane >> 5) * 16384 + (lane & 31) * 16, sso = WS_ST + ho * 512;
    {
      f4 st[8];
#pragma unroll
      for (int j = 0; j < 8; ++j) st[j] = *(const LAS f4*)(L + L_ST + (j * 8 + 2 * g) * 8);
      float M0 = st[0].x, M1 = st[0].z;
#pragma unroll
      for (int j = 1; j < 8; ++j) { M0 = fmaxf(M0, st[j].x); M1 = fmaxf(M1, st[j].z); }
      float l0 = 0.f, l1 = 0.f;
#pragma unroll
      for (int j = 0; j < 8; ++j) { st[j].x = ex2(st[j].x - M0); st[j].z = ex2(st[j].z - M1); l0 = fmaf(st[j].x, st[j].y, l0); l1 = fmaf(st[j].z, st[j].w, l1); }
      if (w == 0 && n16 < 2)
        stc4(rws, (2 * g + n16) * 512, WS_ST + ((ab * 32 + ahkv * 8) * 32 + sp) * 16, v4u{asu(n16 ? M1 : M0), tag, asu(n16 ? l1 : l0), tag});
      STAMP(5);
      const LAS unsigned char* vp = L + (4 * g + q4) * 256 + ((w ^ (4 * (g & 1) + q4)) * 32) + p4 * 8;
#pragma unroll
      for (int j = 0; j < 8; ++j) {
        const v4u pa = *(const LAS v4u*)(L + L_P + j * 2048 + lane * 16), pb = *(const LAS v4u*)(L + L_P + j * 2048 + 1024 + lane * 16);
        if (j == 6) { SG = ldc4(rws, svo, sso); SB(); }
        if (j < 7) lds_wait<LSLP>(L, F_FULL, 13 + j); else lds_wait<0>(L, F_FULL, 20);
        const LAS unsigned char* vq = vp + ((4 + j) & 7) * 16384;
        const v4u va = trfrag(vq, 0, 4096), vb = trfrag(vq, 8192, 12288);
        if (j < 4) lds_signal(L, F_DV + j, lane);
        f4 d = mfma32(pa, va, f4{0.f, 0.f, 0.f, 0.f});
        d = mfma32(pb, vb, d);
        o0 = fmaf(st[j].x, d.x + d.y, o0);
        o1 = fmaf(st[j].z, d.z + d.w, o1);
      }
      const bool odd = (lane & 1) != 0;
      const float rcv = dpp<XOR1>(odd ? o0 : o1);
      const unsigned gr = odd ? pk(rcv, o1) : pk(o0, rcv);
      const unsigned gn = dppu<XOR2>(gr);
      if (!(lane & 2))
        stc4(rws, (2 * g + (lane & 1)) * 16384 + (n16 >> 2) * 16, WS_OP + (((ab * 32 + ahkv * 8) * 8 + w) * 32 + sp) * 64, v4u{gr, tag, gn, tag});
    }
    STAMP(6);
    if (__builtin_expect(__any(((SG.y ^ tag) | (SG.w ^ tag)) != 0u), 0)) {
      for (unsigned spin = 0; spin < SPIN_CAP; ++spin) {
        __builtin_amdgcn_s_sleep(2);
        SG = ldc4(rws, svo, sso);
        if (!__any(((SG.y ^ tag) | (SG.w ^ tag)) != 0u)) break;
      }
    }
    v4u WF;
    {
      const float m = asf(SG.x), M = x16_max(row_max(m));
      const float e = ex2(m - M), Ls = x16_sum(row_sum(e * asf(SG.z)));
      const float wt = e * rcp(Ls), wh = rb(wt);
      LAS unsigned short* wl = (LAS unsigned short*)(L + L_WT + w * 256);
      wl[(lane >> 5) * 64 + (lane & 31)] = (unsigned short)(asu(wh) >> 16);
      wl[(lane >> 5) * 64 + 32 + (lane & 31)] = (unsigned short)pk(wt - wh, 0.f);
      FENCE();
      WF = *(const LAS v4u*)(wl + (n16 & 3) * 32 + 8 * g);
    }
    const LAS unsigned char* wp = L + (8 * g + q4) * 256 + ((w ^ (q4 | ((g & 1) << 2))) * 32) + p4 * 8;
    const int fvo = (n16 >> 2) * 2048 + (lane & 3) * 16;
    {
      v4u A[4];
#pragma unroll
      for (int s = 0; s < 4; ++s) A[s] = *(const LAS v4u*)(L + L_HS + (lane & 1) * 272 + s * 64 + g * 16);
      lds_wait<LSLP>(L, F_FULL, 22);
      f4 d = {0.f, 0.f, 0.f, 0.f};
#pragma unroll
      for (int s = 0; s < 4; ++s) d = mfma32(A[s], trfrag(wp + 4 * 16384, s * 8192, s * 8192 + 1024), d);
      if (lane < 16) stc4(rws2, fvo, WS_MP + ((cgo * 32 + 4 * w) * 32 + ks2) * 64, v4u{asu(d.x), tag, asu(d.y), tag});
    }
    STAMP(7);
    lds_wait<0>(L, F_FULL, 24);
    v4u Bo[4];
#pragma unroll
    for (int s = 0; s < 4; ++s) Bo[s] = trfrag(wp + 6 * 16384, s * 8192, s * 8192 + 1024);
    asm volatile("" : "+v"(Bo[0]), "+v"(Bo[1]), "+v"(Bo[2]), "+v"(Bo[3]), "+v"(WF));
    STAMP(8);
    {
      const int ovo = (n16 >> 3) * 524288 + g * 512 + (n16 & 7) * 8, oso = WS_OP + (ho * 8 + w) * 2048;
      v2u G[8];
#pragma unroll
      for (int j = 0; j < 8; ++j) G[j] = ldc2(rws, ovo + j * 64, oso);
      for (unsigned spin = 0; spin < SPIN_CAP; ++spin) {
        bool miss[8], any = false;
#pragma unroll
        for (int j = 0; j < 8; ++j) { miss[j] = __any(G[j].y != tag); any |= miss[j]; }
        if (!any) break;
        FENCE();
#pragma unroll
        for (int j = 0; j < 8; ++j)
          if (miss[j]) G[j] = ldc2(rws, ovo + j * 64, oso);
      }
      STAMP(9);
      const f4 dl = mfma32(v4u{plo(G[0].x, G[1].x), plo(G[2].x, G[3].x), plo(G[4].x, G[5].x), plo(G[6].x, G[7].x)}, WF, f4{0.f, 0.f, 0.f, 0.f});
      const f4 dh = mfma32(v4u{phi(G[0].x, G[1].x), phi(G[2].x, G[3].x), phi(G[4].x, G[5].x), phi(G[6].x, G[7].x)}, WF, f4{0.f, 0.f, 0.f, 0.f});
      if (n16 < 4 && (n16 >> 1) == (g >> 1))
        *(LAS v4u*)(L + L_OS + n16 * 272 + w * 32 + (g & 1) * 16) = v4u{pk(dl.x, dh.x), pk(dl.y, dh.y), pk(dl.z, dh.z), pk(dl.w, dh.w)};
      LGKM0();
      __builtin_amdgcn_s_barrier();
      FENCE();
      f4 d = {0.f, 0.f, 0.f, 0.f};
#pragma unroll
      for (int s = 0; s < 4; ++s) d = mfma32(*(const LAS v4u*)(L + L_OS + (lane & 3) * 272 + s * 64 + g * 16), Bo[s], d);
      if (lane < 16) stc4(rws, fvo, WS_FP + ((cgo * 32 + 4 * w) * 32 + ho) * 64, v4u{asu(d.x + d.y), tag, asu(d.z + d.w), tag});
    }
    STAMP(10);
  }
}

}

extern "C" void kernel_launch(void* const* d_in, const int* in_sizes, int n_in, void* d_out, int out_size, void* d_ws, size_t ws_size,
                              hipStream_t stream) {
  (void)in_sizes; (void)n_in; (void)out_size; (void)ws_size;
  fused_decode<<<256, NT, 0, stream>>>((const unsigned short*)d_in[0], d_in[1], d_in[2], d_in[3], d_in[4], d_in[6], d_in[7], d_in[8], d_in[9],
                                       d_out, (char*)d_ws + 0);
}
```

```cpp
#define PF 0
#define TOMID 330
#define TOFF 20
#define TAU 57
#define PA 8
#define PB 9
#include <hip/hip_runtime.h>
#include <hip/hip_bf16.h>

namespace {

typedef unsigned v4u __attribute__((ext_vector_type(4)));
typedef unsigned v2u __attribute__((ext_vector_type(2)));
typedef float f4 __attribute__((ext_vector_type(4)));
typedef float f2 __attribute__((ext_vector_type(2)));
typedef __bf16 bf8 __attribute__((ext_vector_type(8)));
typedef __bf16 bf2 __attribute__((ext_vector_type(2)));
typedef short s4 __attribute__((ext_vector_type(4)));
typedef __amdgpu_buffer_rsrc_t rsrc_t;
#define LAS __attribute__((address_space(3)))

constexpr int NT = 576;
constexpr unsigned KVB = 2u * 4 * 16384 * 128 * 2, WQB = 1024u * 4096 * 2, W1B = 1024u * 3584 * 2;
constexpr unsigned WS_E = 0;
constexpr unsigned WS_QP = 4096;
constexpr unsigned WS_HP = WS_QP + 131072;
constexpr unsigned WS_QF = WS_HP + 114688;
constexpr unsigned WS_HF = WS_QF + 32768;
constexpr unsigned WS_ST = WS_HF + 28672;
constexpr unsigned WS_OP = WS_ST + 32768;
constexpr unsigned WS_FP = WS_OP + 1048576;
constexpr unsigned WS_MP = WS_FP + 262144;
constexpr unsigned WS_END = WS_MP + 262144;
constexpr int L_P = 131072, L_XS = L_P, L_YS = L_P + 4096, L_QS = L_P + 16384, L_HS = L_QS + 8 * 272, L_OS = L_HS + 2 * 272,
              L_ST = L_OS + 8 * 272, L_WT = L_ST + 512, L_FL = L_WT + 256, L_STAMP = L_FL + 256, L_END = L_STAMP + 9 * 256;
constexpr int LDS_BYTES = L_END;
static_assert(L_END <= 163840 && (L_HS % 16) == 0 && (L_OS % 16) == 0 && (L_ST % 16) == 0 && (L_WT % 16) == 0 && (L_FL % 16) == 0, "lds map");
enum { F_FULL = 0, F_LN = 1, F_Q = 2, F_P = 3, F_H = 4, F_REF = 5, F_TAG = 6, F_W = 7, F_D = 8, F_DQ = F_D, F_DH = F_D + 2, F_DK = F_D + 4, F_DV = F_D + 12, F_T4 = 24, F_SUM = 25, F_WORDS = 32 };
#define AUXL 16
#define AUXS 16
constexpr int AUX_SC1 = 16;
constexpr unsigned SPIN_CAP = 1u << 15, LSPIN_CAP = 1u << 20;
constexpr float SM_C = 0.088388347648318440f * 1.4426950408889634f;
#define LSLP 2
#define QGATE 10
#define OGATE 23
#define PSLP 2
#define WLO 16
#define WHI 36
#define TTH 20
#define TOLO 700
#define CTL 1
#define WIN 24

__device__ __forceinline__ rsrc_t mk(const void* p, unsigned bytes) { return __builtin_amdgcn_make_buffer_rsrc((void*)p, 0, bytes, 0x00020000); }
__device__ __forceinline__ v4u ldc4(rsrc_t r, int vo, int so) { return __builtin_amdgcn_raw_buffer_load_b128(r, vo, so, AUXL); }
__device__ __forceinline__ v2u ldc2(rsrc_t r, int vo, int so) { return __builtin_amdgcn_raw_buffer_load_b64(r, vo, so, AUXL); }
__device__ __forceinline__ void stc4(rsrc_t r, int vo, int so, v4u v) { __builtin_amdgcn_raw_buffer_store_b128(v, r, vo, so, AUXS); }
__device__ __forceinline__ float asf(unsigned u) { return __builtin_bit_cast(float, u); }
__device__ __forceinline__ unsigned asu(float f) { return __builtin_bit_cast(unsigned, f); }
__device__ __forceinline__ unsigned pk(float a, float b) { f2 v = {a, b}; return __builtin_bit_cast(unsigned, __builtin_convertvector(v, bf2)); }
__device__ __forceinline__ float blo(unsigned u) { return asf(u << 16); }
__device__ __forceinline__ float bhi(unsigned u) { return asf(u & 0xffff0000u); }
__device__ __forceinline__ float rb(float a) { return blo(pk(a, a)); }
__device__ __forceinline__ unsigned plo(unsigned a, unsigned b) { return __builtin_amdgcn_perm(b, a, 0x05040100u); }
__device__ __forceinline__ unsigned phi(unsigned a, unsigned b) { return __builtin_amdgcn_perm(b, a, 0x07060302u); }
__device__ __forceinline__ f4 mfma32(v4u a, v4u b, f4 c) { return __builtin_amdgcn_mfma_f32_16x16x32_bf16(__builtin_bit_cast(bf8, a), __builtin_bit_cast(bf8, b), c, 0, 0, 0); }
__device__ __forceinline__ f4 mfma16(v2u a, v2u b, f4 c) { return __builtin_amdgcn_mfma_f32_16x16x16bf16_1k(__builtin_bit_cast(s4, a), __builtin_bit_cast(s4, b), c, 0, 0, 0); }
template <int C> __device__ __forceinline__ float dpp(float x) { return asf(__builtin_amdgcn_mov_dpp(asu(x), C, 0xf, 0xf, true)); }
template <int C> __device__ __forceinline__ unsigned dppu(unsigned x) { return __builtin_amdgcn_mov_dpp(x, C, 0xf, 0xf, true); }
constexpr int XOR1 = 0xB1, XOR2 = 0x4E, ROR4 = 0x124, ROR8 = 0x128;
__device__ __forceinline__ float x16_sum(float x) { auto s = __builtin_amdgcn_permlane16_swap(asu(x), asu(x), false, false); return asf(s[0]) + asf(s[1]); }
__device__ __forceinline__ float x32_sum(float x) { auto s = __builtin_amdgcn_permlane32_swap(asu(x), asu(x), false, false); return asf(s[0]) + asf(s[1]); }
__device__ __forceinline__ float x16_max(float x) { auto s = __builtin_amdgcn_permlane16_swap(asu(x), asu(x), false, false); return fmaxf(asf(s[0]), asf(s[1])); }
__device__ __forceinline__ float x32_max(float x) { auto s = __builtin_amdgcn_permlane32_swap(asu(x), asu(x), false, false); return fmaxf(asf(s[0]), asf(s[1])); }
__device__ __forceinline__ float row_sum(float x) { x += dpp<XOR1>(x); x += dpp<XOR2>(x); x += dpp<ROR4>(x); x += dpp<ROR8>(x); return x; }
__device__ __forceinline__ float row_max(float x) { x = fmaxf(x, dpp<XOR1>(x)); x = fmaxf(x, dpp<XOR2>(x)); x = fmaxf(x, dpp<ROR4>(x)); x = fmaxf(x, dpp<ROR8>(x)); return x; }
__device__ __forceinline__ float wave_sum(float x) { return x32_sum(x16_sum(row_sum(x))); }
__device__ __forceinline__ float ex2(float x) { return __builtin_amdgcn_exp2f(x); }
__device__ __forceinline__ float rcp(float x) { return __builtin_amdgcn_rcpf(x); }
__device__ __forceinline__ unsigned tg(float v, unsigned t12) { return ((asu(v) + 0x800u) & 0xfffff000u) | t12; }
__device__ __forceinline__ v4u tg4(f4 v, unsigned ta, unsigned tb) { return v4u{tg(v.x, ta), tg(v.y, tb), tg(v.z, ta), tg(v.w, tb)}; }
__device__ __forceinline__ float untg(unsigned u) { return asf(u & 0xfffff000u); }
__device__ __forceinline__ unsigned bad4(v4u u, unsigned ta, unsigned tb) { return ((u.x ^ ta) | (u.y ^ tb) | (u.z ^ ta) | (u.w ^ tb)) & 0xfffu; }
__device__ __forceinline__ float red4(v4u a, v4u b) {
  const auto s01 = __builtin_amdgcn_permlane32_swap(asu(untg(a.x) + untg(b.x)), asu(untg(a.y) + untg(b.y)), false, false);
  const auto s23 = __builtin_amdgcn_permlane32_swap(asu(untg(a.z) + untg(b.z)), asu(untg(a.w) + untg(b.w)), false, false);
  const auto t = __builtin_amdgcn_permlane16_swap(asu(asf(s01[0]) + asf(s01[1])), asu(asf(s23[0]) + asf(s23[1])), false, false);
  float v = asf(t[0]) + asf(t[1]);
  v += dpp<ROR4>(v); v += dpp<ROR8>(v);
  return v;
}
__device__ __forceinline__ v4u trfrag(const LAS unsigned char* a, int off0, int off1) {
  const v2u lo = __builtin_bit_cast(v2u, __builtin_amdgcn_ds_read_tr16_b64_v4i16((LAS s4*)(a + off0)));
  const v2u hi = __builtin_bit_cast(v2u, __builtin_amdgcn_ds_read_tr16_b64_v4i16((LAS s4*)(a + off1)));
  return v4u{lo.x, lo.y, hi.x, hi.y};
}

#define SB() __builtin_amdgcn_sched_barrier(0)
#define FENCE() do { SB(); asm volatile("" ::: "memory"); SB(); } while (0)
#define LGKM0() do { SB(); asm volatile("s_waitcnt lgkmcnt(0)" ::: "memory"); SB(); } while (0)
#define STAMP(i) do { if (((i) == PA || (i) == PB) && ((i) >= 25 || wv == 3)) { SB(); ((volatile LAS unsigned*)(L + L_FL))[(i) == PA ? 30 : 31] = (unsigned)__builtin_amdgcn_s_memrealtime(); SB(); } } while (0)

template <int SLEEP> __device__ __forceinline__ void lds_wait(LAS unsigned char* L, int word, unsigned want) {
  FENCE();
  const volatile LAS unsigned* p = (const volatile LAS unsigned*)(L + L_FL) + word;
#pragma nounroll
  for (unsigned i = 0; i < LSPIN_CAP; ++i) {
    if ((unsigned)__builtin_amdgcn_readfirstlane(*p) >= want) break;
    if (SLEEP) __builtin_amdgcn_s_sleep(SLEEP);
  }
  FENCE();
}
__device__ __forceinline__ void lds_signal(LAS unsigned char* L, int word, int lane) {
  LGKM0();
  if (lane == 0) __hip_atomic_fetch_add((LAS unsigned*)(L + L_FL) + word, 1u, __ATOMIC_RELAXED, __HIP_MEMORY_SCOPE_WORKGROUP);
  FENCE();
}
template <int GS> __device__ __forceinline__ void dma4(rsrc_t r, int v0, int v1, int v2, int v3, int& so, unsigned& m0v) {
  asm volatile("s_mov_b32 m0, %1\n\ts_nop 0\n\t"
               "buffer_load_dwordx4 %2, %6, %0 offen nt lds\n\ts_add_u32 %0, %0, %7\n\t"
               "buffer_load_dwordx4 %3, %6, %0 offen offset:1024 nt lds\n\ts_add_u32 %0, %0, %7\n\t"
               "buffer_load_dwordx4 %4, %6, %0 offen offset:2048 nt lds\n\ts_add_u32 %0, %0, %7\n\t"
               "buffer_load_dwordx4 %5, %6, %0 offen offset:3072 nt lds\n\ts_add_u32 %0, %0, %8\n\ts_add_u32 %1, %1, 0x1000"
               : "+s"(so), "+s"(m0v) : "v"(v0), "v"(v1), "v"(v2), "v"(v3), "s"(r), "i"(GS - 1024), "i"(GS + 3072) : "memory", "scc");
}
struct Ld { unsigned cnt, nxt, last, vfull; };
__device__ __forceinline__ void pace(Ld& s, unsigned w4, unsigned to) {
  unsigned a, b, v;
  asm volatile("0:\n\t"
               "s_memtime vcc\n\t"
               "s_getreg_b32 %0, hwreg(HW_REG_IB_STS)\n\t"
               "s_and_b32 %1, %0, 15\n\t"
               "s_bfe_u32 %0, %0, 0x20016\n\t"
               "s_lshl4_add_u32 %0, %0, %1\n\t"
               "s_sub_u32 %1, %5, %0\n\t"
               "s_cmp_lt_u32 %1, %3\n\t"
               "s_cbranch_scc1 1f\n\t"
               "s_lshr_b32 %1, %1, 4\n\t"
               "v_mov_b32 %2, %1\n\t"
               "ds_write_b32 %6, %2\n\t"
               "s_lshl_b32 %1, %1, 4\n\t"
               "s_add_u32 %3, %1, 16\n"
               "1:\n\t"
               "s_waitcnt lgkmcnt(0)\n\t"
               "s_cmp_le_u32 %0, %7\n\t"
               "s_cbranch_scc1 2f\n\t"
               "s_sub_u32 %1, vcc_lo, %4\n\t"
               "s_cmp_ge_u32 %1, %8\n\t"
               "s_cselect_b32 %1, %9, 0\n\t"
               "s_cmp_le_u32 %0, %1\n\t"
               "s_cbranch_scc0 0b\n"
               "2:\n\t"
               "s_mov_b32 %4, vcc_lo"
               : "=&s"(a), "=&s"(b), "=&v"(v), "+s"(s.nxt), "+s"(s.last)
               : "s"(s.cnt), "v"(s.vfull), "s"(w4), "s"(to), "i"(WHI - 4) : "memory", "scc", "vcc");
}
template <int GS, int F0, int NF>
__device__ __forceinline__ void stream(LAS unsigned char* L, Ld& s, rsrc_t r, int v0, int v1, int v2, int v3, int so, int rot, unsigned lds0, unsigned& w4, unsigned& to) {
  volatile LAS unsigned* fl = (volatile LAS unsigned*)(L + L_FL);
  const int base = so;
#pragma nounroll
  for (int f = F0; f < F0 + NF; ++f) {
    unsigned m0v = lds0 + (f & 7) * 16384;
    if (NF == 8) so = base + ((f - F0 + rot) & 7) * 16384;
    if (F0 + NF > 8 && f >= 8) {
      const unsigned now = (unsigned)__builtin_amdgcn_s_memrealtime(), ref = __builtin_amdgcn_readfirstlane(fl[F_REF]);
      const int lag = (CTL && ref) ? (int)(now - ref) - (TOFF + (f - 5) * TAU) : 0;
      w4 = lag > TTH ? WHI - 4 : lag < -TTH ? WLO - 4 : WIN - 4;
      to = lag < -TTH ? TOLO : TOMID;
      lds_wait<LSLP>(L, F_D + (f < 12 ? (f & ~1) : f) - 8, f < 12 ? 4u : f < 20 ? 1u : 8u);
    }
#pragma nounroll
    for (int i = 0; i < 4; ++i) {
      pace(s, w4, to);
      dma4<GS>(r, v0, v1, v2, v3, so, m0v);
      s.cnt += 4;
    }
  }
}

__global__ void __launch_bounds__(NT)
fused_decode(const unsigned short* __restrict__ x, const void* __restrict__ kc, const void* __restrict__ vc, const void* __restrict__ wq,
             const void* __restrict__ wo, const void* __restrict__ w1, const void* __restrict__ b1, const void* __restrict__ w2,
             const void* __restrict__ b2, void* __restrict__ out, void* ws) {
  __shared__ __attribute__((aligned(16))) unsigned char smem[LDS_BYTES];
  LAS unsigned char* const L = (LAS unsigned char*)smem;
  const int tid = threadIdx.x, lane = tid & 63, wv = __builtin_amdgcn_readfirstlane(tid >> 6), c = blockIdx.x;
  const int g = lane >> 4, n16 = lane & 15;
  const int grp = c & 7, sp = c >> 3, ab = grp >> 2, ahkv = grp & 3;
  const int ksq = c >> 6, cgq = c & 63;
  const bool has1 = grp != (sp & 7);
  const int c1 = sp * 7 + grp - (grp > (sp & 7) ? 1 : 0);
  const int ks1 = c1 / 56, cg1 = c1 % 56;
  const int cgo = sp & 7;
  const int ho = grp * 4 + (sp >> 3);
  const int ks2 = (sp >> 3) * 8 + grp;
  const bool has2 = ks2 < 28;

  if (wv == 0) {
    __builtin_amdgcn_s_setprio(3);
    STAMP(0);
    const rsrc_t rq = mk(wq, WQB);
    const int r8 = lane >> 3, c8 = lane & 7;
    const int f0 = r8 & 2;
    const int vq0 = r8 * 8192 + (c8 ^ f0) * 16, vq1 = vq0 ^ 64;
    int so = ksq * 256 * 8192 + cgq * 128;
    unsigned m0v = (unsigned)(size_t)smem;
#pragma unroll
    for (int i = 0; i < WIN / 4; ++i) dma4<8 * 8192>(rq, vq0, vq1, vq0, vq1, so, m0v);
    SB();
    volatile LAS unsigned* fl = (volatile LAS unsigned*)(L + L_FL);
    const unsigned lds0 = (unsigned)(size_t)smem;
    Ld st = {WIN, 16, 0, lds0 + L_FL + F_FULL * 4};
    unsigned w4 = WIN - 4, to = 0x7fffffffu;
    __builtin_amdgcn_s_barrier();
    FENCE();
#pragma nounroll
    for (int i = WIN / 4; i < 8; ++i) { pace(st, w4, to); dma4<8 * 8192>(rq, vq0, vq1, vq0, vq1, so, m0v); st.cnt += 4; }
    to = TOMID;
    {
      const int v0 = r8 * 7168 + (c8 ^ f0) * 16;
      stream<8 * 7168, 2, 2>(L, st, mk(w1, has1 ? W1B : 0u), v0, v0 ^ 64, v0, v0 ^ 64, ks1 * 256 * 7168 + cg1 * 128, 0, lds0, w4, to);
    }
    const int kvb = (grp * 16384 + sp * 512) * 256;
    {
      const int v0 = g * 256 + (n16 ^ g) * 16;
      stream<1024, 4, 8>(L, st, mk(kc, KVB), v0, v0 ^ 64, v0 ^ 128, v0 ^ 192, kvb, sp, lds0, w4, to);
    }
    {
      const int v0 = g * 256 + (n16 ^ (2 * g)) * 16;
      stream<1024, 12, 8>(L, st, mk(vc, KVB), v0, v0 ^ 128, v0, v0 ^ 128, kvb, sp, lds0, w4, to);
    }
    {
      const int v0 = g * 2048 + (n16 ^ (2 * g)) * 16;
      stream<8192, 20, 2>(L, st, mk(w2, has2 ? W1B : 0u), v0, v0, v0 ^ 128, v0 ^ 128, ks2 * 128 * 2048 + cgo * 256, 0, lds0, w4, to);
      stream<8192, 22, 2>(L, st, mk(wo, WQB), v0, v0, v0 ^ 128, v0 ^ 128, ho * 128 * 2048 + cgo * 256, 0, lds0, w4, to);
    }
    pace(st, 0u, 0x7fffffffu);
    asm volatile("s_waitcnt vmcnt(0)" ::: "memory");
    fl[F_FULL] = 24;
    {
      const rsrc_t rws = mk(ws, c < 128 ? WS_END : 0u);
      const int col2 = ((lane >> 1) & 1) * 8 + ((g >> 1) | ((g & 1) << 1)) * 2;
      const int fo = (lane & 1) * 2048 + col2;
      const unsigned short xfin = __builtin_amdgcn_raw_buffer_load_b16(mk(x, c < 128 ? 4096u : 0u), fo, c * 16, 0);
      const unsigned short b2fin = __builtin_amdgcn_raw_buffer_load_b16(mk(b2, c < 128 ? 2048u : 0u), col2, c * 16, 0);
      const unsigned tag = __builtin_amdgcn_readfirstlane(fl[F_TAG]), ta = tag & 0xfffu, tb = (tag >> 12) & 0xfffu;
      const unsigned mlive = lane < 48 ? 0xffffffffu : 0u;
      v4u M0 = ldc4(rws, lane * 16, WS_MP + c * 2048), M1 = ldc4(rws, lane * 16, WS_MP + c * 2048 + 1024);
      SB();
      __builtin_amdgcn_s_barrier();
      FENCE();
      if (c < 128) {
        v4u F0 = ldc4(rws, lane * 16, WS_FP + c * 2048), F1 = ldc4(rws, lane * 16, WS_FP + c * 2048 + 1024);
        SB();
        if (__any((bad4(M0, ta, tb) | (bad4(M1, ta, tb) & mlive)) != 0u)) {
#pragma nounroll
          for (unsigned spin = 0; spin < SPIN_CAP; ++spin) {
            FENCE();
            M0 = ldc4(rws, lane * 16, WS_MP + c * 2048); M1 = ldc4(rws, lane * 16, WS_MP + c * 2048 + 1024);
            if (!__any((bad4(M0, ta, tb) | (bad4(M1, ta, tb) & mlive)) != 0u)) break;
          }
        }
        const float m = rb(rb(red4(M0, M1 & mlive)) + blo(b2fin));
        float gt = rb(m * rcp(1.0f + ex2(-1.4426950408889634f * m))), xv = blo(xfin);
        asm volatile("" : "+v"(gt), "+v"(xv));
#pragma nounroll
        for (unsigned spin = 0; spin < SPIN_CAP; ++spin) {
          const bool m0 = __any(bad4(F0, ta, tb) != 0u), m1 = __any(bad4(F1, ta, tb) != 0u);
          if (!(m0 || m1)) break;
          FENCE();
          if (m0) F0 = ldc4(rws, lane * 16, WS_FP + c * 2048);
          if (m1) F1 = ldc4(rws, lane * 16, WS_FP + c * 2048 + 1024);
        }
        STAMP(25);
        const float o = xv + rb(rb(red4(F0, F1)) * gt);
        if ((lane & 12) == 0) __builtin_amdgcn_raw_buffer_store_b16((unsigned short)pk(o, o), mk(out, 4096), fo, c * 16, 0);
        if (c == 0 && lane == 0) __builtin_amdgcn_raw_buffer_store_b32(tag, rws, 0, WS_E, AUX_SC1);
        STAMP(26);
        { LGKM0(); unsigned d = 4 * (__builtin_amdgcn_readfirstlane(fl[31]) - __builtin_amdgcn_readfirstlane(fl[30])); if (d > 4000) d = 4000; const unsigned t1 = (unsigned)__builtin_amdgcn_s_memrealtime(); for (int it = 0; it < 100000; ++it) if ((unsigned)__builtin_amdgcn_s_memrealtime() - t1 >= d) break; }
      }
    }
  } else {
    const int w = wv - 1;
    STAMP(0);
    const rsrc_t rws = mk(ws, WS_END);
    const rsrc_t rws2 = mk(ws, has2 ? WS_END : 0u);
    const unsigned eraw = __builtin_amdgcn_raw_buffer_load_b32(rws, 0, WS_E, AUX_SC1);
    const rsrc_t rxl = mk(x, w < 2 ? 4096u : 0u);
    const v4u xa = __builtin_amdgcn_raw_buffer_load_b128(rxl, lane * 16, w * 2048, 0);
    const v4u xb = __builtin_amdgcn_raw_buffer_load_b128(rxl, lane * 16, w * 2048 + 1024, 0);
    const unsigned b1p = __builtin_amdgcn_raw_buffer_load_b32(mk(b1, (w == 6 && has1) ? 7168u : 0u), (n16 >> 2) * 8 + (lane & 1) * 4, c1 * 32, 0);
    const v4u codepf = __builtin_amdgcn_raw_buffer_load_b128(mk((const void*)(__builtin_amdgcn_s_getpc() & ~255ull), w == 3 ? PF * 1024u : 0u), lane * 16, sp * 1024, 0);
    SB();
    if (w == 7 && lane < F_WORDS) ((volatile LAS unsigned*)(L + L_FL))[lane] = 0u;
    LGKM0();
    __builtin_amdgcn_s_barrier();
    FENCE();
    if (w < 2) {
      float xf[16];
#pragma unroll
      for (int i = 0; i < 4; ++i) { xf[2 * i] = blo(xa[i]); xf[2 * i + 1] = bhi(xa[i]); xf[8 + 2 * i] = blo(xb[i]); xf[9 + 2 * i] = bhi(xb[i]); }
      const float piv = asf(__builtin_amdgcn_readfirstlane(asu(xf[0])));
      float s1 = 0.f, s2 = 0.f;
#pragma unroll
      for (int i = 0; i < 16; ++i) { const float d = xf[i] - piv; s1 += d; s2 = fmaf(d, d, s2); }
      float a1 = wave_sum(s1) * (1.0f / 1024), a2 = wave_sum(s2) * (1.0f / 1024);
      float mean = rb(piv + a1), inv = rcp(rb(__builtin_amdgcn_sqrtf(rb(rb(fmaxf(a2 - a1 * a1, 0.f)) + 1e-5f))));
      v4u pa, pb;
      s1 = 0.f; s2 = 0.f;
#pragma unroll
      for (int i = 0; i < 8; ++i) {
        const unsigned t = pk(xf[2 * i] - mean, xf[2 * i + 1] - mean), u = pk(blo(t) * inv, bhi(t) * inv);
        if (i < 4) pa[i] = u; else pb[i - 4] = u;
        xf[2 * i] = blo(u); xf[2 * i + 1] = bhi(u);
        s1 += xf[2 * i] + xf[2 * i + 1]; s2 = fmaf(xf[2 * i], xf[2 * i], fmaf(xf[2 * i + 1], xf[2 * i + 1], s2));
      }
      *(LAS v4u*)(L + L_XS + w * 2048 + lane * 16) = pa;
      *(LAS v4u*)(L + L_XS + w * 2048 + 1024 + lane * 16) = pb;
      a1 = wave_sum(s1) * (1.0f / 1024); a2 = wave_sum(s2) * (1.0f / 1024);
      mean = rb(a1); inv = rcp(rb(__builtin_amdgcn_sqrtf(rb(rb(fmaxf(a2 - a1 * a1, 0.f)) + 1e-5f))));
#pragma unroll
      for (int i = 0; i < 8; ++i) {
        const unsigned t = pk(xf[2 * i] - mean, xf[2 * i + 1] - mean), u = pk(blo(t) * inv, bhi(t) * inv);
        if (i < 4) pa[i] = u; else pb[i - 4] = u;
      }
      *(LAS v4u*)(L + L_YS + w * 2048 + lane * 16) = pa;
      *(LAS v4u*)(L + L_YS + w * 2048 + 1024 + lane * 16) = pb;
      lds_signal(L, F_LN, lane);
    }
    const unsigned tag = __builtin_amdgcn_readfirstlane(eraw) * 0x9E3779B1u + 0x7F4A7C15u;
    const unsigned ta = tag & 0xfffu, tb = (tag >> 12) & 0xfffu;
    if (w == 7) ((volatile LAS unsigned*)(L + L_FL))[F_TAG] = tag;
    const int q4 = n16 >> 2, p4 = lane & 3;
    unsigned t4 = 0;
    STAMP(1);
    {
      const bool isq = w < 4;
      lds_wait<LSLP>(L, F_LN, 2);
      const LAS unsigned char* ap = L + (isq ? L_XS + ksq * 512 : L_YS + (ks1 & 3) * 512) + (lane & 1) * 2048 + g * 16;
      v4u A[8];
#pragma unroll
      for (int s = 0; s < 8; ++s) A[s] = *(const LAS v4u*)(ap + s * 64);
      lds_wait<LSLP>(L, F_FULL, isq ? 2 : 4);
      if (CTL && w == 7) { t4 = (unsigned)__builtin_amdgcn_s_memrealtime(); ((volatile LAS unsigned*)(L + L_FL))[F_T4] = t4; }
      const LAS unsigned char* bp = L + (isq ? 0 : 32768) + (8 * g + q4) * 128 + (((w & 3) ^ ((q4 >> 1) | ((g & 1) << 1))) * 32) + p4 * 8;
      v4u Bf[8];
#pragma unroll
      for (int s = 0; s < 8; ++s) Bf[s] = trfrag(bp, s * 4096, s * 4096 + 512);
      lds_signal(L, isq ? F_DQ : F_DH, lane);
      f4 d0 = mfma32(Bf[0], A[0], f4{0.f, 0.f, 0.f, 0.f}), d1 = mfma32(Bf[1], A[1], f4{0.f, 0.f, 0.f, 0.f});
#pragma unroll
      for (int s = 2; s < 8; s += 2) { d0 = mfma32(Bf[s], A[s], d0); d1 = mfma32(Bf[s + 1], A[s + 1], d1); }
      const rsrc_t rp = mk(ws, (isq || has1) ? WS_END : 0u);
      const int pso = (isq ? WS_QP + cgq * 2048 + ksq * 128 : WS_HP + cg1 * 2048 + ks1 * 128) + (w & 3) * 512;
      if (n16 < 2) stc4(rp, g * 32 + n16 * 16, pso, tg4(d0 + d1, ta, tb));
    }
    STAMP(2);
    if (w >= 6 && (w == 7 || has1)) {
      const int rso = w == 7 ? WS_QP + c * 512 : WS_HP + c1 * 512;
      v2u G;
#pragma nounroll
      for (unsigned spin = 0;; ++spin) {
        G = ldc2(rws, lane * 8, rso);
        if (!__any((((G.x ^ ta) | (G.y ^ tb)) & 0xfffu) != 0u) || spin > SPIN_CAP) break;
        __builtin_amdgcn_s_sleep(PSLP);
      }
      float h0 = x32_sum(x16_sum(untg(G.x))), h1 = x32_sum(x16_sum(untg(G.y)));
      if (w == 6) {
        h0 = rb(rb(h0) + blo(b1p)); h1 = rb(rb(h1) + bhi(b1p));
        constexpr float GC = -2.0f * 0.7978845608028654f * 1.4426950408889634f;
        h0 = h0 * rcp(1.0f + ex2(GC * (h0 + 0.044715f * h0 * h0 * h0)));
        h1 = h1 * rcp(1.0f + ex2(GC * (h1 + 0.044715f * h1 * h1 * h1)));
      }
      const unsigned gr = pk(h0, h1), gn = dppu<XOR1>(gr);
      const unsigned ty = w == 7 ? (tag << 12) | (t4 & 0xfffu) : tag;
      if (lane < 16 && !(lane & 1))
        stc4(rws, ((lane >> 1) & 1) * (w == 7 ? 16384 : 14336) + (lane >> 2) * 16, w == 7 ? WS_QF + c * 64 : WS_HF + c1 * 64, v4u{gr, ty, gn, ty});
    }
    STAMP(13);
    asm volatile("" :: "v"(codepf));
    v4u K[16];
    const LAS unsigned char* kp = L + ((4 + w) & 7) * 16384 + n16 * 256;
#define READK() do { lds_wait<LSLP>(L, F_FULL, 5 + w); \
      _Pragma("unroll") for (int s = 0; s < 4; ++s) { const LAS unsigned char* kq = kp + (((4 * s + g) ^ n16) * 16); \
        _Pragma("unroll") for (int t = 0; t < 4; ++t) K[4 * t + s] = *(const LAS v4u*)(kq + t * 4096); } \
      lds_signal(L, F_DK + w, lane); } while (0)
    {
      if (w < 6) READK();
      lds_wait<LSLP>(L, F_FULL, QGATE);
      const int qso = WS_QF + grp * 4096 + w * 512;
      v2u QG = ldc2(rws, lane * 8, qso);
      SB();
      if (w >= 6) READK();
#pragma nounroll
      for (unsigned spin = 0; __any((QG.y >> 12) != (tag & 0xfffffu)) && spin < SPIN_CAP; ++spin) {
        __builtin_amdgcn_s_sleep(PSLP);
        QG = ldc2(rws, lane * 8, qso);
      }
      *(LAS unsigned*)(L + L_QS + w * 272 + lane * 4) = QG.x;
      if (CTL) {
        const int dsum = (int)wave_sum((float)((int)((QG.y - ((volatile LAS unsigned*)(L + L_FL))[F_T4]) << 20) >> 20));
        if (lane == 0) __hip_atomic_fetch_add((LAS int*)(L + L_FL) + F_SUM, dsum, __ATOMIC_RELAXED, __HIP_MEMORY_SCOPE_WORKGROUP);
      }
      lds_signal(L, F_Q, lane);
      STAMP(3);
    }
    lds_wait<LSLP>(L, F_Q, 8);
    if (CTL && w == 0) { volatile LAS unsigned* fl = (volatile LAS unsigned*)(L + L_FL); fl[F_REF] = (fl[F_T4] + ((int)fl[F_SUM] >> 9)) | 1u; }
    {
      const LAS unsigned char* qb = L + L_QS + (n16 >> 1) * 272 + g * 16;
      v4u qf[4];
#pragma unroll
      for (int s = 0; s < 4; ++s) qf[s] = *(const LAS v4u*)(qb + s * 64);
      f4 sc[4];
#pragma unroll
      for (int t = 0; t < 4; ++t) sc[t] = mfma32(K[4 * t], qf[0], f4{0.f, 0.f, 0.f, 0.f});
#pragma unroll
      for (int s = 1; s < 4; ++s)
#pragma unroll
        for (int t = 0; t < 4; ++t) sc[t] = mfma32(K[4 * t + s], qf[s], sc[t]);
      float mw = fmaxf(fmaxf(sc[0].x, sc[0].y), fmaxf(sc[0].z, sc[0].w));
#pragma unroll
      for (int t = 1; t < 4; ++t) mw = fmaxf(mw, fmaxf(fmaxf(sc[t].x, sc[t].y), fmaxf(sc[t].z, sc[t].w)));
      mw = x32_max(x16_max(mw)) * SM_C;
      float lw = 0.f;
      const unsigned pm = (lane & 1) ? 0xffff0000u : 0u;
      unsigned P[8];
#pragma unroll
      for (int t = 0; t < 4; ++t)
#pragma unroll
        for (int r = 0; r < 4; r += 2) {
          const float v0 = ex2(fmaf(sc[t][r], SM_C, -mw)), v1 = ex2(fmaf(sc[t][r + 1], SM_C, -mw));
          lw += v0 + v1;
          const unsigned h = pk(v0, v1);
          P[2 * t + (r >> 1)] = pk(v0 - asf((h << 16) & pm), v1 - asf(h & pm));
        }
      *(LAS v4u*)(L + L_P + w * 2048 + lane * 16) = v4u{P[0], P[1], P[2], P[3]};
      *(LAS v4u*)(L + L_P + w * 2048 + 1024 + lane * 16) = v4u{P[4], P[5], P[6], P[7]};
      lw = x32_sum(x16_sum(lw));
      if (lane < 16 && !(lane & 1)) *(LAS f2*)(L + L_ST + (w * 8 + (lane >> 1)) * 8) = f2{mw, lw};
      lds_signal(L, F_P, lane);
    }
    STAMP(4);
    lds_wait<LSLP>(L, F_P, 8);
    float o0 = 0.f, o1 = 0.f;
    v4u SG;
    v2u HG;
    const int hvo = ((lane >> 3) & 1) * 14336 + (lane & 7) * 8, hso = WS_HF + ks2 * 512 + w * 64;
    const unsigned tag2 = has2 ? tag : 0u;
    const int svo = (lane >> 5) * 16384 + (lane & 31) * 128, sso = WS_ST + (ho >> 3) * 4096 + (ho & 7) * 16;
    const rsrc_t rst = mk(ws, w == 1 ? WS_END : 0u);
    {
      f4 st[8];
#pragma unroll
      for (int j = 0; j < 8; ++j) st[j] = *(const LAS f4*)(L + L_ST + (j * 8 + 2 * g) * 8);
      float M0 = st[0].x, M1 = st[0].z;
#pragma unroll
      for (int j = 1; j < 8; ++j) { M0 = fmaxf(M0, st[j].x); M1 = fmaxf(M1, st[j].z); }
      float l0 = 0.f, l1 = 0.f;
#pragma unroll
      for (int j = 0; j < 8; ++j) { st[j].x = ex2(st[j].x - M0); st[j].z = ex2(st[j].z - M1); l0 = fmaf(st[j].x, st[j].y, l0); l1 = fmaf(st[j].z, st[j].w, l1); }
      if (w == 0 && n16 < 2)
        stc4(rws, (2 * g + n16) * 16, WS_ST + (grp * 32 + sp) * 128, v4u{asu(n16 ? M1 : M0), tag, asu(n16 ? l1 : l0), tag});
      STAMP(5);
      const LAS unsigned char* vp = L + (4 * g + q4) * 256 + ((w ^ (4 * (g & 1) + q4)) * 32) + p4 * 8;
#pragma unroll
      for (int j = 0; j < 8; ++j) {
        const v4u pa = *(const LAS v4u*)(L + L_P + j * 2048 + lane * 16), pb = *(const LAS v4u*)(L + L_P + j * 2048 + 1024 + lane * 16);
        if (j == 2) { HG = ldc2(rws2, hvo, hso); SB(); }
        if (j == 5) {
#pragma nounroll
          for (unsigned spin = 0; __any(HG.y != tag2) && spin < SPIN_CAP; ++spin) {
            __builtin_amdgcn_s_sleep(PSLP);
            HG = ldc2(rws2, hvo, hso);
          }
          if (lane < 16) *(LAS unsigned*)(L + L_HS + (lane >> 3) * 272 + w * 32 + (lane & 7) * 4) = HG.x;
          lds_signal(L, F_H, lane);
        }
        if (j == 6) { SG = ldc4(rst, svo, sso); SB(); }
        if (j < 7) lds_wait<LSLP>(L, F_FULL, 13 + j); else lds_wait<0>(L, F_FULL, 20);
        const LAS unsigned char* vq = vp + ((4 + j) & 7) * 16384;
        const v4u va = trfrag(vq, 0, 4096), vb = trfrag(vq, 8192, 12288);
        if (j < 4) lds_signal(L, F_DV + j, lane);
        f4 d = mfma32(pa, va, f4{0.f, 0.f, 0.f, 0.f});
        d = mfma32(pb, vb, d);
        o0 = fmaf(st[j].x, d.x + d.y, o0);
        o1 = fmaf(st[j].z, d.z + d.w, o1);
      }
      const bool odd = (lane & 1) != 0;
      const float rcv = dpp<XOR1>(odd ? o0 : o1);
      const unsigned gr = odd ? pk(rcv, o1) : pk(o0, rcv);
      const unsigned gn = dppu<XOR2>(gr);
      if (!(lane & 2))
        stc4(rws, (2 * g + (lane & 1)) * 32768 + (n16 >> 2) * 16, WS_OP + ((ahkv * 64 + w) * 32 + sp) * 128 + ab * 64, v4u{gr, tag, gn, tag});
    }
    STAMP(6);
    if (w == 1) {
      if (__builtin_expect(__any(((SG.y ^ tag) | (SG.w ^ tag)) != 0u), 0)) {
#pragma nounroll
        for (unsigned spin = 0; spin < SPIN_CAP; ++spin) {
          __builtin_amdgcn_s_sleep(2);
          SG = ldc4(rst, svo, sso);
          if (!__any(((SG.y ^ tag) | (SG.w ^ tag)) != 0u)) break;
        }
      }
      const float m = asf(SG.x), M = x16_max(row_max(m));
      const float e = ex2(m - M), Ls = x16_sum(row_sum(e * asf(SG.z)));
      const float wt = e * rcp(Ls), wh = rb(wt);
      LAS unsigned short* wl = (LAS unsigned short*)(L + L_WT);
      wl[(lane >> 5) * 64 + (lane & 31)] = (unsigned short)(asu(wh) >> 16);
      wl[(lane >> 5) * 64 + 32 + (lane & 31)] = (unsigned short)pk(wt - wh, 0.f);
      lds_signal(L, F_W, lane);
    }
    const LAS unsigned char* wp = L + (8 * g + q4) * 256 + ((w ^ (q4 | ((g & 1) << 2))) * 32) + p4 * 8;
    const int fvo = (g >> 1) * 2048 + (g & 1) * 32;
    {
      v4u A[4];
      lds_wait<0>(L, F_H, 8);
#pragma unroll
      for (int s = 0; s < 4; ++s) A[s] = *(const LAS v4u*)(L + L_HS + (lane & 1) * 272 + s * 64 + g * 16);
      lds_wait<LSLP>(L, F_FULL, 22);
      f4 d = {0.f, 0.f, 0.f, 0.f};
#pragma unroll
      for (int s = 0; s < 4; ++s) d = mfma32(trfrag(wp + 4 * 16384, s * 8192, s * 8192 + 1024), A[s], d);
      if (n16 < 2) stc4(rws2, fvo + n16 * 16, WS_MP + ((cgo * 16 + 2 * w) * 32 + ks2) * 64, tg4(d, ta, tb));
    }
    STAMP(7);
    {
      const int ovo = ((n16 >> 2) & 1) * 2048 + g * 512 + (n16 >> 3) * 64 + (lane & 3) * 16, oso = WS_OP + (ho * 8 + w) * 4096;
      v4u G[4];
      lds_wait<0>(L, F_FULL, OGATE);
#pragma unroll
      for (int j = 0; j < 4; ++j) G[j] = ldc4(rws, ovo + j * 128, oso);
      SB();
      lds_wait<0>(L, F_W, 1);
      v2u WF = *(const LAS v2u*)(L + L_WT + (((n16 >> 2) & 1) * 2 + (lane & 1)) * 64 + ((lane >> 1) & 1) * 32 + g * 8);
      LAS unsigned char* osw = L + L_OS + n16 * 272 + w * 32;
      const LAS unsigned char* osr = L + L_OS + (lane & 7) * 272 + g * 16;
      const LAS unsigned char* wop = wp + 6 * 16384;
      int fv2 = fvo + (n16 >> 2) * 16, fso = WS_FP + ((cgo * 16 + 2 * w) * 32 + ho) * 64;
      unsigned vta = ta, vtb = tb;
      asm volatile("" : "+v"(WF), "+v"(osw), "+v"(osr), "+v"(wop), "+v"(fv2), "+s"(fso), "+v"(vta), "+v"(vtb));
      lds_wait<0>(L, F_FULL, 24);
      v4u Bo[4];
#pragma unroll
      for (int s = 0; s < 4; ++s) Bo[s] = trfrag(wop, s * 8192, s * 8192 + 1024);
      asm volatile("" : "+v"(Bo[0]), "+v"(Bo[1]), "+v"(Bo[2]), "+v"(Bo[3]));
      STAMP(8);
#pragma nounroll
      for (unsigned spin = 0; spin < SPIN_CAP; ++spin) {
        bool miss[4], any = false;
#pragma unroll
        for (int j = 0; j < 4; ++j) { miss[j] = __any(((G[j].y ^ tag) | (G[j].w ^ tag)) != 0u); any |= miss[j]; }
        if (!any) break;
        FENCE();
#pragma unroll
        for (int j = 0; j < 4; ++j)
          if (miss[j]) G[j] = ldc4(rws, ovo + j * 128, oso);
      }
      STAMP(9);
      const f4 z = {0.f, 0.f, 0.f, 0.f};
      const f4 e0 = mfma16(v2u{plo(G[0].x, G[1].x), plo(G[2].x, G[3].x)}, WF, z), e1 = mfma16(v2u{phi(G[0].x, G[1].x), phi(G[2].x, G[3].x)}, WF, z);
      const f4 e2 = mfma16(v2u{plo(G[0].z, G[1].z), plo(G[2].z, G[3].z)}, WF, z), e3 = mfma16(v2u{phi(G[0].z, G[1].z), phi(G[2].z, G[3].z)}, WF, z);
      if ((n16 >> 1) == g) {
        *(LAS v4u*)osw = v4u{pk(e0.x, e1.x), pk(e2.x, e3.x), pk(e0.y, e1.y), pk(e2.y, e3.y)};
        *(LAS v4u*)(osw + 16) = v4u{pk(e0.z, e1.z), pk(e2.z, e3.z), pk(e0.w, e1.w), pk(e2.w, e3.w)};
      }
      LGKM0();
      __builtin_amdgcn_s_barrier();
      FENCE();
      v4u a0 = *(const LAS v4u*)osr, a1 = *(const LAS v4u*)(osr + 64), a2 = *(const LAS v4u*)(osr + 128), a3 = *(const LAS v4u*)(osr + 192);
      SB();
      f4 d = mfma32(Bo[0], a0, z), d2 = mfma32(Bo[1], a1, z);
      d = mfma32(Bo[2], a2, d); d2 = mfma32(Bo[3], a3, d2);
      d += d2;
      d.x += dpp<XOR1>(d.x); d.y += dpp<XOR1>(d.y); d.z += dpp<XOR1>(d.z); d.w += dpp<XOR1>(d.w);
      d.x += dpp<XOR2>(d.x); d.y += dpp<XOR2>(d.y); d.z += dpp<XOR2>(d.z); d.w += dpp<XOR2>(d.w);
      if ((n16 & 11) == 0) stc4(rws, fv2, fso, tg4(d, vta, vtb));
    }
    STAMP(10);
  }
}

}

extern "C" void kernel_launch(void* const* d_in, const int* in_sizes, int n_in, void* d_out, int out_size, void* d_ws, size_t ws_size,
                              hipStream_t stream) {
  (void)in_sizes; (void)n_in; (void)out_size; (void)ws_size;
  fused_decode<<<256, NT, 0, stream>>>((const unsigned short*)d_in[0], d_in[1], d_in[2], d_in[3], d_in[4], d_in[6], d_in[7], d_in[8], d_in[9],
                                       d_out, d_ws);
}
```

```cpp
#define PB 0
#define PW 3
#define AMP 4
#include <hip/hip_runtime.h>
#include <hip/hip_bf16.h>

namespace {

typedef unsigned v4u __attribute__((ext_vector_type(4)));
typedef unsigned v2u __attribute__((ext_vector_type(2)));
typedef float f4 __attribute__((ext_vector_type(4)));
typedef float f2 __attribute__((ext_vector_type(2)));
typedef __bf16 bf8 __attribute__((ext_vector_type(8)));
typedef __bf16 bf2 __attribute__((ext_vector_type(2)));
typedef short s4 __attribute__((ext_vector_type(4)));
typedef __amdgpu_buffer_rsrc_t rsrc_t;
#define LAS __attribute__((address_space(3)))

constexpr int NWAVES = 10;
constexpr int NT = 64 * NWAVES;
constexpr unsigned KVB = 2u * 4 * 16384 * 128 * 2, WQB = 1024u * 4096 * 2, W1B = 1024u * 3584 * 2;
constexpr unsigned WS_E = 0;
constexpr unsigned WS_QP = 4096;
constexpr unsigned WS_HP = WS_QP + 131072;
constexpr unsigned WS_QF = WS_HP + 114688;
constexpr unsigned WS_HF = WS_QF + 32768;
constexpr unsigned WS_ST = WS_HF + 28672;
constexpr unsigned WS_OP = WS_ST + 32768;
constexpr unsigned WS_FP = WS_OP + 1048576;
constexpr unsigned WS_MP = WS_FP + 262144;
constexpr unsigned WS_END = WS_MP + 262144;
constexpr int L_P = 131072, L_XS = L_P, L_YS = L_P + 4096, L_QS = L_P + 16384, L_HS = L_QS + 8 * 272, L_OS = L_HS + 2 * 272,
              L_ST = L_OS + 4 * 272, L_WT = L_ST + 512, L_FL = L_WT + 256, L_SINK = L_FL + 128, L_STAMP = L_SINK + 512, L_END = L_STAMP + NWAVES * 256;
constexpr int LDS_BYTES = L_END;
static_assert(L_END <= 163840 && (L_HS % 16) == 0 && (L_OS % 16) == 0 && (L_ST % 16) == 0 && (L_WT % 16) == 0 && (L_FL % 16) == 0, "lds map");
enum { F_FULL = 0, F_LN = 1, F_Q = 2, F_P = 3, F_H = 4, F_REF = 5, F_TAG = 6, F_W = 7, F_D = 8, F_DQ = F_D, F_DH = F_D + 2, F_DK = F_D + 4, F_DV = F_D + 12, F_WORDS = 32 };
constexpr int OOB = 0x7ff00000;
#define AUXL 16
#define AUXS 16
constexpr int AUX_SC1 = 16;
constexpr unsigned SPIN_CAP = 1u << 15, LSPIN_CAP = 1u << 20;
constexpr float LOG2E = 1.4426950408889634f;
#define LSLP 2
#define QGATE 10
#define OGATE 23
#define PSLP 2
#define PF 14
#define WLO 16
#define WHI 36
#define TAU 56
#define TOFF 15
#define TTH 20
#define TOMID 330
#define TOLO 700
#define TAU2 46
#define PROP 0
#define TPROP 16
#define TOHI 200
#define S1GATE 5
#define SGATE 19
#define MPLATE 0
#define PFB 3
#define OPF 1
#define OAUX 16
#define EAUX 16
#define CTL 1
#define WIN 24

__device__ __forceinline__ rsrc_t mk(const void* p, unsigned bytes) { return __builtin_amdgcn_make_buffer_rsrc((void*)p, 0, bytes, 0x00020000); }
__device__ __forceinline__ v4u ldc4(rsrc_t r, int vo, int so) { return __builtin_amdgcn_raw_buffer_load_b128(r, vo, so, AUXL); }
__device__ __forceinline__ v2u ldc2(rsrc_t r, int vo, int so) { return __builtin_amdgcn_raw_buffer_load_b64(r, vo, so, AUXL); }
__device__ __forceinline__ void stc4(rsrc_t r, int vo, int so, v4u v) { __builtin_amdgcn_raw_buffer_store_b128(v, r, vo, so, AUXS); }
__device__ __forceinline__ float asf(unsigned u) { return __builtin_bit_cast(float, u); }
__device__ __forceinline__ unsigned asu(float f) { return __builtin_bit_cast(unsigned, f); }
__device__ __forceinline__ unsigned pk(float a, float b) { f2 v = {a, b}; return __builtin_bit_cast(unsigned, __builtin_convertvector(v, bf2)); }
__device__ __forceinline__ float blo(unsigned u) { return asf(u << 16); }
__device__ __forceinline__ float bhi(unsigned u) { return asf(u & 0xffff0000u); }
__device__ __forceinline__ float rb(float a) { return blo(pk(a, a)); }
__device__ __forceinline__ unsigned plo(unsigned a, unsigned b) { return __builtin_amdgcn_perm(b, a, 0x05040100u); }
__device__ __forceinline__ unsigned phi(unsigned a, unsigned b) { return __builtin_amdgcn_perm(b, a, 0x07060302u); }
__device__ __forceinline__ f4 mfma32(v4u a, v4u b, f4 c) { return __builtin_amdgcn_mfma_f32_16x16x32_bf16(__builtin_bit_cast(bf8, a), __builtin_bit_cast(bf8, b), c, 0, 0, 0); }
__device__ __forceinline__ f4 mfma16(v2u a, v2u b, f4 c) { return __builtin_amdgcn_mfma_f32_16x16x16bf16_1k(__builtin_bit_cast(s4, a), __builtin_bit_cast(s4, b), c, 0, 0, 0); }
template <int C> __device__ __forceinline__ float dpp(float x) { return asf(__builtin_amdgcn_mov_dpp(asu(x), C, 0xf, 0xf, true)); }
template <int C> __device__ __forceinline__ unsigned dppu(unsigned x) { return __builtin_amdgcn_mov_dpp(x, C, 0xf, 0xf, true); }
constexpr int XOR1 = 0xB1, XOR2 = 0x4E, ROR4 = 0x124, ROR8 = 0x128;
__device__ __forceinline__ float x16_sum(float x) { auto s = __builtin_amdgcn_permlane16_swap(asu(x), asu(x), false, false); return asf(s[0]) + asf(s[1]); }
__device__ __forceinline__ float x32_sum(float x) { auto s = __builtin_amdgcn_permlane32_swap(asu(x), asu(x), false, false); return asf(s[0]) + asf(s[1]); }
__device__ __forceinline__ float x16_max(float x) { auto s = __builtin_amdgcn_permlane16_swap(asu(x), asu(x), false, false); return fmaxf(asf(s[0]), asf(s[1])); }
__device__ __forceinline__ float x32_max(float x) { auto s = __builtin_amdgcn_permlane32_swap(asu(x), asu(x), false, false); return fmaxf(asf(s[0]), asf(s[1])); }
__device__ __forceinline__ float row_sum(float x) { x += dpp<XOR1>(x); x += dpp<XOR2>(x); x += dpp<ROR4>(x); x += dpp<ROR8>(x); return x; }
__device__ __forceinline__ float row_max(float x) { x = fmaxf(x, dpp<XOR1>(x)); x = fmaxf(x, dpp<XOR2>(x)); x = fmaxf(x, dpp<ROR4>(x)); x = fmaxf(x, dpp<ROR8>(x)); return x; }
__device__ __forceinline__ float wave_sum(float x) { return x32_sum(x16_sum(row_sum(x))); }
__device__ __forceinline__ float ex2(float x) { return __builtin_amdgcn_exp2f(x); }
__device__ __forceinline__ float rcp(float x) { return __builtin_amdgcn_rcpf(x); }
__device__ __forceinline__ unsigned tg(float v, unsigned t8) { return ((asu(v) + 0x80u) & 0xffffff00u) | t8; }
__device__ __forceinline__ v4u tg4(f4 v, unsigned ta, unsigned tb) { return v4u{tg(v.x, ta), tg(v.y, tb), tg(v.z, ta), tg(v.w, tb)}; }
__device__ __forceinline__ float untg(unsigned u) { return asf(u & 0xffffff00u); }
__device__ __forceinline__ unsigned xoror(unsigned a, unsigned b, unsigned c) { return __builtin_amdgcn_bitop3_b32(a, b, c, 0xBE); }
__device__ __forceinline__ unsigned bad4(v4u u, unsigned ta, unsigned tb) { return xoror(u.x, ta, xoror(u.y, tb, xoror(u.z, ta, u.w ^ tb))) & 0xffu; }
__device__ __forceinline__ float red4(v4u a, v4u b) {
  const auto s01 = __builtin_amdgcn_permlane32_swap(asu(untg(a.x) + untg(b.x)), asu(untg(a.y) + untg(b.y)), false, false);
  const auto s23 = __builtin_amdgcn_permlane32_swap(asu(untg(a.z) + untg(b.z)), asu(untg(a.w) + untg(b.w)), false, false);
  const auto t = __builtin_amdgcn_permlane16_swap(asu(asf(s01[0]) + asf(s01[1])), asu(asf(s23[0]) + asf(s23[1])), false, false);
  float v = asf(t[0]) + asf(t[1]);
  v += dpp<ROR4>(v); v += dpp<ROR8>(v);
  return v;
}
__device__ __forceinline__ v4u trfrag(const LAS unsigned char* a, int off0, int off1) {
  const v2u lo = __builtin_bit_cast(v2u, __builtin_amdgcn_ds_read_tr16_b64_v4i16((LAS s4*)(a + off0)));
  const v2u hi = __builtin_bit_cast(v2u, __builtin_amdgcn_ds_read_tr16_b64_v4i16((LAS s4*)(a + off1)));
  return v4u{lo.x, lo.y, hi.x, hi.y};
}

#define SB() __builtin_amdgcn_sched_barrier(0)
#define FENCE() do { SB(); asm volatile("" ::: "memory"); SB(); } while (0)
#define LGKM0() do { SB(); asm volatile("s_waitcnt lgkmcnt(0)" ::: "memory"); SB(); } while (0)
#define STAMP(i) do { if ((i) == PB && wv == PW) { SB(); ((volatile LAS unsigned*)(L + L_STAMP))[1] = (unsigned)__builtin_amdgcn_s_memrealtime(); SB(); } } while (0)

template <int SLEEP> __device__ __forceinline__ void lds_wait(LAS unsigned char* L, int word, unsigned want) {
  FENCE();
  const volatile LAS unsigned* p = (const volatile LAS unsigned*)(L + L_FL) + word;
#pragma nounroll
  for (unsigned i = 0; i < LSPIN_CAP; ++i) {
    if ((unsigned)__builtin_amdgcn_readfirstlane(*p) >= want) break;
    if (SLEEP) __builtin_amdgcn_s_sleep(SLEEP);
  }
  FENCE();
}
template <int WORD> __device__ __forceinline__ void lds_signal(unsigned vsig) {
  SB();
  asm volatile("s_waitcnt lgkmcnt(0)\n\tds_add_u32 %0, %1 offset:%2" :: "v"(vsig), "v"(1u), "i"(WORD * 4) : "memory");
  SB();
}
template <int GS> __device__ __forceinline__ void dma4(rsrc_t r, int v0, int v1, int v2, int v3, int& so, unsigned& m0v) {
  asm volatile("s_mov_b32 m0, %1\n\ts_nop 0\n\t"
               "buffer_load_dwordx4 %2, %6, %0 offen nt lds\n\ts_add_u32 %0, %0, %7\n\t"
               "buffer_load_dwordx4 %3, %6, %0 offen offset:1024 nt lds\n\ts_add_u32 %0, %0, %7\n\t"
               "buffer_load_dwordx4 %4, %6, %0 offen offset:2048 nt lds\n\ts_add_u32 %0, %0, %7\n\t"
               "buffer_load_dwordx4 %5, %6, %0 offen offset:3072 nt lds\n\ts_add_u32 %0, %0, %8\n\ts_add_u32 %1, %1, 0x1000"
               : "+s"(so), "+s"(m0v) : "v"(v0), "v"(v1), "v"(v2), "v"(v3), "s"(r), "i"(GS - 1024), "i"(GS + 3072) : "memory", "scc");
}
struct Ld { unsigned cnt, nxt, last, vfull; };
__device__ __forceinline__ void pace(Ld& s, unsigned w4, unsigned to) {
  unsigned a, b, v;
  asm volatile("0:\n\t"
               "s_memtime vcc\n\t"
               "s_getreg_b32 %0, hwreg(HW_REG_IB_STS)\n\t"
               "s_and_b32 %1, %0, 15\n\t"
               "s_bfe_u32 %0, %0, 0x20016\n\t"
               "s_lshl4_add_u32 %0, %0, %1\n\t"
               "s_sub_u32 %1, %5, %0\n\t"
               "s_cmp_lt_u32 %1, %3\n\t"
               "s_cbranch_scc1 1f\n\t"
               "s_lshr_b32 %1, %1, 4\n\t"
               "v_mov_b32 %2, %1\n\t"
               "ds_write_b32 %6, %2\n\t"
               "s_lshl_b32 %1, %1, 4\n\t"
               "s_add_u32 %3, %1, 16\n"
               "1:\n\t"
               "s_waitcnt lgkmcnt(0)\n\t"
               "s_cmp_le_u32 %0, %7\n\t"
               "s_cbranch_scc1 2f\n\t"
               "s_sub_u32 %1, vcc_lo, %4\n\t"
               "s_cmp_ge_u32 %1, %8\n\t"
               "s_cselect_b32 %1, %9, 0\n\t"
               "s_cmp_le_u32 %0, %1\n\t"
               "s_cbranch_scc0 0b\n"
               "2:\n\t"
               "s_mov_b32 %4, vcc_lo"
               : "=&s"(a), "=&s"(b), "=&v"(v), "+s"(s.nxt), "+s"(s.last)
               : "s"(s.cnt), "v"(s.vfull), "s"(w4), "s"(to), "i"(WHI - 4) : "memory", "scc", "vcc");
}
template <int GS, int F0, int NF>
__device__ __forceinline__ void stream(LAS unsigned char* L, Ld& s, rsrc_t r, int v0, int v1, int v2, int v3, int so, int rot, unsigned lds0, unsigned& w4, unsigned& to) {
  volatile LAS unsigned* fl = (volatile LAS unsigned*)(L + L_FL);
  const int base = so;
#pragma nounroll
  for (int f = F0; f < F0 + NF; ++f) {
    unsigned m0v = lds0 + (f & 7) * 16384;
    if (NF == 8) so = base + ((f - F0 + rot) & 7) * 16384;
    if (F0 + NF > 8 && f >= 8) {
      const unsigned now = (unsigned)__builtin_amdgcn_s_memrealtime(), ref = __builtin_amdgcn_readfirstlane(fl[F_REF]);
      const int lag = (CTL && ref) ? (int)(now - ref) - (F0 < 20 ? TOFF + (f - 5) * TAU : TOFF + 15 * TAU + (f - 20) * TAU2) : 0;
      w4 = lag > TTH ? WHI - 4 : lag < -TTH ? WLO - 4 : WIN - 4;
      to = lag < -TTH ? TOLO : TOMID;
      lds_wait<LSLP>(L, F_D + (f < 12 ? (f & ~1) : f) - 8, f < 12 ? 4u : f < 20 ? 1u : 8u);
    }
#pragma nounroll
    for (int i = 0; i < 4; ++i) {
      pace(s, w4, to);
      dma4<GS>(r, v0, v1, v2, v3, so, m0v);
      s.cnt += 4;
    }
  }
}

__global__ void __launch_bounds__(NT)
fused_decode(const unsigned short* __restrict__ x, const void* __restrict__ kc, const void* __restrict__ vc, const void* __restrict__ wq,
             const void* __restrict__ wo, const void* __restrict__ w1, const void* __restrict__ b1, const void* __restrict__ w2,
             const void* __restrict__ b2, void* __restrict__ out, void* ws) {
  const unsigned pt0 = (unsigned)__builtin_amdgcn_s_memrealtime();
  unsigned long long wq_early;
  asm volatile("s_load_dwordx2 %0, %1, 0x18" : "=s"(wq_early) : "s"(__builtin_amdgcn_kernarg_segment_ptr()));
  SB();
  __shared__ __attribute__((aligned(16))) unsigned char smem[LDS_BYTES];
  LAS unsigned char* const L = (LAS unsigned char*)smem;
  const int tid = threadIdx.x, lane = tid & 63, wv = __builtin_amdgcn_readfirstlane(tid >> 6), c = blockIdx.x;
  const int g = lane >> 4, n16 = lane & 15;
  const int grp = c & 7, sp = c >> 3, ab = grp >> 2, ahkv = grp & 3;
  const int ksq = c >> 6, cgq = c & 63;
  const bool has1 = grp != (sp & 7);
  const int c1 = sp * 7 + grp - (grp > (sp & 7) ? 1 : 0);
  const int ks1 = c1 / 56, cg1 = c1 % 56;
  const int cgo = sp & 7;
  const int ho = grp * 4 + (sp >> 3);
  const int ks2 = (sp >> 3) * 8 + grp;
  const bool has2 = ks2 < 28;

  const unsigned vsig = (unsigned)(size_t)smem + (lane == 0 ? L_FL : L_SINK + lane * 4);
  SB();
  asm volatile("s_waitcnt lgkmcnt(0)" : "+s"(wq_early));
  SB();
  if (wv >= 1 && wv < NWAVES - 1) {
    const int w = wv - 1;
    STAMP(0);
    const rsrc_t rws = mk(ws, WS_END);
    const rsrc_t rws2 = mk(ws, has2 ? WS_END : 0u);
    const rsrc_t rxl = mk(x, w < 2 ? 4096u : 0u);
    const v4u xa = __builtin_amdgcn_raw_buffer_load_b128(rxl, lane * 16, w * 2048, 0);
    const v4u xb = __builtin_amdgcn_raw_buffer_load_b128(rxl, lane * 16, w * 2048 + 1024, 0);
    FENCE();
    __builtin_amdgcn_s_barrier();
    FENCE();
    if (w < 2) {
      float xf[16];
#pragma unroll
      for (int i = 0; i < 4; ++i) { xf[2 * i] = blo(xa[i]); xf[2 * i + 1] = bhi(xa[i]); xf[8 + 2 * i] = blo(xb[i]); xf[9 + 2 * i] = bhi(xb[i]); }
      const float piv = asf(__builtin_amdgcn_readfirstlane(asu(xf[0])));
      float s1 = 0.f, s2 = 0.f;
#pragma unroll
      for (int i = 0; i < 16; ++i) { const float d = xf[i] - piv; s1 += d; s2 = fmaf(d, d, s2); }
      float a1 = wave_sum(s1) * (1.0f / 1024), a2 = wave_sum(s2) * (1.0f / 1024);
      float mean = rb(piv + a1), inv = rcp(rb(__builtin_amdgcn_sqrtf(rb(rb(fmaxf(a2 - a1 * a1, 0.f)) + 1e-5f))));
      v4u pa, pb;
      s1 = 0.f; s2 = 0.f;
#pragma unroll
      for (int i = 0; i < 8; ++i) {
        const unsigned t = pk(xf[2 * i] - mean, xf[2 * i + 1] - mean), u = pk(blo(t) * inv, bhi(t) * inv);
        if (i < 4) pa[i] = u; else pb[i - 4] = u;
        xf[2 * i] = blo(u); xf[2 * i + 1] = bhi(u);
        s1 += xf[2 * i] + xf[2 * i + 1]; s2 = fmaf(xf[2 * i], xf[2 * i], fmaf(xf[2 * i + 1], xf[2 * i + 1], s2));
      }
      *(LAS v4u*)(L + L_XS + w * 2048 + lane * 16) = pa;
      *(LAS v4u*)(L + L_XS + w * 2048 + 1024 + lane * 16) = pb;
      a1 = wave_sum(s1) * (1.0f / 1024); a2 = wave_sum(s2) * (1.0f / 1024);
      mean = rb(a1); inv = rcp(rb(__builtin_amdgcn_sqrtf(rb(rb(fmaxf(a2 - a1 * a1, 0.f)) + 1e-5f))));
#pragma unroll
      for (int i = 0; i < 8; ++i) {
        const unsigned t = pk(xf[2 * i] - mean, xf[2 * i + 1] - mean), u = pk(blo(t) * inv, bhi(t) * inv);
        if (i < 4) pa[i] = u; else pb[i - 4] = u;
      }
      *(LAS v4u*)(L + L_YS + w * 2048 + lane * 16) = pa;
      *(LAS v4u*)(L + L_YS + w * 2048 + 1024 + lane * 16) = pb;
      lds_signal<F_LN>(vsig);
    }
    const int q4 = n16 >> 2, p4 = lane & 3;
    lds_wait<LSLP>(L, F_LN, 3);
    const unsigned tag = __builtin_amdgcn_readfirstlane(((volatile LAS unsigned*)(L + L_FL))[F_TAG]);
    const unsigned ta = tag & 0xffu, tb = (tag >> 8) & 0xffu;
    STAMP(1);
    {
      const bool isq = w < 4;
      const LAS unsigned char* ap = L + (isq ? L_XS + ksq * 512 : L_YS + (ks1 & 3) * 512) + (lane & 1) * 2048 + g * 16;
      v4u A[8];
#pragma unroll
      for (int s = 0; s < 8; ++s) A[s] = *(const LAS v4u*)(ap + s * 64);
      lds_wait<LSLP>(L, F_FULL, isq ? 2 : 4);
      const LAS unsigned char* bp = L + (isq ? 0 : 32768) + (8 * g + q4) * 128 + (((w & 3) ^ ((q4 >> 1) | ((g & 1) << 1))) * 32) + p4 * 8;
      v4u Bf[8];
#pragma unroll
      for (int s = 0; s < 8; ++s) Bf[s] = trfrag(bp, s * 4096, s * 4096 + 512);
      lds_signal<F_DQ>(vsig + (isq ? 0 : (F_DH - F_DQ) * 4));
      f4 d0 = mfma32(Bf[0], A[0], f4{0.f, 0.f, 0.f, 0.f}), d1 = mfma32(Bf[1], A[1], f4{0.f, 0.f, 0.f, 0.f});
#pragma unroll
      for (int s = 2; s < 8; s += 2) { d0 = mfma32(Bf[s], A[s], d0); d1 = mfma32(Bf[s + 1], A[s + 1], d1); }
      const rsrc_t rp = mk(ws, (isq || has1) ? WS_END : 0u);
      const int pso = (isq ? WS_QP + cgq * 2048 + ksq * 128 : WS_HP + cg1 * 2048 + ks1 * 128) + (w & 3) * 512;
      stc4(rp, n16 < 2 ? g * 32 + n16 * 16 : OOB, pso, tg4(d0 + d1, ta, tb));
    }
    STAMP(2);
    v4u K[16];
    {
      const LAS unsigned char* kp = L + ((4 + w) & 7) * 16384 + n16 * 256;
      lds_wait<1>(L, F_FULL, 5 + w);
#pragma unroll
      for (int s = 0; s < 4; ++s) {
        const LAS unsigned char* kq = kp + (((4 * s + g) ^ n16) * 16);
#pragma unroll
        for (int t = 0; t < 4; ++t) K[4 * t + s] = *(const LAS v4u*)(kq + t * 4096);
      }
      lds_signal<F_DK>(vsig + w * 4);
    }
    STAMP(3);
    lds_wait<1>(L, F_Q, 1);
    {
      const LAS unsigned char* qb = L + L_QS + (n16 >> 1) * 272 + g * 16;
      v4u qf[4];
#pragma unroll
      for (int s = 0; s < 4; ++s) qf[s] = *(const LAS v4u*)(qb + s * 64);
      f4 sc[4];
#pragma unroll
      for (int t = 0; t < 4; ++t) sc[t] = mfma32(K[4 * t], qf[0], f4{0.f, 0.f, 0.f, 0.f});
#pragma unroll
      for (int s = 1; s < 4; ++s)
#pragma unroll
        for (int t = 0; t < 4; ++t) sc[t] = mfma32(K[4 * t + s], qf[s], sc[t]);
#pragma unroll
      for (int t = 0; t < 4; ++t)
#pragma unroll
        for (int r = 0; r < 4; r += 2) {
          const unsigned u = pk(sc[t][r], sc[t][r + 1]), v = pk(blo(u) * 0.088388347648318440f, bhi(u) * 0.088388347648318440f);
          sc[t][r] = blo(v); sc[t][r + 1] = bhi(v);
        }
      float mw = fmaxf(fmaxf(sc[0].x, sc[0].y), fmaxf(sc[0].z, sc[0].w));
#pragma unroll
      for (int t = 1; t < 4; ++t) mw = fmaxf(mw, fmaxf(fmaxf(sc[t].x, sc[t].y), fmaxf(sc[t].z, sc[t].w)));
      mw = x32_max(x16_max(mw)) * LOG2E;
      float lw = 0.f;
      const unsigned pm = (lane & 1) ? 0xffff0000u : 0u;
      unsigned P[8];
#pragma unroll
      for (int t = 0; t < 4; ++t)
#pragma unroll
        for (int r = 0; r < 4; r += 2) {
          const float v0 = ex2(fmaf(sc[t][r], LOG2E, -mw)), v1 = ex2(fmaf(sc[t][r + 1], LOG2E, -mw));
          lw += v0 + v1;
          const unsigned h = pk(v0, v1);
          P[2 * t + (r >> 1)] = pk(v0 - asf((h << 16) & pm), v1 - asf(h & pm));
        }
      *(LAS v4u*)(L + L_P + w * 2048 + lane * 16) = v4u{P[0], P[1], P[2], P[3]};
      *(LAS v4u*)(L + L_P + w * 2048 + 1024 + lane * 16) = v4u{P[4], P[5], P[6], P[7]};
      lw = x32_sum(x16_sum(lw));
      if (lane < 16 && !(lane & 1)) *(LAS f2*)(L + L_ST + (w * 8 + (lane >> 1)) * 8) = f2{mw, lw};
      lds_signal<F_P>(vsig);
    }
    STAMP(4);
    lds_wait<1>(L, F_P, 8);
    float o0 = 0.f, o1 = 0.f;
    {
      f4 st[8];
#pragma unroll
      for (int j = 0; j < 8; ++j) st[j] = *(const LAS f4*)(L + L_ST + (j * 8 + 2 * g) * 8);
      float M0 = st[0].x, M1 = st[0].z;
#pragma unroll
      for (int j = 1; j < 8; ++j) { M0 = fmaxf(M0, st[j].x); M1 = fmaxf(M1, st[j].z); }
      M0 = __builtin_ceilf(M0); M1 = __builtin_ceilf(M1);
#pragma unroll
      for (int j = 0; j < 8; ++j) { st[j].x = ex2(st[j].x - M0); st[j].z = ex2(st[j].z - M1); }
      STAMP(5);
      int ovs = (lane & 2) ? OOB : (2 * g + (lane & 1)) * 32768 + (n16 >> 2) * 16;
      asm volatile("" : "+v"(ovs));
      const LAS unsigned char* vp = L + (4 * g + q4) * 256 + ((w ^ (4 * (g & 1) + q4)) * 32) + p4 * 8;
#pragma unroll
      for (int j = 0; j < 8; ++j) {
        const v4u pa = *(const LAS v4u*)(L + L_P + j * 2048 + lane * 16), pb = *(const LAS v4u*)(L + L_P + j * 2048 + 1024 + lane * 16);
        if (j < 7) lds_wait<LSLP>(L, F_FULL, 13 + j); else lds_wait<0>(L, F_FULL, 20);
        const LAS unsigned char* vq = vp + ((4 + j) & 7) * 16384;
        const v4u va = trfrag(vq, 0, 4096), vb = trfrag(vq, 8192, 12288);
        if (j == 0) lds_signal<F_DV>(vsig); else if (j == 1) lds_signal<F_DV + 1>(vsig); else if (j == 2) lds_signal<F_DV + 2>(vsig); else if (j == 3) lds_signal<F_DV + 3>(vsig);
        f4 d = mfma32(pa, va, f4{0.f, 0.f, 0.f, 0.f});
        d = mfma32(pb, vb, d);
        o0 = fmaf(st[j].x, d.x + d.y, o0);
        o1 = fmaf(st[j].z, d.z + d.w, o1);
      }
      const bool odd = (lane & 1) != 0;
      const float rcv = dpp<XOR1>(odd ? o0 : o1);
      const unsigned gr = odd ? pk(rcv, o1) : pk(o0, rcv);
      const unsigned gn = dppu<XOR2>(gr);
      stc4(rws, ovs, WS_OP + ((ahkv * 64 + w) * 32 + sp) * 128 + ab * 64, v4u{gr, tag, gn, tag});
    }
    STAMP(6);
    const LAS unsigned char* wp = L + (8 * g + q4) * 256 + ((w ^ (q4 | ((g & 1) << 2))) * 32) + p4 * 8;
    const int fvo = (g >> 1) * 2048 + (g & 1) * 32;
    v4u MPv;
    {
      v4u A[4];
      lds_wait<1>(L, F_H, 1);
#pragma unroll
      for (int s = 0; s < 4; ++s) A[s] = *(const LAS v4u*)(L + L_HS + (lane & 1) * 272 + s * 64 + g * 16);
      lds_wait<LSLP>(L, F_FULL, 22);
      f4 d = {0.f, 0.f, 0.f, 0.f};
#pragma unroll
      for (int s = 0; s < 4; ++s) d = mfma32(trfrag(wp + 4 * 16384, s * 8192, s * 8192 + 1024), A[s], d);
      MPv = tg4(d, ta, tb);
      if (!MPLATE) stc4(rws2, n16 < 2 ? fvo + n16 * 16 : OOB, WS_MP + ((cgo * 16 + 2 * w) * 32 + ks2) * 64, MPv);
    }
    STAMP(7);
    {
      const int ovo = ((n16 >> 2) & 1) * 2048 + g * 512 + (n16 >> 3) * 64 + (lane & 3) * 16, oso = WS_OP + (ho * 8 + w) * 4096;
      v4u G[4];
      lds_wait<0>(L, F_FULL, OGATE);
#pragma unroll
      for (int j = 0; j < 4; ++j) G[j] = ldc4(rws, ovo + j * 128, oso);
      SB();
      if (MPLATE) stc4(rws2, n16 < 2 ? fvo + n16 * 16 : OOB, WS_MP + ((cgo * 16 + 2 * w) * 32 + ks2) * 64, MPv);
      SB();
      lds_wait<0>(L, F_W, 1);
      v2u WF = *(const LAS v2u*)(L + L_WT + (lane & 3) * 32 + g * 8);
      float invl = ((const LAS float*)(L + L_WT + 128))[(lane >> 1) & 1];
      LAS unsigned char* osw = L + (n16 == g ? L_OS + g * 272 + w * 32 : L_P + w * 2048 + lane * 32);
      const LAS unsigned char* osr = L + L_OS + (lane & 3) * 272 + g * 16;
      const LAS unsigned char* wop = wp + 6 * 16384;
      int fv2 = (n16 & 13) == 0 ? fvo + (n16 >> 1) * 16 : OOB, fso = WS_FP + ((cgo * 16 + 2 * w) * 32 + ho) * 64;
      unsigned vta = ta, vtb = tb, vtag = tag;
      asm volatile("" : "+v"(WF), "+v"(invl), "+v"(osw), "+v"(osr), "+v"(wop), "+v"(fv2), "+s"(fso), "+v"(vta), "+v"(vtb), "+v"(vtag));
      lds_wait<0>(L, F_FULL, 24);
      v4u Bo[4];
#pragma unroll
      for (int s = 0; s < 4; ++s) Bo[s] = trfrag(wop, s * 8192, s * 8192 + 1024);
      SB();
      STAMP(8);
      const f4 z = {0.f, 0.f, 0.f, 0.f};
      f4 e0, e1, e2, e3;
#define COMBINE() do { \
        e0 = mfma16(v2u{plo(G[0].x, G[1].x), plo(G[2].x, G[3].x)}, WF, z); e1 = mfma16(v2u{phi(G[0].x, G[1].x), phi(G[2].x, G[3].x)}, WF, z); \
        e2 = mfma16(v2u{plo(G[0].z, G[1].z), plo(G[2].z, G[3].z)}, WF, z); e3 = mfma16(v2u{phi(G[0].z, G[1].z), phi(G[2].z, G[3].z)}, WF, z); } while (0)
#define OBAD(j, acc) xoror(G[j].y, vtag, xoror(G[j].w, vtag, acc))
      COMBINE();
      SB();
      if (__builtin_expect(__any((OBAD(0, OBAD(1, 0u)) | OBAD(2, OBAD(3, 0u))) != 0u), 0)) {
#pragma nounroll
        for (unsigned spin = 0; spin < SPIN_CAP; ++spin) {
          const bool m0 = __any(OBAD(0, 0u) != 0u), m1 = __any(OBAD(1, 0u) != 0u), m2 = __any(OBAD(2, 0u) != 0u), m3 = __any(OBAD(3, 0u) != 0u);
          if (!(m0 || m1 || m2 || m3)) break;
          FENCE();
          if (m0) G[0] = ldc4(rws, ovo, oso);
          if (m1) G[1] = ldc4(rws, ovo + 128, oso);
          if (m2) G[2] = ldc4(rws, ovo + 256, oso);
          if (m3) G[3] = ldc4(rws, ovo + 384, oso);
        }
        COMBINE();
      }
      STAMP(9);
      *(LAS v4u*)osw = v4u{pk(e0.x, e1.x), pk(e2.x, e3.x), pk(e0.y, e1.y), pk(e2.y, e3.y)};
      *(LAS v4u*)(osw + 16) = v4u{pk(e0.z, e1.z), pk(e2.z, e3.z), pk(e0.w, e1.w), pk(e2.w, e3.w)};
      LGKM0();
      __builtin_amdgcn_s_barrier();
      FENCE();
      v4u a0 = *(const LAS v4u*)osr, a1 = *(const LAS v4u*)(osr + 64), a2 = *(const LAS v4u*)(osr + 128), a3 = *(const LAS v4u*)(osr + 192);
      SB();
      f4 d = mfma32(Bo[0], a0, z), d2 = mfma32(Bo[1], a1, z);
      d = mfma32(Bo[2], a2, d); d2 = mfma32(Bo[3], a3, d2);
      d += d2;
      d.x += dpp<XOR1>(d.x); d.y += dpp<XOR1>(d.y); d.z += dpp<XOR1>(d.z); d.w += dpp<XOR1>(d.w);
      d *= invl;
      stc4(rws, fv2, fso, v4u{(asu(d.x) & 0xffffff00u) | vta, (asu(d.y) & 0xffffff00u) | vtb, (asu(d.z) & 0xffffff00u) | vta, (asu(d.w) & 0xffffff00u) | vtb});
    }
    STAMP(10);
  } else if (wv == NWAVES - 1) {
    STAMP(0);
    volatile LAS unsigned* fl = (volatile LAS unsigned*)(L + L_FL);
    const rsrc_t rws = mk(ws, WS_END), rws1 = mk(ws, has1 ? WS_END : 0u), rws2 = mk(ws, has2 ? WS_END : 0u), rout = mk(out, 4096);
    const unsigned eraw = __builtin_amdgcn_raw_buffer_load_b32(rws, 0, WS_E, AUX_SC1);
    const unsigned b1p = __builtin_amdgcn_raw_buffer_load_b32(mk(b1, has1 ? 7168u : 0u), (n16 >> 2) * 8 + (lane & 1) * 4, c1 * 32, 0);
    const int col2 = ((lane >> 1) & 1) * 8 + ((g >> 1) | ((g & 1) << 1)) * 2;
    const int fo = (lane & 1) * 2048 + col2;
    const unsigned short xfin = __builtin_amdgcn_raw_buffer_load_b16(mk(x, c < 128 ? 4096u : 0u), fo, c * 16, 0);
    const unsigned short b2fin = __builtin_amdgcn_raw_buffer_load_b16(mk(b2, c < 128 ? 2048u : 0u), col2, c * 16, 0);
    const v4u codepf = __builtin_amdgcn_raw_buffer_load_b128(mk((const void*)((__builtin_amdgcn_s_getpc() & ~255ull) - PFB * 1024u), PF * 1024u), lane * 16, sp * 1024, 0);
    SB();
    if (lane < F_WORDS) fl[lane] = 0u;
    LGKM0();
    __builtin_amdgcn_s_barrier();
    FENCE();
    const unsigned tag = __builtin_amdgcn_readfirstlane(eraw) * 0x9E3779B1u + 0x7F4A7C15u;
    const unsigned ta = tag & 0xffu, tb = (tag >> 8) & 0xffu;
    fl[F_TAG] = tag;
    lds_signal<F_LN>(vsig);
    asm volatile("" :: "v"(codepf));
    lds_wait<1>(L, F_FULL, 4);
    const unsigned t4 = (unsigned)__builtin_amdgcn_s_memrealtime();
    lds_wait<LSLP>(L, F_FULL, S1GATE);
    {
      const int fvo = (lane < 16 && !(lane & 1)) ? (lane >> 2) * 16 : OOB, fb = (lane >> 1) & 1;
      bool needq = true, needh = has1;
#pragma nounroll
      for (unsigned spin = 0; (needq || needh) && spin < SPIN_CAP; ++spin) {
        const v2u GQ = ldc2(rws, needq ? lane * 8 : OOB, WS_QP + c * 512), GH = ldc2(rws1, needh ? lane * 8 : OOB, WS_HP + c1 * 512);
        if (needq && !__any((((GQ.x ^ ta) | (GQ.y ^ tb)) & 0xffu) != 0u)) {
          const unsigned gr = pk(x32_sum(x16_sum(untg(GQ.x))), x32_sum(x16_sum(untg(GQ.y)))), gn = dppu<XOR1>(gr), ty = (tag << 12) | (t4 & 0xfffu);
          stc4(rws, fvo + fb * 16384, WS_QF + c * 64, v4u{gr, ty, gn, ty});
          needq = false;
          STAMP(13);
        }
        if (needh && !__any((((GH.x ^ ta) | (GH.y ^ tb)) & 0xffu) != 0u)) {
          float h0 = rb(rb(x32_sum(x16_sum(untg(GH.x)))) + blo(b1p)), h1 = rb(rb(x32_sum(x16_sum(untg(GH.y)))) + bhi(b1p));
          constexpr float GC = -2.0f * 0.7978845608028654f * 1.4426950408889634f;
          h0 = h0 * rcp(1.0f + ex2(GC * (h0 + 0.044715f * h0 * h0 * h0)));
          h1 = h1 * rcp(1.0f + ex2(GC * (h1 + 0.044715f * h1 * h1 * h1)));
          const unsigned gr = pk(h0, h1), gn = dppu<XOR1>(gr);
          stc4(rws, fvo + fb * 14336, WS_HF + c1 * 64, v4u{gr, tag, gn, tag});
          needh = false;
          STAMP(14);
        }
        __builtin_amdgcn_s_sleep(PSLP);
      }
    }
    lds_wait<LSLP>(L, F_FULL, QGATE);
    {
      v4u Q[4];
      const unsigned t20 = tag & 0xfffffu;
#pragma nounroll
      for (unsigned spin = 0; spin < SPIN_CAP; ++spin) {
        unsigned bad = 0;
#pragma unroll
        for (int i = 0; i < 4; ++i) Q[i] = ldc4(rws, lane * 16, WS_QF + grp * 4096 + i * 1024);
#pragma unroll
        for (int i = 0; i < 4; ++i) bad |= ((Q[i].y >> 12) ^ t20) | ((Q[i].w >> 12) ^ t20);
        if (!__any(bad != 0u)) break;
        __builtin_amdgcn_s_sleep(PSLP);
      }
#pragma unroll
      for (int i = 0; i < 4; ++i) *(LAS v2u*)(L + L_QS + (2 * i + (lane >> 5)) * 272 + (lane & 31) * 8) = v2u{Q[i].x, Q[i].z};
      lds_signal<F_Q>(vsig);
      STAMP(15);
      if (CTL) {
        int d = 0;
#pragma unroll
        for (int i = 0; i < 4; ++i) d += (int)((Q[i].y - t4) << 20) >> 20;
        fl[F_REF] = (t4 + (int)(wave_sum((float)d) * (1.0f / 256))) | 1u;
      }
    }
    lds_wait<1>(L, F_P, 8);
    {
      f2 sv[8];
#pragma unroll
      for (int j = 0; j < 8; ++j) sv[j] = *(const LAS f2*)(L + L_ST + (j * 8 + (lane & 7)) * 8);
      float M = sv[0].x, Ls = 0.f;
#pragma unroll
      for (int j = 1; j < 8; ++j) M = fmaxf(M, sv[j].x);
      M = __builtin_ceilf(M);
#pragma unroll
      for (int j = 0; j < 8; ++j) Ls = fmaf(ex2(sv[j].x - M), sv[j].y, Ls);
      stc4(rws, lane < 8 ? (lane >> 2) * 2048 + (lane & 3) * 16 : OOB, WS_ST + grp * 4096 + sp * 64, v4u{asu(M), tag, asu(Ls), tag});
      const unsigned tag2 = has2 ? tag : 0u;
      v4u H;
#pragma nounroll
      for (unsigned spin = 0; spin < SPIN_CAP; ++spin) {
        H = ldc4(rws2, (lane >> 5) * 14336 + (lane & 31) * 16, WS_HF + ks2 * 512);
        if (!__any(((H.y ^ tag2) | (H.w ^ tag2)) != 0u)) break;
        __builtin_amdgcn_s_sleep(PSLP);
      }
      *(LAS v2u*)(L + L_HS + (lane >> 5) * 272 + (lane & 31) * 8) = v2u{H.x, H.z};
      lds_signal<F_H>(vsig);
      STAMP(16);
    }
    lds_wait<LSLP>(L, F_FULL, SGATE);
    {
      v4u SG;
#pragma nounroll
      for (unsigned spin = 0; spin < SPIN_CAP; ++spin) {
        SG = ldc4(rws, (lane >> 5) * 16384 + (lane & 31) * 64, WS_ST + (ho >> 2) * 2048 + (ho & 3) * 16);
        if (!__any(((SG.y ^ tag) | (SG.w ^ tag)) != 0u)) break;
        __builtin_amdgcn_s_sleep(PSLP);
      }
      const float m = asf(SG.x), M = x16_max(row_max(m));
      const float e = rb(ex2(m - M)), Ls = x16_sum(row_sum(e * asf(SG.z)));
      ((LAS unsigned short*)(L + L_WT))[lane] = (unsigned short)(asu(e) >> 16);
      ((LAS float*)(L + L_WT + 128))[lane >> 5] = rcp(Ls) * (1.0f + 1.0f / 131072);
      lds_signal<F_W>(vsig);
      STAMP(17);
    }
    {
      const rsrc_t rwf = mk(ws, c < 128 ? WS_END : 0u);
      const unsigned mlive = lane < 48 ? 0xffffffffu : 0u;
      __builtin_amdgcn_s_barrier();
      FENCE();
      if (c < 128) {
        v4u M0 = ldc4(rwf, lane * 16, WS_MP + c * 2048), M1 = ldc4(rwf, lane * 16, WS_MP + c * 2048 + 1024);
        v4u F0 = ldc4(rwf, lane * 16, WS_FP + c * 2048), F1 = ldc4(rwf, lane * 16, WS_FP + c * 2048 + 1024);
        SB();
        if (__any((bad4(M0, ta, tb) | (bad4(M1, ta, tb) & mlive)) != 0u)) {
#pragma nounroll
          for (unsigned spin = 0; spin < SPIN_CAP; ++spin) {
            FENCE();
            M0 = ldc4(rwf, lane * 16, WS_MP + c * 2048); M1 = ldc4(rwf, lane * 16, WS_MP + c * 2048 + 1024);
            if (!__any((bad4(M0, ta, tb) | (bad4(M1, ta, tb) & mlive)) != 0u)) break;
          }
        }
        const float m = rb(rb(red4(M0, M1 & mlive)) + blo(b2fin));
        float gt = rb(m * rcp(1.0f + ex2(-1.4426950408889634f * m))), xv = blo(xfin);
        asm volatile("" : "+v"(gt), "+v"(xv));
#pragma nounroll
        for (unsigned spin = 0; spin < SPIN_CAP; ++spin) {
          const bool m0 = __any(bad4(F0, ta, tb) != 0u), m1 = __any(bad4(F1, ta, tb) != 0u);
          if (!(m0 || m1)) break;
          FENCE();
          if (m0) F0 = ldc4(rwf, lane * 16, WS_FP + c * 2048);
          if (m1) F1 = ldc4(rwf, lane * 16, WS_FP + c * 2048 + 1024);
        }
        STAMP(25);
        const float o = xv + rb(rb(red4(F0, F1)) * gt);
        __builtin_amdgcn_raw_buffer_store_b16((unsigned short)pk(o, o), rout, (lane & 12) == 0 ? fo : OOB, c * 16, OAUX);
        __builtin_amdgcn_raw_buffer_store_b32(tag, rws, (c | lane) == 0 ? 0 : OOB, WS_E, EAUX);
        STAMP(26);
        { LGKM0(); unsigned d = AMP * (__builtin_amdgcn_readfirstlane(((volatile LAS unsigned*)(L + L_STAMP))[1]) - pt0); if (d > 6000) d = 6000; const unsigned t1 = (unsigned)__builtin_amdgcn_s_memrealtime(); for (int it = 0; it < 100000; ++it) if ((unsigned)__builtin_amdgcn_s_memrealtime() - t1 >= d) break; }
      }
    }
  } else {
    __builtin_amdgcn_s_setprio(3);
    STAMP(0);
    const rsrc_t rq = mk((const void*)wq_early, WQB);
    const int r8 = lane >> 3, c8 = lane & 7;
    const int f0 = r8 & 2;
    const int vq0 = r8 * 8192 + (c8 ^ f0) * 16, vq1 = vq0 ^ 64;
    int so = ksq * 256 * 8192 + cgq * 128;
    unsigned m0v = (unsigned)(size_t)smem;
#pragma unroll
    for (int i = 0; i < WIN / 4; ++i) dma4<8 * 8192>(rq, vq0, vq1, vq0, vq1, so, m0v);
    SB();
    volatile LAS unsigned* fl = (volatile LAS unsigned*)(L + L_FL);
    const unsigned lds0 = (unsigned)(size_t)smem;
    Ld st = {WIN, 16, 0, lds0 + L_FL + F_FULL * 4};
    unsigned w4 = WIN - 4, to = 0x7fffffffu;
    __builtin_amdgcn_s_barrier();
    FENCE();
#pragma nounroll
    for (int i = WIN / 4; i < 8; ++i) { pace(st, w4, to); dma4<8 * 8192>(rq, vq0, vq1, vq0, vq1, so, m0v); st.cnt += 4; }
    to = TOMID;
    {
      const int v0 = r8 * 7168 + (c8 ^ f0) * 16;
      stream<8 * 7168, 2, 2>(L, st, mk(w1, has1 ? W1B : 0u), v0, v0 ^ 64, v0, v0 ^ 64, ks1 * 256 * 7168 + cg1 * 128, 0, lds0, w4, to);
    }
    const int kvb = (grp * 16384 + sp * 512) * 256;
    {
      const int v0 = g * 256 + (n16 ^ g) * 16;
      stream<1024, 4, 8>(L, st, mk(kc, KVB), v0, v0 ^ 64, v0 ^ 128, v0 ^ 192, kvb, sp, lds0, w4, to);
    }
    {
      const int v0 = g * 256 + (n16 ^ (2 * g)) * 16;
      stream<1024, 12, 8>(L, st, mk(vc, KVB), v0, v0 ^ 128, v0, v0 ^ 128, kvb, sp, lds0, w4, to);
    }
    {
      const int v0 = g * 2048 + (n16 ^ (2 * g)) * 16;
      stream<8192, 20, 2>(L, st, mk(w2, has2 ? W1B : 0u), v0, v0, v0 ^ 128, v0 ^ 128, ks2 * 128 * 2048 + cgo * 256, 0, lds0, w4, to);
      stream<8192, 22, 2>(L, st, mk(wo, WQB), v0, v0, v0 ^ 128, v0 ^ 128, ho * 128 * 2048 + cgo * 256, 0, lds0, w4, to);
    }
    pace(st, 0u, 0x7fffffffu);
    asm volatile("s_waitcnt vmcnt(0)" ::: "memory");
    fl[F_FULL] = 24;
  }
}

}

extern "C" void kernel_launch(void* const* d_in, const int* in_sizes, int n_in, void* d_out, int out_size, void* d_ws, size_t ws_size,
                              hipStream_t stream) {
  (void)in_sizes; (void)n_in; (void)out_size; (void)ws_size;
  fused_decode<<<256, NT, 0, stream>>>((const unsigned short*)d_in[0], d_in[1], d_in[2], d_in[3], d_in[4], d_in[6], d_in[7], d_in[8], d_in[9],
                                       d_out, d_ws);
}
```

```cpp
#include <hip/hip_runtime.h>
#include <hip/hip_bf16.h>

namespace {

typedef unsigned v4u __attribute__((ext_vector_type(4)));
typedef unsigned v2u __attribute__((ext_vector_type(2)));
typedef float f4 __attribute__((ext_vector_type(4)));
typedef float f2 __attribute__((ext_vector_type(2)));
typedef __bf16 bf8 __attribute__((ext_vector_type(8)));
typedef __bf16 bf2 __attribute__((ext_vector_type(2)));
typedef short s4 __attribute__((ext_vector_type(4)));
typedef __amdgpu_buffer_rsrc_t rsrc_t;
#define LAS __attribute__((address_space(3)))

constexpr int NWAVES = 10;
constexpr int NT = 64 * NWAVES;
constexpr unsigned KVB = 2u * 4 * 16384 * 128 * 2, WQB = 1024u * 4096 * 2, W1B = 1024u * 3584 * 2;
constexpr unsigned WS_E = 0;
constexpr unsigned WS_QP = 4096;
constexpr unsigned WS_HP = WS_QP + 131072;
constexpr unsigned WS_QF = WS_HP + 114688;
constexpr unsigned WS_HF = WS_QF + 32768;
constexpr unsigned WS_ST = WS_HF + 28672;
constexpr unsigned WS_OP = WS_ST + 32768;
constexpr unsigned WS_FP = WS_OP + 1048576;
constexpr unsigned WS_MP = WS_FP + 262144;
constexpr unsigned WS_END = WS_MP + 262144;
constexpr int L_P = 131072, L_XS = L_P, L_YS = L_P + 4096, L_QS = L_P + 16384, L_HS = L_QS + 8 * 272, L_OS = L_HS + 2 * 272,
              L_ST = L_OS + 4 * 272, L_WT = L_ST + 512, L_FL = L_WT + 256, L_SINK = L_FL + 128, L_STAMP = L_SINK + 512, L_END = L_STAMP + NWAVES * 256;
constexpr int LDS_BYTES = L_END;
static_assert(L_END <= 163840 && (L_HS % 16) == 0 && (L_OS % 16) == 0 && (L_ST % 16) == 0 && (L_WT % 16) == 0 && (L_FL % 16) == 0, "lds map");
enum { F_FULL = 0, F_LN = 1, F_Q = 2, F_P = 3, F_H = 4, F_REF = 5, F_TAG = 6, F_W = 7, F_D = 8, F_DQ = F_D, F_DH = F_D + 2, F_DK = F_D + 4, F_DV = F_D + 12, F_WORDS = 32 };
constexpr int OOB = 0x7ff00000;
#define AUXL 16
#define AUXS 16
constexpr int AUX_SC1 = 16;
constexpr unsigned SPIN_CAP = 1u << 15, LSPIN_CAP = 1u << 20;
constexpr float LOG2E = 1.4426950408889634f;
#define LSLP 2
#define QGATE 10
#define OGATE 23
#define PSLP 2
#define PF 14
#define WLO 16
#define WHI 36
#define TAU 56
#define TOFF 15
#define TTH 20
#define TOMID 330
#define TOLO 700
#define TAU2 46
#define PROP 0
#define TPROP 16
#define TOHI 200
#define S1GATE 5
#define SGATE 19
#define MPLATE 0
#define PFB 3
#define OPF 1
#define OAUX 16
#define EAUX 16
#define CTL 1
#define WIN 24

__device__ __forceinline__ rsrc_t mk(const void* p, unsigned bytes) { return __builtin_amdgcn_make_buffer_rsrc((void*)p, 0, bytes, 0x00020000); }
__device__ __forceinline__ v4u ldc4(rsrc_t r, int vo, int so) { return __builtin_amdgcn_raw_buffer_load_b128(r, vo, so, AUXL); }
__device__ __forceinline__ v2u ldc2(rsrc_t r, int vo, int so) { return __builtin_amdgcn_raw_buffer_load_b64(r, vo, so, AUXL); }
__device__ __forceinline__ void stc4(rsrc_t r, int vo, int so, v4u v) { __builtin_amdgcn_raw_buffer_store_b128(v, r, vo, so, AUXS); }
__device__ __forceinline__ float asf(unsigned u) { return __builtin_bit_cast(float, u); }
__device__ __forceinline__ unsigned asu(float f) { return __builtin_bit_cast(unsigned, f); }
__device__ __forceinline__ unsigned pk(float a, float b) { f2 v = {a, b}; return __builtin_bit_cast(unsigned, __builtin_convertvector(v, bf2)); }
__device__ __forceinline__ float blo(unsigned u) { return asf(u << 16); }
__device__ __forceinline__ float bhi(unsigned u) { return asf(u & 0xffff0000u); }
__device__ __forceinline__ float rb(float a) { return blo(pk(a, a)); }
__device__ __forceinline__ unsigned plo(unsigned a, unsigned b) { return __builtin_amdgcn_perm(b, a, 0x05040100u); }
__device__ __forceinline__ unsigned phi(unsigned a, unsigned b) { return __builtin_amdgcn_perm(b, a, 0x07060302u); }
__device__ __forceinline__ f4 mfma32(v4u a, v4u b, f4 c) { return __builtin_amdgcn_mfma_f32_16x16x32_bf16(__builtin_bit_cast(bf8, a), __builtin_bit_cast(bf8, b), c, 0, 0, 0); }
__device__ __forceinline__ f4 mfma16(v2u a, v2u b, f4 c) { return __builtin_amdgcn_mfma_f32_16x16x16bf16_1k(__builtin_bit_cast(s4, a), __builtin_bit_cast(s4, b), c, 0, 0, 0); }
template <int C> __device__ __forceinline__ float dpp(float x) { return asf(__builtin_amdgcn_mov_dpp(asu(x), C, 0xf, 0xf, true)); }
template <int C> __device__ __forceinline__ unsigned dppu(unsigned x) { return __builtin_amdgcn_mov_dpp(x, C, 0xf, 0xf, true); }
constexpr int XOR1 = 0xB1, XOR2 = 0x4E, ROR4 = 0x124, ROR8 = 0x128;
__device__ __forceinline__ float x16_sum(float x) { auto s = __builtin_amdgcn_permlane16_swap(asu(x), asu(x), false, false); return asf(s[0]) + asf(s[1]); }
__device__ __forceinline__ float x32_sum(float x) { auto s = __builtin_amdgcn_permlane32_swap(asu(x), asu(x), false, false); return asf(s[0]) + asf(s[1]); }
__device__ __forceinline__ float x16_max(float x) { auto s = __builtin_amdgcn_permlane16_swap(asu(x), asu(x), false, false); return fmaxf(asf(s[0]), asf(s[1])); }
__device__ __forceinline__ float x32_max(float x) { auto s = __builtin_amdgcn_permlane32_swap(asu(x), asu(x), false, false); return fmaxf(asf(s[0]), asf(s[1])); }
__device__ __forceinline__ float row_sum(float x) { x += dpp<XOR1>(x); x += dpp<XOR2>(x); x += dpp<ROR4>(x); x += dpp<ROR8>(x); return x; }
__device__ __forceinline__ float row_max(float x) { x = fmaxf(x, dpp<XOR1>(x)); x = fmaxf(x, dpp<XOR2>(x)); x = fmaxf(x, dpp<ROR4>(x)); x = fmaxf(x, dpp<ROR8>(x)); return x; }
__device__ __forceinline__ float wave_sum(float x) { return x32_sum(x16_sum(row_sum(x))); }
__device__ __forceinline__ float ex2(float x) { return __builtin_amdgcn_exp2f(x); }
__device__ __forceinline__ float rcp(float x) { return __builtin_amdgcn_rcpf(x); }
__device__ __forceinline__ unsigned tg(float v, unsigned t8) { return ((asu(v) + 0x80u) & 0xffffff00u) | t8; }
__device__ __forceinline__ v4u tg4(f4 v, unsigned ta, unsigned tb) { return v4u{tg(v.x, ta), tg(v.y, tb), tg(v.z, ta), tg(v.w, tb)}; }
__device__ __forceinline__ float untg(unsigned u) { return asf(u & 0xffffff00u); }
__device__ __forceinline__ unsigned xoror(unsigned a, unsigned b, unsigned c) { return __builtin_amdgcn_bitop3_b32(a, b, c, 0xBE); }
__device__ __forceinline__ unsigned bad4(v4u u, unsigned ta, unsigned tb) { return xoror(u.x, ta, xoror(u.y, tb, xoror(u.z, ta, u.w ^ tb))) & 0xffu; }
__device__ __forceinline__ float red4(v4u a, v4u b) {
  const auto s01 = __builtin_amdgcn_permlane32_swap(asu(untg(a.x) + untg(b.x)), asu(untg(a.y) + untg(b.y)), false, false);
  const auto s23 = __builtin_amdgcn_permlane32_swap(asu(untg(a.z) + untg(b.z)), asu(untg(a.w) + untg(b.w)), false, false);
  const auto t = __builtin_amdgcn_permlane16_swap(asu(asf(s01[0]) + asf(s01[1])), asu(asf(s23[0]) + asf(s23[1])), false, false);
  float v = asf(t[0]) + asf(t[1]);
  v += dpp<ROR4>(v); v += dpp<ROR8>(v);
  return v;
}
__device__ __forceinline__ v4u trfrag(const LAS unsigned char* a, int off0, int off1) {
  const v2u lo = __builtin_bit_cast(v2u, __builtin_amdgcn_ds_read_tr16_b64_v4i16((LAS s4*)(a + off0)));
  const v2u hi = __builtin_bit_cast(v2u, __builtin_amdgcn_ds_read_tr16_b64_v4i16((LAS s4*)(a + off1)));
  return v4u{lo.x, lo.y, hi.x, hi.y};
}

#define SB() __builtin_amdgcn_sched_barrier(0)
#define FENCE() do { SB(); asm volatile("" ::: "memory"); SB(); } while (0)
#define LGKM0() do { SB(); asm volatile("s_waitcnt lgkmcnt(0)" ::: "memory"); SB(); } while (0)
#define STAMP(i)

template <int SLEEP> __device__ __forceinline__ void lds_wait(LAS unsigned char* L, int word, unsigned want) {
  FENCE();
  const volatile LAS unsigned* p = (const volatile LAS unsigned*)(L + L_FL) + word;
#pragma nounroll
  for (unsigned i = 0; i < LSPIN_CAP; ++i) {
    if ((unsigned)__builtin_amdgcn_readfirstlane(*p) >= want) break;
    if (SLEEP) __builtin_amdgcn_s_sleep(SLEEP);
  }
  FENCE();
}
template <int WORD> __device__ __forceinline__ void lds_signal(unsigned vsig) {
  SB();
  asm volatile("s_waitcnt lgkmcnt(0)\n\tds_add_u32 %0, %1 offset:%2" :: "v"(vsig), "v"(1u), "i"(WORD * 4) : "memory");
  SB();
}
template <int GS> __device__ __forceinline__ void dma4(rsrc_t r, int v0, int v1, int v2, int v3, int& so, unsigned& m0v) {
  asm volatile("s_mov_b32 m0, %1\n\ts_nop 0\n\t"
               "buffer_load_dwordx4 %2, %6, %0 offen nt lds\n\ts_add_u32 %0, %0, %7\n\t"
               "buffer_load_dwordx4 %3, %6, %0 offen offset:1024 nt lds\n\ts_add_u32 %0, %0, %7\n\t"
               "buffer_load_dwordx4 %4, %6, %0 offen offset:2048 nt lds\n\ts_add_u32 %0, %0, %7\n\t"
               "buffer_load_dwordx4 %5, %6, %0 offen offset:3072 nt lds\n\ts_add_u32 %0, %0, %8\n\ts_add_u32 %1, %1, 0x1000"
               : "+s"(so), "+s"(m0v) : "v"(v0), "v"(v1), "v"(v2), "v"(v3), "s"(r), "i"(GS - 1024), "i"(GS + 3072) : "memory", "scc");
}
struct Ld { unsigned cnt, nxt, last, vfull; };
__device__ __forceinline__ void pace(Ld& s, unsigned w4, unsigned to) {
  unsigned a, b, v;
  asm volatile("0:\n\t"
               "s_memtime vcc\n\t"
               "s_getreg_b32 %0, hwreg(HW_REG_IB_STS)\n\t"
               "s_and_b32 %1, %0, 15\n\t"
               "s_bfe_u32 %0, %0, 0x20016\n\t"
               "s_lshl4_add_u32 %0, %0, %1\n\t"
               "s_sub_u32 %1, %5, %0\n\t"
               "s_cmp_lt_u32 %1, %3\n\t"
               "s_cbranch_scc1 1f\n\t"
               "s_lshr_b32 %1, %1, 4\n\t"
               "v_mov_b32 %2, %1\n\t"
               "ds_write_b32 %6, %2\n\t"
               "s_lshl_b32 %1, %1, 4\n\t"
               "s_add_u32 %3, %1, 16\n"
               "1:\n\t"
               "s_waitcnt lgkmcnt(0)\n\t"
               "s_cmp_le_u32 %0, %7\n\t"
               "s_cbranch_scc1 2f\n\t"
               "s_sub_u32 %1, vcc_lo, %4\n\t"
               "s_cmp_ge_u32 %1, %8\n\t"
               "s_cselect_b32 %1, %9, 0\n\t"
               "s_cmp_le_u32 %0, %1\n\t"
               "s_cbranch_scc0 0b\n"
               "2:\n\t"
               "s_mov_b32 %4, vcc_lo"
               : "=&s"(a), "=&s"(b), "=&v"(v), "+s"(s.nxt), "+s"(s.last)
               : "s"(s.cnt), "v"(s.vfull), "s"(w4), "s"(to), "i"(WHI - 4) : "memory", "scc", "vcc");
}
template <int GS, int F0, int NF>
__device__ __forceinline__ void stream(LAS unsigned char* L, Ld& s, rsrc_t r, int v0, int v1, int v2, int v3, int so, int rot, unsigned lds0, unsigned& w4, unsigned& to) {
  volatile LAS unsigned* fl = (volatile LAS unsigned*)(L + L_FL);
  const int base = so;
#pragma nounroll
  for (int f = F0; f < F0 + NF; ++f) {
    unsigned m0v = lds0 + (f & 7) * 16384;
    if (NF == 8) so = base + ((f - F0 + rot) & 7) * 16384;
    if (F0 + NF > 8 && f >= 8) {
      const unsigned now = (unsigned)__builtin_amdgcn_s_memrealtime(), ref = __builtin_amdgcn_readfirstlane(fl[F_REF]);
      const int lag = (CTL && ref) ? (int)(now - ref) - (F0 < 20 ? TOFF + (f - 5) * TAU : TOFF + 15 * TAU + (f - 20) * TAU2) : 0;
      w4 = lag > TTH ? WHI - 4 : lag < -TTH ? WLO - 4 : WIN - 4;
      to = lag < -TTH ? TOLO : TOMID;
      lds_wait<LSLP>(L, F_D + (f < 12 ? (f & ~1) : f) - 8, f < 12 ? 4u : f < 20 ? 1u : 8u);
    }
#pragma nounroll
    for (int i = 0; i < 4; ++i) {
      pace(s, w4, to);
      dma4<GS>(r, v0, v1, v2, v3, so, m0v);
      s.cnt += 4;
    }
  }
}

__global__ void __launch_bounds__(NT)
fused_decode(const unsigned short* __restrict__ x, const void* __restrict__ kc, const void* __restrict__ vc, const void* __restrict__ wq,
             const void* __restrict__ wo, const void* __restrict__ w1, const void* __restrict__ b1, const void* __restrict__ w2,
             const void* __restrict__ b2, void* __restrict__ out, void* ws) {
  const unsigned pt0 = (unsigned)__builtin_amdgcn_s_memrealtime();
  unsigned long long wq_early;
  asm volatile("s_load_dwordx2 %0, %1, 0x18" : "=s"(wq_early) : "s"(__builtin_amdgcn_kernarg_segment_ptr()));
  SB();
  __shared__ __attribute__((aligned(16))) unsigned char smem[LDS_BYTES];
  LAS unsigned char* const L = (LAS unsigned char*)smem;
  const int tid = threadIdx.x, lane = tid & 63, wv = __builtin_amdgcn_readfirstlane(tid >> 6), c = blockIdx.x;
  const int g = lane >> 4, n16 = lane & 15;
  const int grp = c & 7, sp = c >> 3, ab = grp >> 2, ahkv = grp & 3;
  const int ksq = c >> 6, cgq = c & 63;
  const bool has1 = grp != (sp & 7);
  const int c1 = sp * 7 + grp - (grp > (sp & 7) ? 1 : 0);
  const int ks1 = c1 / 56, cg1 = c1 % 56;
  const int cgo = sp & 7;
  const int ho = grp * 4 + (sp >> 3);
  const int ks2 = (sp >> 3) * 8 + grp;
  const bool has2 = ks2 < 28;

  const unsigned vsig = (unsigned)(size_t)smem + (lane == 0 ? L_FL : L_SINK + lane * 4);
  SB();
  asm volatile("s_waitcnt lgkmcnt(0)" : "+s"(wq_early));
  SB();
  const unsigned pt1 = (unsigned)__builtin_amdgcn_s_memrealtime();
  SB();
  if (wv >= 1 && wv < NWAVES - 1) {
    const int w = wv - 1;
    STAMP(0);
    const rsrc_t rws = mk(ws, WS_END);
    const rsrc_t rws2 = mk(ws, has2 ? WS_END : 0u);
    const rsrc_t rxl = mk(x, w < 2 ? 4096u : 0u);
    const v4u xa = __builtin_amdgcn_raw_buffer_load_b128(rxl, lane * 16, w * 2048, 0);
    const v4u xb = __builtin_amdgcn_raw_buffer_load_b128(rxl, lane * 16, w * 2048 + 1024, 0);
    FENCE();
    __builtin_amdgcn_s_barrier();
    FENCE();
    if (w < 2) {
      float xf[16];
#pragma unroll
      for (int i = 0; i < 4; ++i) { xf[2 * i] = blo(xa[i]); xf[2 * i + 1] = bhi(xa[i]); xf[8 + 2 * i] = blo(xb[i]); xf[9 + 2 * i] = bhi(xb[i]); }
      const float piv = asf(__builtin_amdgcn_readfirstlane(asu(xf[0])));
      float s1 = 0.f, s2 = 0.f;
#pragma unroll
      for (int i = 0; i < 16; ++i) { const float d = xf[i] - piv; s1 += d; s2 = fmaf(d, d, s2); }
      float a1 = wave_sum(s1) * (1.0f / 1024), a2 = wave_sum(s2) * (1.0f / 1024);
      float mean = rb(piv + a1), inv = rcp(rb(__builtin_amdgcn_sqrtf(rb(rb(fmaxf(a2 - a1 * a1, 0.f)) + 1e-5f))));
      v4u pa, pb;
      s1 = 0.f; s2 = 0.f;
#pragma unroll
      for (int i = 0; i < 8; ++i) {
        const unsigned t = pk(xf[2 * i] - mean, xf[2 * i + 1] - mean), u = pk(blo(t) * inv, bhi(t) * inv);
        if (i < 4) pa[i] = u; else pb[i - 4] = u;
        xf[2 * i] = blo(u); xf[2 * i + 1] = bhi(u);
        s1 += xf[2 * i] + xf[2 * i + 1]; s2 = fmaf(xf[2 * i], xf[2 * i], fmaf(xf[2 * i + 1], xf[2 * i + 1], s2));
      }
      *(LAS v4u*)(L + L_XS + w * 2048 + lane * 16) = pa;
      *(LAS v4u*)(L + L_XS + w * 2048 + 1024 + lane * 16) = pb;
      a1 = wave_sum(s1) * (1.0f / 1024); a2 = wave_sum(s2) * (1.0f / 1024);
      mean = rb(a1); inv = rcp(rb(__builtin_amdgcn_sqrtf(rb(rb(fmaxf(a2 - a1 * a1, 0.f)) + 1e-5f))));
#pragma unroll
      for (int i = 0; i < 8; ++i) {
        const unsigned t = pk(xf[2 * i] - mean, xf[2 * i + 1] - mean), u = pk(blo(t) * inv, bhi(t) * inv);
        if (i < 4) pa[i] = u; else pb[i - 4] = u;
      }
      *(LAS v4u*)(L + L_YS + w * 2048 + lane * 16) = pa;
      *(LAS v4u*)(L + L_YS + w * 2048 + 1024 + lane * 16) = pb;
      lds_signal<F_LN>(vsig);
    }
    const int q4 = n16 >> 2, p4 = lane & 3;
    lds_wait<LSLP>(L, F_LN, 3);
    const unsigned tag = __builtin_amdgcn_readfirstlane(((volatile LAS unsigned*)(L + L_FL))[F_TAG]);
    const unsigned ta = tag & 0xffu, tb = (tag >> 8) & 0xffu;
    STAMP(1);
    {
      const bool isq = w < 4;
      const LAS unsigned char* ap = L + (isq ? L_XS + ksq * 512 : L_YS + (ks1 & 3) * 512) + (lane & 1) * 2048 + g * 16;
      v4u A[8];
#pragma unroll
      for (int s = 0; s < 8; ++s) A[s] = *(const LAS v4u*)(ap + s * 64);
      lds_wait<LSLP>(L, F_FULL, isq ? 2 : 4);
      const LAS unsigned char* bp = L + (isq ? 0 : 32768) + (8 * g + q4) * 128 + (((w & 3) ^ ((q4 >> 1) | ((g & 1) << 1))) * 32) + p4 * 8;
      v4u Bf[8];
#pragma unroll
      for (int s = 0; s < 8; ++s) Bf[s] = trfrag(bp, s * 4096, s * 4096 + 512);
      lds_signal<F_DQ>(vsig + (isq ? 0 : (F_DH - F_DQ) * 4));
      f4 d0 = mfma32(Bf[0], A[0], f4{0.f, 0.f, 0.f, 0.f}), d1 = mfma32(Bf[1], A[1], f4{0.f, 0.f, 0.f, 0.f});
#pragma unroll
      for (int s = 2; s < 8; s += 2) { d0 = mfma32(Bf[s], A[s], d0); d1 = mfma32(Bf[s + 1], A[s + 1], d1); }
      const rsrc_t rp = mk(ws, (isq || has1) ? WS_END : 0u);
      const int pso = (isq ? WS_QP + cgq * 2048 + ksq * 128 : WS_HP + cg1 * 2048 + ks1 * 128) + (w & 3) * 512;
      stc4(rp, n16 < 2 ? g * 32 + n16 * 16 : OOB, pso, tg4(d0 + d1, ta, tb));
    }
    STAMP(2);
    v4u K[16];
    {
      const LAS unsigned char* kp = L + ((4 + w) & 7) * 16384 + n16 * 256;
      lds_wait<1>(L, F_FULL, 5 + w);
#pragma unroll
      for (int s = 0; s < 4; ++s) {
        const LAS unsigned char* kq = kp + (((4 * s + g) ^ n16) * 16);
#pragma unroll
        for (int t = 0; t < 4; ++t) K[4 * t + s] = *(const LAS v4u*)(kq + t * 4096);
      }
      lds_signal<F_DK>(vsig + w * 4);
    }
    STAMP(3);
    lds_wait<1>(L, F_Q, 1);
    {
      const LAS unsigned char* qb = L + L_QS + (n16 >> 1) * 272 + g * 16;
      v4u qf[4];
#pragma unroll
      for (int s = 0; s < 4; ++s) qf[s] = *(const LAS v4u*)(qb + s * 64);
      f4 sc[4];
#pragma unroll
      for (int t = 0; t < 4; ++t) sc[t] = mfma32(K[4 * t], qf[0], f4{0.f, 0.f, 0.f, 0.f});
#pragma unroll
      for (int s = 1; s < 4; ++s)
#pragma unroll
        for (int t = 0; t < 4; ++t) sc[t] = mfma32(K[4 * t + s], qf[s], sc[t]);
#pragma unroll
      for (int t = 0; t < 4; ++t)
#pragma unroll
        for (int r = 0; r < 4; r += 2) {
          const unsigned u = pk(sc[t][r], sc[t][r + 1]), v = pk(blo(u) * 0.088388347648318440f, bhi(u) * 0.088388347648318440f);
          sc[t][r] = blo(v); sc[t][r + 1] = bhi(v);
        }
      float mw = fmaxf(fmaxf(sc[0].x, sc[0].y), fmaxf(sc[0].z, sc[0].w));
#pragma unroll
      for (int t = 1; t < 4; ++t) mw = fmaxf(mw, fmaxf(fmaxf(sc[t].x, sc[t].y), fmaxf(sc[t].z, sc[t].w)));
      mw = x32_max(x16_max(mw)) * LOG2E;
      float lw = 0.f;
      const unsigned pm = (lane & 1) ? 0xffff0000u : 0u;
      unsigned P[8];
#pragma unroll
      for (int t = 0; t < 4; ++t)
#pragma unroll
        for (int r = 0; r < 4; r += 2) {
          const float v0 = ex2(fmaf(sc[t][r], LOG2E, -mw)), v1 = ex2(fmaf(sc[t][r + 1], LOG2E, -mw));
          lw += v0 + v1;
          const unsigned h = pk(v0, v1);
          P[2 * t + (r >> 1)] = pk(v0 - asf((h << 16) & pm), v1 - asf(h & pm));
        }
      *(LAS v4u*)(L + L_P + w * 2048 + lane * 16) = v4u{P[0], P[1], P[2], P[3]};
      *(LAS v4u*)(L + L_P + w * 2048 + 1024 + lane * 16) = v4u{P[4], P[5], P[6], P[7]};
      lw = x32_sum(x16_sum(lw));
      if (lane < 16 && !(lane & 1)) *(LAS f2*)(L + L_ST + (w * 8 + (lane >> 1)) * 8) = f2{mw, lw};
      lds_signal<F_P>(vsig);
    }
    STAMP(4);
    lds_wait<1>(L, F_P, 8);
    float o0 = 0.f, o1 = 0.f;
    {
      f4 st[8];
#pragma unroll
      for (int j = 0; j < 8; ++j) st[j] = *(const LAS f4*)(L + L_ST + (j * 8 + 2 * g) * 8);
      float M0 = st[0].x, M1 = st[0].z;
#pragma unroll
      for (int j = 1; j < 8; ++j) { M0 = fmaxf(M0, st[j].x); M1 = fmaxf(M1, st[j].z); }
      M0 = __builtin_ceilf(M0); M1 = __builtin_ceilf(M1);
#pragma unroll
      for (int j = 0; j < 8; ++j) { st[j].x = ex2(st[j].x - M0); st[j].z = ex2(st[j].z - M1); }
      STAMP(5);
      int ovs = (lane & 2) ? OOB : (2 * g + (lane & 1)) * 32768 + (n16 >> 2) * 16;
      asm volatile("" : "+v"(ovs));
      const LAS unsigned char* vp = L + (4 * g + q4) * 256 + ((w ^ (4 * (g & 1) + q4)) * 32) + p4 * 8;
#pragma unroll
      for (int j = 0; j < 8; ++j) {
        const v4u pa = *(const LAS v4u*)(L + L_P + j * 2048 + lane * 16), pb = *(const LAS v4u*)(L + L_P + j * 2048 + 1024 + lane * 16);
        if (j < 7) lds_wait<LSLP>(L, F_FULL, 13 + j); else lds_wait<0>(L, F_FULL, 20);
        const LAS unsigned char* vq = vp + ((4 + j) & 7) * 16384;
        const v4u va = trfrag(vq, 0, 4096), vb = trfrag(vq, 8192, 12288);
        if (j == 0) lds_signal<F_DV>(vsig); else if (j == 1) lds_signal<F_DV + 1>(vsig); else if (j == 2) lds_signal<F_DV + 2>(vsig); else if (j == 3) lds_signal<F_DV + 3>(vsig);
        f4 d = mfma32(pa, va, f4{0.f, 0.f, 0.f, 0.f});
        d = mfma32(pb, vb, d);
        o0 = fmaf(st[j].x, d.x + d.y, o0);
        o1 = fmaf(st[j].z, d.z + d.w, o1);
      }
      const bool odd = (lane & 1) != 0;
      const float rcv = dpp<XOR1>(odd ? o0 : o1);
      const unsigned gr = odd ? pk(rcv, o1) : pk(o0, rcv);
      const unsigned gn = dppu<XOR2>(gr);
      stc4(rws, ovs, WS_OP + ((ahkv * 64 + w) * 32 + sp) * 128 + ab * 64, v4u{gr, tag, gn, tag});
    }
    STAMP(6);
    const LAS unsigned char* wp = L + (8 * g + q4) * 256 + ((w ^ (q4 | ((g & 1) << 2))) * 32) + p4 * 8;
    const int fvo = (g >> 1) * 2048 + (g & 1) * 32;
    v4u MPv;
    {
      v4u A[4];
      lds_wait<1>(L, F_H, 1);
#pragma unroll
      for (int s = 0; s < 4; ++s) A[s] = *(const LAS v4u*)(L + L_HS + (lane & 1) * 272 + s * 64 + g * 16);
      lds_wait<LSLP>(L, F_FULL, 22);
      f4 d = {0.f, 0.f, 0.f, 0.f};
#pragma unroll
      for (int s = 0; s < 4; ++s) d = mfma32(trfrag(wp + 4 * 16384, s * 8192, s * 8192 + 1024), A[s], d);
      MPv = tg4(d, ta, tb);
      if (!MPLATE) stc4(rws2, n16 < 2 ? fvo + n16 * 16 : OOB, WS_MP + ((cgo * 16 + 2 * w) * 32 + ks2) * 64, MPv);
    }
    STAMP(7);
    {
      const int ovo = ((n16 >> 2) & 1) * 2048 + g * 512 + (n16 >> 3) * 64 + (lane & 3) * 16, oso = WS_OP + (ho * 8 + w) * 4096;
      v4u G[4];
      lds_wait<0>(L, F_FULL, OGATE);
#pragma unroll
      for (int j = 0; j < 4; ++j) G[j] = ldc4(rws, ovo + j * 128, oso);
      SB();
      if (MPLATE) stc4(rws2, n16 < 2 ? fvo + n16 * 16 : OOB, WS_MP + ((cgo * 16 + 2 * w) * 32 + ks2) * 64, MPv);
      SB();
      lds_wait<0>(L, F_W, 1);
      v2u WF = *(const LAS v2u*)(L + L_WT + (lane & 3) * 32 + g * 8);
      float invl = ((const LAS float*)(L + L_WT + 128))[(lane >> 1) & 1];
      LAS unsigned char* osw = L + (n16 == g ? L_OS + g * 272 + w * 32 : L_P + w * 2048 + lane * 32);
      const LAS unsigned char* osr = L + L_OS + (lane & 3) * 272 + g * 16;
      const LAS unsigned char* wop = wp + 6 * 16384;
      int fv2 = (n16 & 13) == 0 ? fvo + (n16 >> 1) * 16 : OOB, fso = WS_FP + ((cgo * 16 + 2 * w) * 32 + ho) * 64;
      unsigned vta = ta, vtb = tb, vtag = tag;
      asm volatile("" : "+v"(WF), "+v"(invl), "+v"(osw), "+v"(osr), "+v"(wop), "+v"(fv2), "+s"(fso), "+v"(vta), "+v"(vtb), "+v"(vtag));
      lds_wait<0>(L, F_FULL, 24);
      v4u Bo[4];
#pragma unroll
      for (int s = 0; s < 4; ++s) Bo[s] = trfrag(wop, s * 8192, s * 8192 + 1024);
      SB();
      STAMP(8);
      const f4 z = {0.f, 0.f, 0.f, 0.f};
      f4 e0, e1, e2, e3;
#define COMBINE() do { \
        e0 = mfma16(v2u{plo(G[0].x, G[1].x), plo(G[2].x, G[3].x)}, WF, z); e1 = mfma16(v2u{phi(G[0].x, G[1].x), phi(G[2].x, G[3].x)}, WF, z); \
        e2 = mfma16(v2u{plo(G[0].z, G[1].z), plo(G[2].z, G[3].z)}, WF, z); e3 = mfma16(v2u{phi(G[0].z, G[1].z), phi(G[2].z, G[3].z)}, WF, z); } while (0)
#define OBAD(j, acc) xoror(G[j].y, vtag, xoror(G[j].w, vtag, acc))
      COMBINE();
      SB();
      if (__builtin_expect(__any((OBAD(0, OBAD(1, 0u)) | OBAD(2, OBAD(3, 0u))) != 0u), 0)) {
#pragma nounroll
        for (unsigned spin = 0; spin < SPIN_CAP; ++spin) {
          const bool m0 = __any(OBAD(0, 0u) != 0u), m1 = __any(OBAD(1, 0u) != 0u), m2 = __any(OBAD(2, 0u) != 0u), m3 = __any(OBAD(3, 0u) != 0u);
          if (!(m0 || m1 || m2 || m3)) break;
          FENCE();
          if (m0) G[0] = ldc4(rws, ovo, oso);
          if (m1) G[1] = ldc4(rws, ovo + 128, oso);
          if (m2) G[2] = ldc4(rws, ovo + 256, oso);
          if (m3) G[3] = ldc4(rws, ovo + 384, oso);
        }
        COMBINE();
      }
      STAMP(9);
      *(LAS v4u*)osw = v4u{pk(e0.x, e1.x), pk(e2.x, e3.x), pk(e0.y, e1.y), pk(e2.y, e3.y)};
      *(LAS v4u*)(osw + 16) = v4u{pk(e0.z, e1.z), pk(e2.z, e3.z), pk(e0.w, e1.w), pk(e2.w, e3.w)};
      LGKM0();
      __builtin_amdgcn_s_barrier();
      FENCE();
      v4u a0 = *(const LAS v4u*)osr, a1 = *(const LAS v4u*)(osr + 64), a2 = *(const LAS v4u*)(osr + 128), a3 = *(const LAS v4u*)(osr + 192);
      SB();
      f4 d = mfma32(Bo[0], a0, z), d2 = mfma32(Bo[1], a1, z);
      d = mfma32(Bo[2], a2, d); d2 = mfma32(Bo[3], a3, d2);
      d += d2;
      d.x += dpp<XOR1>(d.x); d.y += dpp<XOR1>(d.y); d.z += dpp<XOR1>(d.z); d.w += dpp<XOR1>(d.w);
      d *= invl;
      stc4(rws, fv2, fso, v4u{(asu(d.x) & 0xffffff00u) | vta, (asu(d.y) & 0xffffff00u) | vtb, (asu(d.z) & 0xffffff00u) | vta, (asu(d.w) & 0xffffff00u) | vtb});
    }
    STAMP(10);
  } else if (wv == NWAVES - 1) {
    STAMP(0);
    const unsigned pt2 = (unsigned)__builtin_amdgcn_s_memrealtime();
    SB();
    volatile LAS unsigned* fl = (volatile LAS unsigned*)(L + L_FL);
    const rsrc_t rws = mk(ws, WS_END), rws1 = mk(ws, has1 ? WS_END : 0u), rws2 = mk(ws, has2 ? WS_END : 0u), rout = mk(out, 4096);
    const unsigned eraw = __builtin_amdgcn_raw_buffer_load_b32(rws, 0, WS_E, AUX_SC1);
    const unsigned b1p = __builtin_amdgcn_raw_buffer_load_b32(mk(b1, has1 ? 7168u : 0u), (n16 >> 2) * 8 + (lane & 1) * 4, c1 * 32, 0);
    const int col2 = ((lane >> 1) & 1) * 8 + ((g >> 1) | ((g & 1) << 1)) * 2;
    const int fo = (lane & 1) * 2048 + col2;
    const unsigned short xfin = __builtin_amdgcn_raw_buffer_load_b16(mk(x, c < 128 ? 4096u : 0u), fo, c * 16, 0);
    const unsigned short b2fin = __builtin_amdgcn_raw_buffer_load_b16(mk(b2, c < 128 ? 2048u : 0u), col2, c * 16, 0);
    const v4u codepf = __builtin_amdgcn_raw_buffer_load_b128(mk((const void*)((__builtin_amdgcn_s_getpc() & ~255ull) - PFB * 1024u), PF * 1024u), lane * 16, sp * 1024, 0);
    SB();
    if (lane < F_WORDS) fl[lane] = 0u;
    LGKM0();
    __builtin_amdgcn_s_barrier();
    FENCE();
    const unsigned tag = __builtin_amdgcn_readfirstlane(eraw) * 0x9E3779B1u + 0x7F4A7C15u;
    const unsigned ta = tag & 0xffu, tb = (tag >> 8) & 0xffu;
    const unsigned pt3 = (unsigned)__builtin_amdgcn_s_memrealtime();
    fl[F_TAG] = tag;
    lds_signal<F_LN>(vsig);
    asm volatile("" :: "v"(codepf));
    lds_wait<0>(L, F_FULL, 1);
    const unsigned pt4 = (unsigned)__builtin_amdgcn_s_memrealtime();
    lds_wait<1>(L, F_FULL, 4);
    const unsigned t4 = (unsigned)__builtin_amdgcn_s_memrealtime();
    lds_wait<LSLP>(L, F_FULL, S1GATE);
    {
      const int fvo = (lane < 16 && !(lane & 1)) ? (lane >> 2) * 16 : OOB, fb = (lane >> 1) & 1;
      bool needq = true, needh = has1;
#pragma nounroll
      for (unsigned spin = 0; (needq || needh) && spin < SPIN_CAP; ++spin) {
        const v2u GQ = ldc2(rws, needq ? lane * 8 : OOB, WS_QP + c * 512), GH = ldc2(rws1, needh ? lane * 8 : OOB, WS_HP + c1 * 512);
        if (needq && !__any((((GQ.x ^ ta) | (GQ.y ^ tb)) & 0xffu) != 0u)) {
          const unsigned gr = pk(x32_sum(x16_sum(untg(GQ.x))), x32_sum(x16_sum(untg(GQ.y)))), gn = dppu<XOR1>(gr), ty = (tag << 12) | (t4 & 0xfffu);
          stc4(rws, fvo + fb * 16384, WS_QF + c * 64, v4u{gr, ty, gn, ty});
          needq = false;
          STAMP(13);
        }
        if (needh && !__any((((GH.x ^ ta) | (GH.y ^ tb)) & 0xffu) != 0u)) {
          float h0 = rb(rb(x32_sum(x16_sum(untg(GH.x)))) + blo(b1p)), h1 = rb(rb(x32_sum(x16_sum(untg(GH.y)))) + bhi(b1p));
          constexpr float GC = -2.0f * 0.7978845608028654f * 1.4426950408889634f;
          h0 = h0 * rcp(1.0f + ex2(GC * (h0 + 0.044715f * h0 * h0 * h0)));
          h1 = h1 * rcp(1.0f + ex2(GC * (h1 + 0.044715f * h1 * h1 * h1)));
          const unsigned gr = pk(h0, h1), gn = dppu<XOR1>(gr);
          stc4(rws, fvo + fb * 14336, WS_HF + c1 * 64, v4u{gr, tag, gn, tag});
          needh = false;
          STAMP(14);
        }
        __builtin_amdgcn_s_sleep(PSLP);
      }
    }
    lds_wait<LSLP>(L, F_FULL, QGATE);
    {
      v4u Q[4];
      const unsigned t20 = tag & 0xfffffu;
#pragma nounroll
      for (unsigned spin = 0; spin < SPIN_CAP; ++spin) {
        unsigned bad = 0;
#pragma unroll
        for (int i = 0; i < 4; ++i) Q[i] = ldc4(rws, lane * 16, WS_QF + grp * 4096 + i * 1024);
#pragma unroll
        for (int i = 0; i < 4; ++i) bad |= ((Q[i].y >> 12) ^ t20) | ((Q[i].w >> 12) ^ t20);
        if (!__any(bad != 0u)) break;
        __builtin_amdgcn_s_sleep(PSLP);
      }
#pragma unroll
      for (int i = 0; i < 4; ++i) *(LAS v2u*)(L + L_QS + (2 * i + (lane >> 5)) * 272 + (lane & 31) * 8) = v2u{Q[i].x, Q[i].z};
      lds_signal<F_Q>(vsig);
      STAMP(15);
      if (CTL) {
        int d = 0;
#pragma unroll
        for (int i = 0; i < 4; ++i) d += (int)((Q[i].y - t4) << 20) >> 20;
        fl[F_REF] = (t4 + (int)(wave_sum((float)d) * (1.0f / 256))) | 1u;
      }
    }
    lds_wait<1>(L, F_P, 8);
    {
      f2 sv[8];
#pragma unroll
      for (int j = 0; j < 8; ++j) sv[j] = *(const LAS f2*)(L + L_ST + (j * 8 + (lane & 7)) * 8);
      float M = sv[0].x, Ls = 0.f;
#pragma unroll
      for (int j = 1; j < 8; ++j) M = fmaxf(M, sv[j].x);
      M = __builtin_ceilf(M);
#pragma unroll
      for (int j = 0; j < 8; ++j) Ls = fmaf(ex2(sv[j].x - M), sv[j].y, Ls);
      stc4(rws, lane < 8 ? (lane >> 2) * 2048 + (lane & 3) * 16 : OOB, WS_ST + grp * 4096 + sp * 64, v4u{asu(M), tag, asu(Ls), tag});
      const unsigned tag2 = has2 ? tag : 0u;
      v4u H;
#pragma nounroll
      for (unsigned spin = 0; spin < SPIN_CAP; ++spin) {
        H = ldc4(rws2, (lane >> 5) * 14336 + (lane & 31) * 16, WS_HF + ks2 * 512);
        if (!__any(((H.y ^ tag2) | (H.w ^ tag2)) != 0u)) break;
        __builtin_amdgcn_s_sleep(PSLP);
      }
      *(LAS v2u*)(L + L_HS + (lane >> 5) * 272 + (lane & 31) * 8) = v2u{H.x, H.z};
      lds_signal<F_H>(vsig);
      STAMP(16);
    }
    lds_wait<LSLP>(L, F_FULL, SGATE);
    {
      v4u SG;
#pragma nounroll
      for (unsigned spin = 0; spin < SPIN_CAP; ++spin) {
        SG = ldc4(rws, (lane >> 5) * 16384 + (lane & 31) * 64, WS_ST + (ho >> 2) * 2048 + (ho & 3) * 16);
        if (!__any(((SG.y ^ tag) | (SG.w ^ tag)) != 0u)) break;
        __builtin_amdgcn_s_sleep(PSLP);
      }
      const float m = asf(SG.x), M = x16_max(row_max(m));
      const float e = rb(ex2(m - M)), Ls = x16_sum(row_sum(e * asf(SG.z)));
      ((LAS unsigned short*)(L + L_WT))[lane] = (unsigned short)(asu(e) >> 16);
      ((LAS float*)(L + L_WT + 128))[lane >> 5] = rcp(Ls) * (1.0f + 1.0f / 131072);
      lds_signal<F_W>(vsig);
      STAMP(17);
    }
    {
      const rsrc_t rwf = mk(ws, c < 128 ? WS_END : 0u);
      const unsigned mlive = lane < 48 ? 0xffffffffu : 0u;
      __builtin_amdgcn_s_barrier();
      FENCE();
      if (c < 128) {
        v4u M0 = ldc4(rwf, lane * 16, WS_MP + c * 2048), M1 = ldc4(rwf, lane * 16, WS_MP + c * 2048 + 1024);
        v4u F0 = ldc4(rwf, lane * 16, WS_FP + c * 2048), F1 = ldc4(rwf, lane * 16, WS_FP + c * 2048 + 1024);
        SB();
        if (__any((bad4(M0, ta, tb) | (bad4(M1, ta, tb) & mlive)) != 0u)) {
#pragma nounroll
          for (unsigned spin = 0; spin < SPIN_CAP; ++spin) {
            FENCE();
            M0 = ldc4(rwf, lane * 16, WS_MP + c * 2048); M1 = ldc4(rwf, lane * 16, WS_MP + c * 2048 + 1024);
            if (!__any((bad4(M0, ta, tb) | (bad4(M1, ta, tb) & mlive)) != 0u)) break;
          }
        }
        const float m = rb(rb(red4(M0, M1 & mlive)) + blo(b2fin));
        float gt = rb(m * rcp(1.0f + ex2(-1.4426950408889634f * m))), xv = blo(xfin);
        asm volatile("" : "+v"(gt), "+v"(xv));
#pragma nounroll
        for (unsigned spin = 0; spin < SPIN_CAP; ++spin) {
          const bool m0 = __any(bad4(F0, ta, tb) != 0u), m1 = __any(bad4(F1, ta, tb) != 0u);
          if (!(m0 || m1)) break;
          FENCE();
          if (m0) F0 = ldc4(rwf, lane * 16, WS_FP + c * 2048);
          if (m1) F1 = ldc4(rwf, lane * 16, WS_FP + c * 2048 + 1024);
        }
        STAMP(25);
        const float o = xv + rb(rb(red4(F0, F1)) * gt);
        __builtin_amdgcn_raw_buffer_store_b16((unsigned short)pk(o, o), rout, (lane & 12) == 0 ? fo : OOB, c * 16, OAUX);
        __builtin_amdgcn_raw_buffer_store_b32(tag, rws, (c | lane) == 0 ? 0 : OOB, WS_E, EAUX);
        { unsigned d = 6 * (pt2 - pt1); if (d > 6000) d = 6000; const unsigned t1 = (unsigned)__builtin_amdgcn_s_memrealtime(); for (int it = 0; it < 100000; ++it) if ((unsigned)__builtin_amdgcn_s_memrealtime() - t1 >= d) break; }
      }
    }
  } else {
    __builtin_amdgcn_s_setprio(3);
    STAMP(0);
    const rsrc_t rq = mk((const void*)wq_early, WQB);
    const int r8 = lane >> 3, c8 = lane & 7;
    const int f0 = r8 & 2;
    const int vq0 = r8 * 8192 + (c8 ^ f0) * 16, vq1 = vq0 ^ 64;
    int so = ksq * 256 * 8192 + cgq * 128;
    unsigned m0v = (unsigned)(size_t)smem;
#pragma unroll
    for (int i = 0; i < WIN / 4; ++i) dma4<8 * 8192>(rq, vq0, vq1, vq0, vq1, so, m0v);
    SB();
    volatile LAS unsigned* fl = (volatile LAS unsigned*)(L + L_FL);
    const unsigned lds0 = (unsigned)(size_t)smem;
    Ld st = {WIN, 16, 0, lds0 + L_FL + F_FULL * 4};
    unsigned w4 = WIN - 4, to = 0x7fffffffu;
    __builtin_amdgcn_s_barrier();
    FENCE();
#pragma nounroll
    for (int i = WIN / 4; i < 8; ++i) { pace(st, w4, to); dma4<8 * 8192>(rq, vq0, vq1, vq0, vq1, so, m0v); st.cnt += 4; }
    to = TOMID;
    {
      const int v0 = r8 * 7168 + (c8 ^ f0) * 16;
      stream<8 * 7168, 2, 2>(L, st, mk(w1, has1 ? W1B : 0u), v0, v0 ^ 64, v0, v0 ^ 64, ks1 * 256 * 7168 + cg1 * 128, 0, lds0, w4, to);
    }
    const int kvb = (grp * 16384 + sp * 512) * 256;
    {
      const int v0 = g * 256 + (n16 ^ g) * 16;
      stream<1024, 4, 8>(L, st, mk(kc, KVB), v0, v0 ^ 64, v0 ^ 128, v0 ^ 192, kvb, sp, lds0, w4, to);
    }
    {
      const int v0 = g * 256 + (n16 ^ (2 * g)) * 16;
      stream<1024, 12, 8>(L, st, mk(vc, KVB), v0, v0 ^ 128, v0, v0 ^ 128, kvb, sp, lds0, w4, to);
    }
    {
      const int v0 = g * 2048 + (n16 ^ (2 * g)) * 16;
      stream<8192, 20, 2>(L, st, mk(w2, has2 ? W1B : 0u), v0, v0, v0 ^ 128, v0 ^ 128, ks2 * 128 * 2048 + cgo * 256, 0, lds0, w4, to);
      stream<8192, 22, 2>(L, st, mk(wo, WQB), v0, v0, v0 ^ 128, v0 ^ 128, ho * 128 * 2048 + cgo * 256, 0, lds0, w4, to);
    }
    pace(st, 0u, 0x7fffffffu);
    asm volatile("s_waitcnt vmcnt(0)" ::: "memory");
    fl[F_FULL] = 24;
  }
}

}

extern "C" void kernel_launch(void* const* d_in, const int* in_sizes, int n_in, void* d_out, int out_size, void* d_ws, size_t ws_size,
                              hipStream_t stream) {
  (void)in_sizes; (void)n_in; (void)out_size; (void)ws_size;
  fused_decode<<<256, NT, 0, stream>>>((const unsigned short*)d_in[0], d_in[1], d_in[2], d_in[3], d_in[4], d_in[6], d_in[7], d_in[8], d_in[9],
                                       d_out, d_ws);
}
```

```cpp
#include <hip/hip_runtime.h>
#include <hip/hip_bf16.h>

namespace {

typedef unsigned v4u __attribute__((ext_vector_type(4)));
typedef unsigned v2u __attribute__((ext_vector_type(2)));
typedef float f4 __attribute__((ext_vector_type(4)));
typedef float f2 __attribute__((ext_vector_type(2)));
typedef __bf16 bf8 __attribute__((ext_vector_type(8)));
typedef __bf16 bf2 __attribute__((ext_vector_type(2)));
typedef short s4 __attribute__((ext_vector_type(4)));
typedef __amdgpu_buffer_rsrc_t rsrc_t;
#define LAS __attribute__((address_space(3)))

constexpr int NWAVES = 10;
constexpr int NT = 64 * NWAVES;
constexpr unsigned KVB = 2u * 4 * 16384 * 128 * 2, WQB = 1024u * 4096 * 2, W1B = 1024u * 3584 * 2;
constexpr unsigned WS_E = 0;
constexpr unsigned WS_QP = 4096;
constexpr unsigned WS_HP = WS_QP + 131072;
constexpr unsigned WS_QF = WS_HP + 114688;
constexpr unsigned WS_HF = WS_QF + 32768;
constexpr unsigned WS_ST = WS_HF + 28672;
constexpr unsigned WS_OP = WS_ST + 32768;
constexpr unsigned WS_FP = WS_OP + 1048576;
constexpr unsigned WS_MP = WS_FP + 262144;
constexpr unsigned WS_END = WS_MP + 262144;
constexpr int L_P = 131072, L_XS = L_P, L_YS = L_P + 4096, L_QS = L_P + 16384, L_HS = L_QS + 8 * 272, L_OS = L_HS + 2 * 272,
              L_ST = L_OS + 4 * 272, L_WT = L_ST + 512, L_FL = L_WT + 256, L_SINK = L_FL + 128, L_STAMP = L_SINK + 512, L_END = L_STAMP + NWAVES * 256;
constexpr int LDS_BYTES = L_END;
static_assert(L_END <= 163840 && (L_HS % 16) == 0 && (L_OS % 16) == 0 && (L_ST % 16) == 0 && (L_WT % 16) == 0 && (L_FL % 16) == 0, "lds map");
enum { F_FULL = 0, F_LN = 1, F_Q = 2, F_P = 3, F_H = 4, F_REF = 5, F_TAG = 6, F_W = 7, F_D = 8, F_DQ = F_D, F_DH = F_D + 2, F_DK = F_D + 4, F_DV = F_D + 12, F_WORDS = 32 };
constexpr int OOB = 0x7ff00000;
#define AUXL 16
#define AUXS 16
constexpr int AUX_SC1 = 16;
constexpr unsigned SPIN_CAP = 1u << 15, LSPIN_CAP = 1u << 20;
constexpr float LOG2E = 1.4426950408889634f;
#define LSLP 2
#define QGATE 10
#define OGATE 23
#define PSLP 2
#define PF 14
#define WLO 16
#define WHI 36
#define TAU 56
#define TOFF 15
#define TTH 20
#define TOMID 330
#define TOLO 700
#define TAU2 46
#define PROP 0
#define TPROP 16
#define TOHI 200
#define S1GATE 5
#define SGATE 19
#define MPLATE 0
#define PFB 3
#define OPF 1
#define OAUX 16
#define EAUX 16
#define CTL 1
#define WIN 24

__device__ __forceinline__ rsrc_t mk(const void* p, unsigned bytes) { return __builtin_amdgcn_make_buffer_rsrc((void*)p, 0, bytes, 0x00020000); }
__device__ __forceinline__ v4u ldc4(rsrc_t r, int vo, int so) { return __builtin_amdgcn_raw_buffer_load_b128(r, vo, so, AUXL); }
__device__ __forceinline__ v2u ldc2(rsrc_t r, int vo, int so) { return __builtin_amdgcn_raw_buffer_load_b64(r, vo, so, AUXL); }
__device__ __forceinline__ void stc4(rsrc_t r, int vo, int so, v4u v) { __builtin_amdgcn_raw_buffer_store_b128(v, r, vo, so, AUXS); }
__device__ __forceinline__ float asf(unsigned u) { return __builtin_bit_cast(float, u); }
__device__ __forceinline__ unsigned asu(float f) { return __builtin_bit_cast(unsigned, f); }
__device__ __forceinline__ unsigned pk(float a, float b) { f2 v = {a, b}; return __builtin_bit_cast(unsigned, __builtin_convertvector(v, bf2)); }
__device__ __forceinline__ float blo(unsigned u) { return asf(u << 16); }
__device__ __forceinline__ float bhi(unsigned u) { return asf(u & 0xffff0000u); }
__device__ __forceinline__ float rb(float a) { return blo(pk(a, a)); }
__device__ __forceinline__ unsigned plo(unsigned a, unsigned b) { return __builtin_amdgcn_perm(b, a, 0x05040100u); }
__device__ __forceinline__ unsigned phi(unsigned a, unsigned b) { return __builtin_amdgcn_perm(b, a, 0x07060302u); }
__device__ __forceinline__ f4 mfma32(v4u a, v4u b, f4 c) { return __builtin_amdgcn_mfma_f32_16x16x32_bf16(__builtin_bit_cast(bf8, a), __builtin_bit_cast(bf8, b), c, 0, 0, 0); }
__device__ __forceinline__ f4 mfma16(v2u a, v2u b, f4 c) { return __builtin_amdgcn_mfma_f32_16x16x16bf16_1k(__builtin_bit_cast(s4, a), __builtin_bit_cast(s4, b), c, 0, 0, 0); }
template <int C> __device__ __forceinline__ float dpp(float x) { return asf(__builtin_amdgcn_mov_dpp(asu(x), C, 0xf, 0xf, true)); }
template <int C> __device__ __forceinline__ unsigned dppu(unsigned x) { return __builtin_amdgcn_mov_dpp(x, C, 0xf, 0xf, true); }
constexpr int XOR1 = 0xB1, XOR2 = 0x4E, ROR4 = 0x124, ROR8 = 0x128;
__device__ __forceinline__ float x16_sum(float x) { auto s = __builtin_amdgcn_permlane16_swap(asu(x), asu(x), false, false); return asf(s[0]) + asf(s[1]); }
__device__ __forceinline__ float x32_sum(float x) { auto s = __builtin_amdgcn_permlane32_swap(asu(x), asu(x), false, false); return asf(s[0]) + asf(s[1]); }
__device__ __forceinline__ float x16_max(float x) { auto s = __builtin_amdgcn_permlane16_swap(asu(x), asu(x), false, false); return fmaxf(asf(s[0]), asf(s[1])); }
__device__ __forceinline__ float x32_max(float x) { auto s = __builtin_amdgcn_permlane32_swap(asu(x), asu(x), false, false); return fmaxf(asf(s[0]), asf(s[1])); }
__device__ __forceinline__ float row_sum(float x) { x += dpp<XOR1>(x); x += dpp<XOR2>(x); x += dpp<ROR4>(x); x += dpp<ROR8>(x); return x; }
__device__ __forceinline__ float row_max(float x) { x = fmaxf(x, dpp<XOR1>(x)); x = fmaxf(x, dpp<XOR2>(x)); x = fmaxf(x, dpp<ROR4>(x)); x = fmaxf(x, dpp<ROR8>(x)); return x; }
__device__ __forceinline__ float wave_sum(float x) { return x32_sum(x16_sum(row_sum(x))); }
__device__ __forceinline__ float ex2(float x) { return __builtin_amdgcn_exp2f(x); }
__device__ __forceinline__ float rcp(float x) { return __builtin_amdgcn_rcpf(x); }
__device__ __forceinline__ unsigned tg(float v, unsigned t8) { return ((asu(v) + 0x80u) & 0xffffff00u) | t8; }
__device__ __forceinline__ v4u tg4(f4 v, unsigned ta, unsigned tb) { return v4u{tg(v.x, ta), tg(v.y, tb), tg(v.z, ta), tg(v.w, tb)}; }
__device__ __forceinline__ float untg(unsigned u) { return asf(u & 0xffffff00u); }
__device__ __forceinline__ unsigned xoror(unsigned a, unsigned b, unsigned c) { return __builtin_amdgcn_bitop3_b32(a, b, c, 0xBE); }
__device__ __forceinline__ unsigned bad4(v4u u, unsigned ta, unsigned tb) { return xoror(u.x, ta, xoror(u.y, tb, xoror(u.z, ta, u.w ^ tb))) & 0xffu; }
__device__ __forceinline__ float red4(v4u a, v4u b) {
  const auto s01 = __builtin_amdgcn_permlane32_swap(asu(untg(a.x) + untg(b.x)), asu(untg(a.y) + untg(b.y)), false, false);
  const auto s23 = __builtin_amdgcn_permlane32_swap(asu(untg(a.z) + untg(b.z)), asu(untg(a.w) + untg(b.w)), false, false);
  const auto t = __builtin_amdgcn_permlane16_swap(asu(asf(s01[0]) + asf(s01[1])), asu(asf(s23[0]) + asf(s23[1])), false, false);
  float v = asf(t[0]) + asf(t[1]);
  v += dpp<ROR4>(v); v += dpp<ROR8>(v);
  return v;
}
__device__ __forceinline__ v4u trfrag(const LAS unsigned char* a, int off0, int off1) {
  const v2u lo = __builtin_bit_cast(v2u, __builtin_amdgcn_ds_read_tr16_b64_v4i16((LAS s4*)(a + off0)));
  const v2u hi = __builtin_bit_cast(v2u, __builtin_amdgcn_ds_read_tr16_b64_v4i16((LAS s4*)(a + off1)));
  return v4u{lo.x, lo.y, hi.x, hi.y};
}

#define SB() __builtin_amdgcn_sched_barrier(0)
#define FENCE() do { SB(); asm volatile("" ::: "memory"); SB(); } while (0)
#define LGKM0() do { SB(); asm volatile("s_waitcnt lgkmcnt(0)" ::: "memory"); SB(); } while (0)
#define STAMP(i)

template <int SLEEP> __device__ __forceinline__ void lds_wait(LAS unsigned char* L, int word, unsigned want) {
  FENCE();
  const volatile LAS unsigned* p = (const volatile LAS unsigned*)(L + L_FL) + word;
#pragma nounroll
  for (unsigned i = 0; i < LSPIN_CAP; ++i) {
    if ((unsigned)__builtin_amdgcn_readfirstlane(*p) >= want) break;
    if (SLEEP) __builtin_amdgcn_s_sleep(SLEEP);
  }
  FENCE();
}
template <int WORD> __device__ __forceinline__ void lds_signal(unsigned vsig) {
  SB();
  asm volatile("s_waitcnt lgkmcnt(0)\n\tds_add_u32 %0, %1 offset:%2" :: "v"(vsig), "v"(1u), "i"(WORD * 4) : "memory");
  SB();
}
template <int GS> __device__ __forceinline__ void dma4(rsrc_t r, int v0, int v1, int v2, int v3, int& so, unsigned& m0v) {
  asm volatile("s_mov_b32 m0, %1\n\ts_nop 0\n\t"
               "buffer_load_dwordx4 %2, %6, %0 offen nt lds\n\ts_add_u32 %0, %0, %7\n\t"
               "buffer_load_dwordx4 %3, %6, %0 offen offset:1024 nt lds\n\ts_add_u32 %0, %0, %7\n\t"
               "buffer_load_dwordx4 %4, %6, %0 offen offset:2048 nt lds\n\ts_add_u32 %0, %0, %7\n\t"
               "buffer_load_dwordx4 %5, %6, %0 offen offset:3072 nt lds\n\ts_add_u32 %0, %0, %8\n\ts_add_u32 %1, %1, 0x1000"
               : "+s"(so), "+s"(m0v) : "v"(v0), "v"(v1), "v"(v2), "v"(v3), "s"(r), "i"(GS - 1024), "i"(GS + 3072) : "memory", "scc");
}
struct Ld { unsigned cnt, nxt, last, vfull; };
__device__ __forceinline__ void pace(Ld& s, unsigned w4, unsigned to) {
  unsigned a, b, v;
  asm volatile("0:\n\t"
               "s_memtime vcc\n\t"
               "s_getreg_b32 %0, hwreg(HW_REG_IB_STS)\n\t"
               "s_and_b32 %1, %0, 15\n\t"
               "s_bfe_u32 %0, %0, 0x20016\n\t"
               "s_lshl4_add_u32 %0, %0, %1\n\t"
               "s_sub_u32 %1, %5, %0\n\t"
               "s_cmp_lt_u32 %1, %3\n\t"
               "s_cbranch_scc1 1f\n\t"
               "s_lshr_b32 %1, %1, 4\n\t"
               "v_mov_b32 %2, %1\n\t"
               "ds_write_b32 %6, %2\n\t"
               "s_lshl_b32 %1, %1, 4\n\t"
               "s_add_u32 %3, %1, 16\n"
               "1:\n\t"
               "s_waitcnt lgkmcnt(0)\n\t"
               "s_cmp_le_u32 %0, %7\n\t"
               "s_cbranch_scc1 2f\n\t"
               "s_sub_u32 %1, vcc_lo, %4\n\t"
               "s_cmp_ge_u32 %1, %8\n\t"
               "s_cselect_b32 %1, %9, 0\n\t"
               "s_cmp_le_u32 %0, %1\n\t"
               "s_cbranch_scc0 0b\n"
               "2:\n\t"
               "s_mov_b32 %4, vcc_lo"
               : "=&s"(a), "=&s"(b), "=&v"(v), "+s"(s.nxt), "+s"(s.last)
               : "s"(s.cnt), "v"(s.vfull), "s"(w4), "s"(to), "i"(WHI - 4) : "memory", "scc", "vcc");
}
template <int GS, int F0, int NF>
__device__ __forceinline__ void stream(LAS unsigned char* L, Ld& s, rsrc_t r, int v0, int v1, int v2, int v3, int so, int rot, unsigned lds0, unsigned& w4, unsigned& to) {
  volatile LAS unsigned* fl = (volatile LAS unsigned*)(L + L_FL);
  const int base = so;
#pragma nounroll
  for (int f = F0; f < F0 + NF; ++f) {
    unsigned m0v = lds0 + (f & 7) * 16384;
    if (NF == 8) so = base + ((f - F0 + rot) & 7) * 16384;
    if (F0 + NF > 8 && f >= 8) {
      const unsigned now = (unsigned)__builtin_amdgcn_s_memrealtime(), ref = __builtin_amdgcn_readfirstlane(fl[F_REF]);
      const int lag = (CTL && ref) ? (int)(now - ref) - (F0 < 20 ? TOFF + (f - 5) * TAU : TOFF + 15 * TAU + (f - 20) * TAU2) : 0;
      w4 = lag > TTH ? WHI - 4 : lag < -TTH ? WLO - 4 : WIN - 4;
      to = lag < -TTH ? TOLO : TOMID;
      lds_wait<LSLP>(L, F_D + (f < 12 ? (f & ~1) : f) - 8, f < 12 ? 4u : f < 20 ? 1u : 8u);
    }
#pragma nounroll
    for (int i = 0; i < 4; ++i) {
      pace(s, w4, to);
      dma4<GS>(r, v0, v1, v2, v3, so, m0v);
      s.cnt += 4;
    }
  }
}

__global__ void __launch_bounds__(NT)
fused_decode(const unsigned short* __restrict__ x, const void* __restrict__ kc, const void* __restrict__ vc, const void* __restrict__ wq,
             const void* __restrict__ wo, const void* __restrict__ w1, const void* __restrict__ b1, const void* __restrict__ w2,
             const void* __restrict__ b2, void* __restrict__ out, void* ws) {
  unsigned long long wq_early;
  asm volatile("s_load_dwordx2 %0, %1, 0x18" : "=s"(wq_early) : "s"(__builtin_amdgcn_kernarg_segment_ptr()));
  SB();
  __shared__ __attribute__((aligned(16))) unsigned char smem[LDS_BYTES];
  LAS unsigned char* const L = (LAS unsigned char*)smem;
  const int tid = threadIdx.x, lane = tid & 63, wv = __builtin_amdgcn_readfirstlane(tid >> 6), c = blockIdx.x;
  const int g = lane >> 4, n16 = lane & 15;
  const int grp = c & 7, sp = c >> 3, ab = grp >> 2, ahkv = grp & 3;
  const int ksq = c >> 6, cgq = c & 63;
  const bool has1 = grp != (sp & 7);
  const int c1 = sp * 7 + grp - (grp > (sp & 7) ? 1 : 0);
  const int ks1 = c1 / 56, cg1 = c1 % 56;
  const int cgo = sp & 7;
  const int ho = grp * 4 + (sp >> 3);
  const int ks2 = (sp >> 3) * 8 + grp;
  const bool has2 = ks2 < 28;

  const unsigned vsig = (unsigned)(size_t)smem + (lane == 0 ? L_FL : L_SINK + lane * 4);
  SB();
  asm volatile("s_waitcnt lgkmcnt(0)" : "+s"(wq_early));
  SB();
  if (wv >= 1 && wv < NWAVES - 1) {
    const int w = wv - 1;
    STAMP(0);
    const rsrc_t rws = mk(ws, WS_END);
    const rsrc_t rws2 = mk(ws, has2 ? WS_END : 0u);
    const rsrc_t rxl = mk(x, w < 2 ? 4096u : 0u);
    const v4u xa = __builtin_amdgcn_raw_buffer_load_b128(rxl, lane * 16, w * 2048, 0);
    const v4u xb = __builtin_amdgcn_raw_buffer_load_b128(rxl, lane * 16, w * 2048 + 1024, 0);
    FENCE();
    __builtin_amdgcn_s_barrier();
    FENCE();
    if (w < 2) {
      float xf[16];
#pragma unroll
      for (int i = 0; i < 4; ++i) { xf[2 * i] = blo(xa[i]); xf[2 * i + 1] = bhi(xa[i]); xf[8 + 2 * i] = blo(xb[i]); xf[9 + 2 * i] = bhi(xb[i]); }
      const float piv = asf(__builtin_amdgcn_readfirstlane(asu(xf[0])));
      float s1 = 0.f, s2 = 0.f;
#pragma unroll
      for (int i = 0; i < 16; ++i) { const float d = xf[i] - piv; s1 += d; s2 = fmaf(d, d, s2); }
      float a1 = wave_sum(s1) * (1.0f / 1024), a2 = wave_sum(s2) * (1.0f / 1024);
      float mean = rb(piv + a1), inv = rcp(rb(__builtin_amdgcn_sqrtf(rb(rb(fmaxf(a2 - a1 * a1, 0.f)) + 1e-5f))));
      v4u pa, pb;
      s1 = 0.f; s2 = 0.f;
#pragma unroll
      for (int i = 0; i < 8; ++i) {
        const unsigned t = pk(xf[2 * i] - mean, xf[2 * i + 1] - mean), u = pk(blo(t) * inv, bhi(t) * inv);
        if (i < 4) pa[i] = u; else pb[i - 4] = u;
        xf[2 * i] = blo(u); xf[2 * i + 1] = bhi(u);
        s1 += xf[2 * i] + xf[2 * i + 1]; s2 = fmaf(xf[2 * i], xf[2 * i], fmaf(xf[2 * i + 1], xf[2 * i + 1], s2));
      }
      *(LAS v4u*)(L + L_XS + w * 2048 + lane * 16) = pa;
      *(LAS v4u*)(L + L_XS + w * 2048 + 1024 + lane * 16) = pb;
      a1 = wave_sum(s1) * (1.0f / 1024); a2 = wave_sum(s2) * (1.0f / 1024);
      mean = rb(a1); inv = rcp(rb(__builtin_amdgcn_sqrtf(rb(rb(fmaxf(a2 - a1 * a1, 0.f)) + 1e-5f))));
#pragma unroll
      for (int i = 0; i < 8; ++i) {
        const unsigned t = pk(xf[2 * i] - mean, xf[2 * i + 1] - mean), u = pk(blo(t) * inv, bhi(t) * inv);
        if (i < 4) pa[i] = u; else pb[i - 4] = u;
      }
      *(LAS v4u*)(L + L_YS + w * 2048 + lane * 16) = pa;
      *(LAS v4u*)(L + L_YS + w * 2048 + 1024 + lane * 16) = pb;
      lds_signal<F_LN>(vsig);
    }
    const int q4 = n16 >> 2, p4 = lane & 3;
    lds_wait<LSLP>(L, F_LN, 3);
    const unsigned tag = __builtin_amdgcn_readfirstlane(((volatile LAS unsigned*)(L + L_FL))[F_TAG]);
    const unsigned ta = tag & 0xffu, tb = (tag >> 8) & 0xffu;
    STAMP(1);
    {
      const bool isq = w < 4;
      const LAS unsigned char* ap = L + (isq ? L_XS + ksq * 512 : L_YS + (ks1 & 3) * 512) + (lane & 1) * 2048 + g * 16;
      v4u A[8];
#pragma unroll
      for (int s = 0; s < 8; ++s) A[s] = *(const LAS v4u*)(ap + s * 64);
      lds_wait<LSLP>(L, F_FULL, isq ? 2 : 4);
      const LAS unsigned char* bp = L + (isq ? 0 : 32768) + (8 * g + q4) * 128 + (((w & 3) ^ ((q4 >> 1) | ((g & 1) << 1))) * 32) + p4 * 8;
      v4u Bf[8];
#pragma unroll
      for (int s = 0; s < 8; ++s) Bf[s] = trfrag(bp, s * 4096, s * 4096 + 512);
      lds_signal<F_DQ>(vsig + (isq ? 0 : (F_DH - F_DQ) * 4));
      f4 d0 = mfma32(Bf[0], A[0], f4{0.f, 0.f, 0.f, 0.f}), d1 = mfma32(Bf[1], A[1], f4{0.f, 0.f, 0.f, 0.f});
#pragma unroll
      for (int s = 2; s < 8; s += 2) { d0 = mfma32(Bf[s], A[s], d0); d1 = mfma32(Bf[s + 1], A[s + 1], d1); }
      const rsrc_t rp = mk(ws, (isq || has1) ? WS_END : 0u);
      const int pso = (isq ? WS_QP + cgq * 2048 + ksq * 128 : WS_HP + cg1 * 2048 + ks1 * 128) + (w & 3) * 512;
      stc4(rp, n16 < 2 ? g * 32 + n16 * 16 : OOB, pso, tg4(d0 + d1, ta, tb));
    }
    STAMP(2);
    v4u K[16];
    {
      const LAS unsigned char* kp = L + ((4 + w) & 7) * 16384 + n16 * 256;
      lds_wait<1>(L, F_FULL, 5 + w);
#pragma unroll
      for (int s = 0; s < 4; ++s) {
        const LAS unsigned char* kq = kp + (((4 * s + g) ^ n16) * 16);
#pragma unroll
        for (int t = 0; t < 4; ++t) K[4 * t + s] = *(const LAS v4u*)(kq + t * 4096);
      }
      lds_signal<F_DK>(vsig + w * 4);
    }
    STAMP(3);
    lds_wait<1>(L, F_Q, 1);
    {
      const LAS unsigned char* qb = L + L_QS + (n16 >> 1) * 272 + g * 16;
      v4u qf[4];
#pragma unroll
      for (int s = 0; s < 4; ++s) qf[s] = *(const LAS v4u*)(qb + s * 64);
      f4 sc[4];
#pragma unroll
      for (int t = 0; t < 4; ++t) sc[t] = mfma32(K[4 * t], qf[0], f4{0.f, 0.f, 0.f, 0.f});
#pragma unroll
      for (int s = 1; s < 4; ++s)
#pragma unroll
        for (int t = 0; t < 4; ++t) sc[t] = mfma32(K[4 * t + s], qf[s], sc[t]);
#pragma unroll
      for (int t = 0; t < 4; ++t)
#pragma unroll
        for (int r = 0; r < 4; r += 2) {
          const unsigned u = pk(sc[t][r], sc[t][r + 1]), v = pk(blo(u) * 0.088388347648318440f, bhi(u) * 0.088388347648318440f);
          sc[t][r] = blo(v); sc[t][r + 1] = bhi(v);
        }
      float mw = fmaxf(fmaxf(sc[0].x, sc[0].y), fmaxf(sc[0].z, sc[0].w));
#pragma unroll
      for (int t = 1; t < 4; ++t) mw = fmaxf(mw, fmaxf(fmaxf(sc[t].x, sc[t].y), fmaxf(sc[t].z, sc[t].w)));
      mw = x32_max(x16_max(mw)) * LOG2E;
      float lw = 0.f;
      const unsigned pm = (lane & 1) ? 0xffff0000u : 0u;
      unsigned P[8];
#pragma unroll
      for (int t = 0; t < 4; ++t)
#pragma unroll
        for (int r = 0; r < 4; r += 2) {
          const float v0 = ex2(fmaf(sc[t][r], LOG2E, -mw)), v1 = ex2(fmaf(sc[t][r + 1], LOG2E, -mw));
          lw += v0 + v1;
          const unsigned h = pk(v0, v1);
          P[2 * t + (r >> 1)] = pk(v0 - asf((h << 16) & pm), v1 - asf(h & pm));
        }
      *(LAS v4u*)(L + L_P + w * 2048 + lane * 16) = v4u{P[0], P[1], P[2], P[3]};
      *(LAS v4u*)(L + L_P + w * 2048 + 1024 + lane * 16) = v4u{P[4], P[5], P[6], P[7]};
      lw = x32_sum(x16_sum(lw));
      if (lane < 16 && !(lane & 1)) *(LAS f2*)(L + L_ST + (w * 8 + (lane >> 1)) * 8) = f2{mw, lw};
      lds_signal<F_P>(vsig);
    }
    STAMP(4);
    lds_wait<1>(L, F_P, 8);
    float o0 = 0.f, o1 = 0.f;
    {
      f4 st[8];
#pragma unroll
      for (int j = 0; j < 8; ++j) st[j] = *(const LAS f4*)(L + L_ST + (j * 8 + 2 * g) * 8);
      float M0 = st[0].x, M1 = st[0].z;
#pragma unroll
      for (int j = 1; j < 8; ++j) { M0 = fmaxf(M0, st[j].x); M1 = fmaxf(M1, st[j].z); }
      M0 = __builtin_ceilf(M0); M1 = __builtin_ceilf(M1);
#pragma unroll
      for (int j = 0; j < 8; ++j) { st[j].x = ex2(st[j].x - M0); st[j].z = ex2(st[j].z - M1); }
      STAMP(5);
      int ovs = (lane & 2) ? OOB : (2 * g + (lane & 1)) * 32768 + (n16 >> 2) * 16;
      asm volatile("" : "+v"(ovs));
      const LAS unsigned char* vp = L + (4 * g + q4) * 256 + ((w ^ (4 * (g & 1) + q4)) * 32) + p4 * 8;
#pragma unroll
      for (int j = 0; j < 8; ++j) {
        const v4u pa = *(const LAS v4u*)(L + L_P + j * 2048 + lane * 16), pb = *(const LAS v4u*)(L + L_P + j * 2048 + 1024 + lane * 16);
        if (j < 7) lds_wait<LSLP>(L, F_FULL, 13 + j); else lds_wait<0>(L, F_FULL, 20);
        const LAS unsigned char* vq = vp + ((4 + j) & 7) * 16384;
        const v4u va = trfrag(vq, 0, 4096), vb = trfrag(vq, 8192, 12288);
        if (j == 0) lds_signal<F_DV>(vsig); else if (j == 1) lds_signal<F_DV + 1>(vsig); else if (j == 2) lds_signal<F_DV + 2>(vsig); else if (j == 3) lds_signal<F_DV + 3>(vsig);
        f4 d = mfma32(pa, va, f4{0.f, 0.f, 0.f, 0.f});
        d = mfma32(pb, vb, d);
        o0 = fmaf(st[j].x, d.x + d.y, o0);
        o1 = fmaf(st[j].z, d.z + d.w, o1);
      }
      const bool odd = (lane & 1) != 0;
      const float rcv = dpp<XOR1>(odd ? o0 : o1);
      const unsigned gr = odd ? pk(rcv, o1) : pk(o0, rcv);
      const unsigned gn = dppu<XOR2>(gr);
      stc4(rws, ovs, WS_OP + ((ahkv * 64 + w) * 32 + sp) * 128 + ab * 64, v4u{gr, tag, gn, tag});
    }
    STAMP(6);
    const LAS unsigned char* wp = L + (8 * g + q4) * 256 + ((w ^ (q4 | ((g & 1) << 2))) * 32) + p4 * 8;
    const int fvo = (g >> 1) * 2048 + (g & 1) * 32;
    v4u MPv;
    {
      v4u A[4];
      lds_wait<1>(L, F_H, 1);
#pragma unroll
      for (int s = 0; s < 4; ++s) A[s] = *(const LAS v4u*)(L + L_HS + (lane & 1) * 272 + s * 64 + g * 16);
      lds_wait<LSLP>(L, F_FULL, 22);
      f4 d = {0.f, 0.f, 0.f, 0.f};
#pragma unroll
      for (int s = 0; s < 4; ++s) d = mfma32(trfrag(wp + 4 * 16384, s * 8192, s * 8192 + 1024), A[s], d);
      MPv = tg4(d, ta, tb);
      if (!MPLATE) stc4(rws2, n16 < 2 ? fvo + n16 * 16 : OOB, WS_MP + ((cgo * 16 + 2 * w) * 32 + ks2) * 64, MPv);
    }
    STAMP(7);
    {
      const int ovo = ((n16 >> 2) & 1) * 2048 + g * 512 + (n16 >> 3) * 64 + (lane & 3) * 16, oso = WS_OP + (ho * 8 + w) * 4096;
      v4u G[4];
      lds_wait<0>(L, F_FULL, OGATE);
#pragma unroll
      for (int j = 0; j < 4; ++j) G[j] = ldc4(rws, ovo + j * 128, oso);
      SB();
      if (MPLATE) stc4(rws2, n16 < 2 ? fvo + n16 * 16 : OOB, WS_MP + ((cgo * 16 + 2 * w) * 32 + ks2) * 64, MPv);
      SB();
      lds_wait<0>(L, F_W, 1);
      v2u WF = *(const LAS v2u*)(L + L_WT + (lane & 3) * 32 + g * 8);
      float invl = ((const LAS float*)(L + L_WT + 128))[(lane >> 1) & 1];
      LAS unsigned char* osw = L + (n16 == g ? L_OS + g * 272 + w * 32 : L_P + w * 2048 + lane * 32);
      const LAS unsigned char* osr = L + L_OS + (lane & 3) * 272 + g * 16;
      const LAS unsigned char* wop = wp + 6 * 16384;
      int fv2 = (n16 & 13) == 0 ? fvo + (n16 >> 1) * 16 : OOB, fso = WS_FP + ((cgo * 16 + 2 * w) * 32 + ho) * 64;
      unsigned vta = ta, vtb = tb, vtag = tag;
      asm volatile("" : "+v"(WF), "+v"(invl), "+v"(osw), "+v"(osr), "+v"(wop), "+v"(fv2), "+s"(fso), "+v"(vta), "+v"(vtb), "+v"(vtag));
      lds_wait<0>(L, F_FULL, 24);
      v4u Bo[4];
#pragma unroll
      for (int s = 0; s < 4; ++s) Bo[s] = trfrag(wop, s * 8192, s * 8192 + 1024);
      SB();
      STAMP(8);
      const f4 z = {0.f, 0.f, 0.f, 0.f};
      f4 e0, e1, e2, e3;
#define COMBINE() do { \
        e0 = mfma16(v2u{plo(G[0].x, G[1].x), plo(G[2].x, G[3].x)}, WF, z); e1 = mfma16(v2u{phi(G[0].x, G[1].x), phi(G[2].x, G[3].x)}, WF, z); \
        e2 = mfma16(v2u{plo(G[0].z, G[1].z), plo(G[2].z, G[3].z)}, WF, z); e3 = mfma16(v2u{phi(G[0].z, G[1].z), phi(G[2].z, G[3].z)}, WF, z); } while (0)
#define OBAD(j, acc) xoror(G[j].y, vtag, xoror(G[j].w, vtag, acc))
      COMBINE();
      SB();
      if (__builtin_expect(__any((OBAD(0, OBAD(1, 0u)) | OBAD(2, OBAD(3, 0u))) != 0u), 0)) {
#pragma nounroll
        for (unsigned spin = 0; spin < SPIN_CAP; ++spin) {
          const bool m0 = __any(OBAD(0, 0u) != 0u), m1 = __any(OBAD(1, 0u) != 0u), m2 = __any(OBAD(2, 0u) != 0u), m3 = __any(OBAD(3, 0u) != 0u);
          if (!(m0 || m1 || m2 || m3)) break;
          FENCE();
          if (m0) G[0] = ldc4(rws, ovo, oso);
          if (m1) G[1] = ldc4(rws, ovo + 128, oso);
          if (m2) G[2] = ldc4(rws, ovo + 256, oso);
          if (m3) G[3] = ldc4(rws, ovo + 384, oso);
        }
        COMBINE();
      }
      STAMP(9);
      *(LAS v4u*)osw = v4u{pk(e0.x, e1.x), pk(e2.x, e3.x), pk(e0.y, e1.y), pk(e2.y, e3.y)};
      *(LAS v4u*)(osw + 16) = v4u{pk(e0.z, e1.z), pk(e2.z, e3.z), pk(e0.w, e1.w), pk(e2.w, e3.w)};
      LGKM0();
      __builtin_amdgcn_s_barrier();
      FENCE();
      v4u a0 = *(const LAS v4u*)osr, a1 = *(const LAS v4u*)(osr + 64), a2 = *(const LAS v4u*)(osr + 128), a3 = *(const LAS v4u*)(osr + 192);
      SB();
      f4 d = mfma32(Bo[0], a0, z), d2 = mfma32(Bo[1], a1, z);
      d = mfma32(Bo[2], a2, d); d2 = mfma32(Bo[3], a3, d2);
      d += d2;
      d.x += dpp<XOR1>(d.x); d.y += dpp<XOR1>(d.y); d.z += dpp<XOR1>(d.z); d.w += dpp<XOR1>(d.w);
      d *= invl;
      stc4(rws, fv2, fso, v4u{(asu(d.x) & 0xffffff00u) | vta, (asu(d.y) & 0xffffff00u) | vtb, (asu(d.z) & 0xffffff00u) | vta, (asu(d.w) & 0xffffff00u) | vtb});
    }
    STAMP(10);
  } else if (wv == NWAVES - 1) {
    STAMP(0);
    volatile LAS unsigned* fl = (volatile LAS unsigned*)(L + L_FL);
    const rsrc_t rws = mk(ws, WS_END), rws1 = mk(ws, has1 ? WS_END : 0u), rws2 = mk(ws, has2 ? WS_END : 0u), rout = mk(out, 4096);
    const unsigned eraw = __builtin_amdgcn_raw_buffer_load_b32(rws, 0, WS_E, AUX_SC1);
    const unsigned b1p = __builtin_amdgcn_raw_buffer_load_b32(mk(b1, has1 ? 7168u : 0u), (n16 >> 2) * 8 + (lane & 1) * 4, c1 * 32, 0);
    const int col2 = ((lane >> 1) & 1) * 8 + ((g >> 1) | ((g & 1) << 1)) * 2;
    const int fo = (lane & 1) * 2048 + col2;
    const unsigned short xfin = __builtin_amdgcn_raw_buffer_load_b16(mk(x, c < 128 ? 4096u : 0u), fo, c * 16, 0);
    const unsigned short b2fin = __builtin_amdgcn_raw_buffer_load_b16(mk(b2, c < 128 ? 2048u : 0u), col2, c * 16, 0);
    const v4u codepf = __builtin_amdgcn_raw_buffer_load_b128(mk((const void*)((__builtin_amdgcn_s_getpc() & ~255ull) - PFB * 1024u), PF * 1024u), lane * 16, sp * 1024, 0);
    SB();
    if (lane < F_WORDS) fl[lane] = 0u;
    LGKM0();
    __builtin_amdgcn_s_barrier();
    FENCE();
    const unsigned tag = __builtin_amdgcn_readfirstlane(eraw) * 0x9E3779B1u + 0x7F4A7C15u;
    const unsigned ta = tag & 0xffu, tb = (tag >> 8) & 0xffu;
    fl[F_TAG] = tag;
    lds_signal<F_LN>(vsig);
    asm volatile("" :: "v"(codepf));
    lds_wait<1>(L, F_FULL, 4);
    const unsigned t4 = (unsigned)__builtin_amdgcn_s_memrealtime();
    lds_wait<LSLP>(L, F_FULL, S1GATE);
    {
      const int fvo = (lane < 16 && !(lane & 1)) ? (lane >> 2) * 16 : OOB, fb = (lane >> 1) & 1;
      bool needq = true, needh = has1;
#pragma nounroll
      for (unsigned spin = 0; (needq || needh) && spin < SPIN_CAP; ++spin) {
        const v2u GQ = ldc2(rws, needq ? lane * 8 : OOB, WS_QP + c * 512), GH = ldc2(rws1, needh ? lane * 8 : OOB, WS_HP + c1 * 512);
        if (needq && !__any((((GQ.x ^ ta) | (GQ.y ^ tb)) & 0xffu) != 0u)) {
          const unsigned gr = pk(x32_sum(x16_sum(untg(GQ.x))), x32_sum(x16_sum(untg(GQ.y)))), gn = dppu<XOR1>(gr), ty = (tag << 12) | (t4 & 0xfffu);
          stc4(rws, fvo + fb * 16384, WS_QF + c * 64, v4u{gr, ty, gn, ty});
          needq = false;
          STAMP(13);
        }
        if (needh && !__any((((GH.x ^ ta) | (GH.y ^ tb)) & 0xffu) != 0u)) {
          float h0 = rb(rb(x32_sum(x16_sum(untg(GH.x)))) + blo(b1p)), h1 = rb(rb(x32_sum(x16_sum(untg(GH.y)))) + bhi(b1p));
          constexpr float GC = -2.0f * 0.7978845608028654f * 1.4426950408889634f;
          h0 = h0 * rcp(1.0f + ex2(GC * (h0 + 0.044715f * h0 * h0 * h0)));
          h1 = h1 * rcp(1.0f + ex2(GC * (h1 + 0.044715f * h1 * h1 * h1)));
          const unsigned gr = pk(h0, h1), gn = dppu<XOR1>(gr);
          stc4(rws, fvo + fb * 14336, WS_HF + c1 * 64, v4u{gr, tag, gn, tag});
          needh = false;
          STAMP(14);
        }
        __builtin_amdgcn_s_sleep(PSLP);
      }
    }
    lds_wait<LSLP>(L, F_FULL, QGATE);
    {
      v4u Q[4];
      const unsigned t20 = tag & 0xfffffu;
#pragma nounroll
      for (unsigned spin = 0; spin < SPIN_CAP; ++spin) {
        unsigned bad = 0;
#pragma unroll
        for (int i = 0; i < 4; ++i) Q[i] = ldc4(rws, lane * 16, WS_QF + grp * 4096 + i * 1024);
#pragma unroll
        for (int i = 0; i < 4; ++i) bad |= ((Q[i].y >> 12) ^ t20) | ((Q[i].w >> 12) ^ t20);
        if (!__any(bad != 0u)) break;
        __builtin_amdgcn_s_sleep(PSLP);
      }
#pragma unroll
      for (int i = 0; i < 4; ++i) *(LAS v2u*)(L + L_QS + (2 * i + (lane >> 5)) * 272 + (lane & 31) * 8) = v2u{Q[i].x, Q[i].z};
      lds_signal<F_Q>(vsig);
      STAMP(15);
      if (CTL) {
        int d = 0;
#pragma unroll
        for (int i = 0; i < 4; ++i) d += (int)((Q[i].y - t4) << 20) >> 20;
        fl[F_REF] = (t4 + (int)(wave_sum((float)d) * (1.0f / 256))) | 1u;
      }
    }
    lds_wait<1>(L, F_P, 8);
    {
      f2 sv[8];
#pragma unroll
      for (int j = 0; j < 8; ++j) sv[j] = *(const LAS f2*)(L + L_ST + (j * 8 + (lane & 7)) * 8);
      float M = sv[0].x, Ls = 0.f;
#pragma unroll
      for (int j = 1; j < 8; ++j) M = fmaxf(M, sv[j].x);
      M = __builtin_ceilf(M);
#pragma unroll
      for (int j = 0; j < 8; ++j) Ls = fmaf(ex2(sv[j].x - M), sv[j].y, Ls);
      stc4(rws, lane < 8 ? (lane >> 2) * 2048 + (lane & 3) * 16 : OOB, WS_ST + grp * 4096 + sp * 64, v4u{asu(M), tag, asu(Ls), tag});
      const unsigned tag2 = has2 ? tag : 0u;
      v4u H;
#pragma nounroll
      for (unsigned spin = 0; spin < SPIN_CAP; ++spin) {
        H = ldc4(rws2, (lane >> 5) * 14336 + (lane & 31) * 16, WS_HF + ks2 * 512);
        if (!__any(((H.y ^ tag2) | (H.w ^ tag2)) != 0u)) break;
        __builtin_amdgcn_s_sleep(PSLP);
      }
      *(LAS v2u*)(L + L_HS + (lane >> 5) * 272 + (lane & 31) * 8) = v2u{H.x, H.z};
      lds_signal<F_H>(vsig);
      STAMP(16);
    }
    lds_wait<LSLP>(L, F_FULL, SGATE);
    {
      v4u SG;
#pragma nounroll
      for (unsigned spin = 0; spin < SPIN_CAP; ++spin) {
        SG = ldc4(rws, (lane >> 5) * 16384 + (lane & 31) * 64, WS_ST + (ho >> 2) * 2048 + (ho & 3) * 16);
        if (!__any(((SG.y ^ tag) | (SG.w ^ tag)) != 0u)) break;
        __builtin_amdgcn_s_sleep(PSLP);
      }
      const float m = asf(SG.x), M = x16_max(row_max(m));
      const float e = rb(ex2(m - M)), Ls = x16_sum(row_sum(e * asf(SG.z)));
      ((LAS unsigned short*)(L + L_WT))[lane] = (unsigned short)(asu(e) >> 16);
      ((LAS float*)(L + L_WT + 128))[lane >> 5] = rcp(Ls) * (1.0f + 1.0f / 131072);
      lds_signal<F_W>(vsig);
      STAMP(17);
    }
    {
      const rsrc_t rwf = mk(ws, c < 128 ? WS_END : 0u);
      const unsigned mlive = lane < 48 ? 0xffffffffu : 0u;
      __builtin_amdgcn_s_barrier();
      FENCE();
      if (c < 128) {
        v4u M0 = ldc4(rwf, lane * 16, WS_MP + c * 2048), M1 = ldc4(rwf, lane * 16, WS_MP + c * 2048 + 1024);
        v4u F0 = ldc4(rwf, lane * 16, WS_FP + c * 2048), F1 = ldc4(rwf, lane * 16, WS_FP + c * 2048 + 1024);
        SB();
        if (__any((bad4(M0, ta, tb) | (bad4(M1, ta, tb) & mlive)) != 0u)) {
#pragma nounroll
          for (unsigned spin = 0; spin < SPIN_CAP; ++spin) {
            FENCE();
            M0 = ldc4(rwf, lane * 16, WS_MP + c * 2048); M1 = ldc4(rwf, lane * 16, WS_MP + c * 2048 + 1024);
            if (!__any((bad4(M0, ta, tb) | (bad4(M1, ta, tb) & mlive)) != 0u)) break;
          }
        }
        const float m = rb(rb(red4(M0, M1 & mlive)) + blo(b2fin));
        float gt = rb(m * rcp(1.0f + ex2(-1.4426950408889634f * m))), xv = blo(xfin);
        asm volatile("" : "+v"(gt), "+v"(xv));
#pragma nounroll
        for (unsigned spin = 0; spin < SPIN_CAP; ++spin) {
          const bool m0 = __any(bad4(F0, ta, tb) != 0u), m1 = __any(bad4(F1, ta, tb) != 0u);
          if (!(m0 || m1)) break;
          FENCE();
          if (m0) F0 = ldc4(rwf, lane * 16, WS_FP + c * 2048);
          if (m1) F1 = ldc4(rwf, lane * 16, WS_FP + c * 2048 + 1024);
        }
        STAMP(25);
        const float o = xv + rb(rb(red4(F0, F1)) * gt);
        __builtin_amdgcn_raw_buffer_store_b16((unsigned short)pk(o, o), rout, (lane & 12) == 0 ? fo : OOB, c * 16, OAUX);
        __builtin_amdgcn_raw_buffer_store_b32(tag, rws, (c | lane) == 0 ? 0 : OOB, WS_E, EAUX);
        STAMP(26);
      }
    }
  } else {
    __builtin_amdgcn_s_setprio(3);
    STAMP(0);
    const rsrc_t rq = mk((const void*)wq_early, WQB);
    const int r8 = lane >> 3, c8 = lane & 7;
    const int f0 = r8 & 2;
    const int vq0 = r8 * 8192 + (c8 ^ f0) * 16, vq1 = vq0 ^ 64;
    int so = ksq * 256 * 8192 + cgq * 128;
    unsigned m0v = (unsigned)(size_t)smem;
#pragma unroll
    for (int i = 0; i < WIN / 4; ++i) dma4<8 * 8192>(rq, vq0, vq1, vq0, vq1, so, m0v);
    SB();
    volatile LAS unsigned* fl = (volatile LAS unsigned*)(L + L_FL);
    const unsigned lds0 = (unsigned)(size_t)smem;
    Ld st = {WIN, 16, 0, lds0 + L_FL + F_FULL * 4};
    unsigned w4 = WIN - 4, to = 0x7fffffffu;
    __builtin_amdgcn_s_barrier();
    FENCE();
#pragma nounroll
    for (int i = WIN / 4; i < 8; ++i) { pace(st, w4, to); dma4<8 * 8192>(rq, vq0, vq1, vq0, vq1, so, m0v); st.cnt += 4; }
    to = TOMID;
    {
      const int v0 = r8 * 7168 + (c8 ^ f0) * 16;
      stream<8 * 7168, 2, 2>(L, st, mk(w1, has1 ? W1B : 0u), v0, v0 ^ 64, v0, v0 ^ 64, ks1 * 256 * 7168 + cg1 * 128, 0, lds0, w4, to);
    }
    const int kvb = (grp * 16384 + sp * 512) * 256;
    {
      const int v0 = g * 256 + (n16 ^ g) * 16;
      stream<1024, 4, 8>(L, st, mk(kc, KVB), v0, v0 ^ 64, v0 ^ 128, v0 ^ 192, kvb, sp, lds0, w4, to);
    }
    {
      const int v0 = g * 256 + (n16 ^ (2 * g)) * 16;
      stream<1024, 12, 8>(L, st, mk(vc, KVB), v0, v0 ^ 128, v0, v0 ^ 128, kvb, sp, lds0, w4, to);
    }
    {
      const int v0 = g * 2048 + (n16 ^ (2 * g)) * 16;
      stream<8192, 20, 2>(L, st, mk(w2, has2 ? W1B : 0u), v0, v0, v0 ^ 128, v0 ^ 128, ks2 * 128 * 2048 + cgo * 256, 0, lds0, w4, to);
      stream<8192, 22, 2>(L, st, mk(wo, WQB), v0, v0, v0 ^ 128, v0 ^ 128, ho * 128 * 2048 + cgo * 256, 0, lds0, w4, to);
    }
    pace(st, 0u, 0x7fffffffu);
    asm volatile("s_waitcnt vmcnt(0)" ::: "memory");
    fl[F_FULL] = 24;
  }
}

}

extern "C" void kernel_launch(void* const* d_in, const int* in_sizes, int n_in, void* d_out, int out_size, void* d_ws, size_t ws_size,
                              hipStream_t stream) {
  (void)in_sizes; (void)n_in; (void)out_size; (void)ws_size;
  fused_decode<<<256, NT, 0, stream>>>((const unsigned short*)d_in[0], d_in[1], d_in[2], d_in[3], d_in[4], d_in[6], d_in[7], d_in[8], d_in[9],
                                       d_out, d_ws);
}
```

```cpp
#include <hip/hip_runtime.h>
#include <hip/hip_bf16.h>

namespace {

typedef unsigned v4u __attribute__((ext_vector_type(4)));
typedef unsigned v2u __attribute__((ext_vector_type(2)));
typedef float f4 __attribute__((ext_vector_type(4)));
typedef float f2 __attribute__((ext_vector_type(2)));
typedef __bf16 bf8 __attribute__((ext_vector_type(8)));
typedef __bf16 bf2 __attribute__((ext_vector_type(2)));
typedef short s4 __attribute__((ext_vector_type(4)));
typedef __amdgpu_buffer_rsrc_t rsrc_t;
#define LAS __attribute__((address_space(3)))

constexpr int NWAVES = 10;
constexpr int NT = 64 * NWAVES;
constexpr unsigned KVB = 2u * 4 * 16384 * 128 * 2, WQB = 1024u * 4096 * 2, W1B = 1024u * 3584 * 2;
constexpr unsigned WS_E = 0;
constexpr unsigned WS_QP = 4096;
constexpr unsigned WS_HP = WS_QP + 131072;
constexpr unsigned WS_QF = WS_HP + 114688;
constexpr unsigned WS_HF = WS_QF + 32768;
constexpr unsigned WS_ST = WS_HF + 28672;
constexpr unsigned WS_OP = WS_ST + 32768;
constexpr unsigned WS_FP = WS_OP + 1048576;
constexpr unsigned WS_MP = WS_FP + 262144;
constexpr unsigned WS_END = WS_MP + 262144;
constexpr int L_P = 131072, L_XS = L_P, L_YS = L_P + 4096, L_QS = L_P + 16384, L_HS = L_QS + 8 * 272, L_OS = L_HS + 2 * 272,
              L_ST = L_OS + 4 * 272, L_WT = L_ST + 512, L_FL = L_WT + 256, L_SINK = L_FL + 128, L_STAMP = L_SINK + 512, L_END = L_STAMP + NWAVES * 256;
constexpr int LDS_BYTES = L_END;
static_assert(L_END <= 163840 && (L_HS % 16) == 0 && (L_OS % 16) == 0 && (L_ST % 16) == 0 && (L_WT % 16) == 0 && (L_FL % 16) == 0, "lds map");
enum { F_FULL = 0, F_LN = 1, F_Q = 2, F_P = 3, F_H = 4, F_REF = 5, F_TAG = 6, F_W = 7, F_D = 8, F_DQ = F_D, F_DH = F_D + 2, F_DK = F_D + 4, F_DV = F_D + 12, F_WORDS = 32 };
constexpr int OOB = 0x7ff00000;
#define AUXL 16
#define AUXS 16
constexpr int AUX_SC1 = 16;
constexpr unsigned SPIN_CAP = 1u << 15, LSPIN_CAP = 1u << 20;
constexpr float LOG2E = 1.4426950408889634f;
#define LSLP 2
#define QGATE 10
#define OGATE 23
#define PSLP 2
#define PF 14
#define WLO 16
#define WHI 36
#define TAU 56
#define TOFF 15
#define TTH 20
#define TOMID 330
#define TOLO 700
#define TAU2 46
#define PROP 0
#define TPROP 16
#define TOHI 200
#define S1GATE 5
#define SGATE 19
#define MPLATE 0
#define PFB 3
#define OPF 1
#define OAUX 16
#define EAUX 16
#define FLATE 1
#define FSLP0 0
#define FSLP 0
#define CTL 1
#define WIN 24

__device__ __forceinline__ rsrc_t mk(const void* p, unsigned bytes) { return __builtin_amdgcn_make_buffer_rsrc((void*)p, 0, bytes, 0x00020000); }
__device__ __forceinline__ v4u ldc4(rsrc_t r, int vo, int so) { return __builtin_amdgcn_raw_buffer_load_b128(r, vo, so, AUXL); }
__device__ __forceinline__ v2u ldc2(rsrc_t r, int vo, int so) { return __builtin_amdgcn_raw_buffer_load_b64(r, vo, so, AUXL); }
__device__ __forceinline__ void stc4(rsrc_t r, int vo, int so, v4u v) { __builtin_amdgcn_raw_buffer_store_b128(v, r, vo, so, AUXS); }
__device__ __forceinline__ float asf(unsigned u) { return __builtin_bit_cast(float, u); }
__device__ __forceinline__ unsigned asu(float f) { return __builtin_bit_cast(unsigned, f); }
__device__ __forceinline__ unsigned pk(float a, float b) { f2 v = {a, b}; return __builtin_bit_cast(unsigned, __builtin_convertvector(v, bf2)); }
__device__ __forceinline__ float blo(unsigned u) { return asf(u << 16); }
__device__ __forceinline__ float bhi(unsigned u) { return asf(u & 0xffff0000u); }
__device__ __forceinline__ float rb(float a) { return blo(pk(a, a)); }
__device__ __forceinline__ unsigned plo(unsigned a, unsigned b) { return __builtin_amdgcn_perm(b, a, 0x05040100u); }
__device__ __forceinline__ unsigned phi(unsigned a, unsigned b) { return __builtin_amdgcn_perm(b, a, 0x07060302u); }
__device__ __forceinline__ f4 mfma32(v4u a, v4u b, f4 c) { return __builtin_amdgcn_mfma_f32_16x16x32_bf16(__builtin_bit_cast(bf8, a), __builtin_bit_cast(bf8, b), c, 0, 0, 0); }
__device__ __forceinline__ f4 mfma16(v2u a, v2u b, f4 c) { return __builtin_amdgcn_mfma_f32_16x16x16bf16_1k(__builtin_bit_cast(s4, a), __builtin_bit_cast(s4, b), c, 0, 0, 0); }
template <int C> __device__ __forceinline__ float dpp(float x) { return asf(__builtin_amdgcn_mov_dpp(asu(x), C, 0xf, 0xf, true)); }
template <int C> __device__ __forceinline__ unsigned dppu(unsigned x) { return __builtin_amdgcn_mov_dpp(x, C, 0xf, 0xf, true); }
constexpr int XOR1 = 0xB1, XOR2 = 0x4E, ROR4 = 0x124, ROR8 = 0x128;
__device__ __forceinline__ float x16_sum(float x) { auto s = __builtin_amdgcn_permlane16_swap(asu(x), asu(x), false, false); return asf(s[0]) + asf(s[1]); }
__device__ __forceinline__ float x32_sum(float x) { auto s = __builtin_amdgcn_permlane32_swap(asu(x), asu(x), false, false); return asf(s[0]) + asf(s[1]); }
__device__ __forceinline__ float x16_max(float x) { auto s = __builtin_amdgcn_permlane16_swap(asu(x), asu(x), false, false); return fmaxf(asf(s[0]), asf(s[1])); }
__device__ __forceinline__ float x32_max(float x) { auto s = __builtin_amdgcn_permlane32_swap(asu(x), asu(x), false, false); return fmaxf(asf(s[0]), asf(s[1])); }
__device__ __forceinline__ float row_sum(float x) { x += dpp<XOR1>(x); x += dpp<XOR2>(x); x += dpp<ROR4>(x); x += dpp<ROR8>(x); return x; }
__device__ __forceinline__ float row_max(float x) { x = fmaxf(x, dpp<XOR1>(x)); x = fmaxf(x, dpp<XOR2>(x)); x = fmaxf(x, dpp<ROR4>(x)); x = fmaxf(x, dpp<ROR8>(x)); return x; }
__device__ __forceinline__ float wave_sum(float x) { return x32_sum(x16_sum(row_sum(x))); }
__device__ __forceinline__ float ex2(float x) { return __builtin_amdgcn_exp2f(x); }
__device__ __forceinline__ float rcp(float x) { return __builtin_amdgcn_rcpf(x); }
__device__ __forceinline__ unsigned tg(float v, unsigned t8) { return ((asu(v) + 0x80u) & 0xffffff00u) | t8; }
__device__ __forceinline__ v4u tg4(f4 v, unsigned ta, unsigned tb) { return v4u{tg(v.x, ta), tg(v.y, tb), tg(v.z, ta), tg(v.w, tb)}; }
__device__ __forceinline__ float untg(unsigned u) { return asf(u & 0xffffff00u); }
__device__ __forceinline__ unsigned xoror(unsigned a, unsigned b, unsigned c) { return __builtin_amdgcn_bitop3_b32(a, b, c, 0xBE); }
__device__ __forceinline__ unsigned bad4(v4u u, unsigned ta, unsigned tb) { return xoror(u.x, ta, xoror(u.y, tb, xoror(u.z, ta, u.w ^ tb))) & 0xffu; }
__device__ __forceinline__ float red4(v4u a, v4u b) {
  const auto s01 = __builtin_amdgcn_permlane32_swap(asu(untg(a.x) + untg(b.x)), asu(untg(a.y) + untg(b.y)), false, false);
  const auto s23 = __builtin_amdgcn_permlane32_swap(asu(untg(a.z) + untg(b.z)), asu(untg(a.w) + untg(b.w)), false, false);
  const auto t = __builtin_amdgcn_permlane16_swap(asu(asf(s01[0]) + asf(s01[1])), asu(asf(s23[0]) + asf(s23[1])), false, false);
  float v = asf(t[0]) + asf(t[1]);
  v += dpp<ROR4>(v); v += dpp<ROR8>(v);
  return v;
}
__device__ __forceinline__ v4u trfrag(const LAS unsigned char* a, int off0, int off1) {
  const v2u lo = __builtin_bit_cast(v2u, __builtin_amdgcn_ds_read_tr16_b64_v4i16((LAS s4*)(a + off0)));
  const v2u hi = __builtin_bit_cast(v2u, __builtin_amdgcn_ds_read_tr16_b64_v4i16((LAS s4*)(a + off1)));
  return v4u{lo.x, lo.y, hi.x, hi.y};
}

#define SB() __builtin_amdgcn_sched_barrier(0)
#define FENCE() do { SB(); asm volatile("" ::: "memory"); SB(); } while (0)
#define LGKM0() do { SB(); asm volatile("s_waitcnt lgkmcnt(0)" ::: "memory"); SB(); } while (0)
#define STAMP(i)

template <int SLEEP> __device__ __forceinline__ void lds_wait(LAS unsigned char* L, int word, unsigned want) {
  FENCE();
  const volatile LAS unsigned* p = (const volatile LAS unsigned*)(L + L_FL) + word;
#pragma nounroll
  for (unsigned i = 0; i < LSPIN_CAP; ++i) {
    if ((unsigned)__builtin_amdgcn_readfirstlane(*p) >= want) break;
    if (SLEEP) __builtin_amdgcn_s_sleep(SLEEP);
  }
  FENCE();
}
template <int WORD> __device__ __forceinline__ void lds_signal(unsigned vsig) {
  SB();
  asm volatile("s_waitcnt lgkmcnt(0)\n\tds_add_u32 %0, %1 offset:%2" :: "v"(vsig), "v"(1u), "i"(WORD * 4) : "memory");
  SB();
}
template <int GS> __device__ __forceinline__ void dma4(rsrc_t r, int v0, int v1, int v2, int v3, int& so, unsigned& m0v) {
  asm volatile("s_mov_b32 m0, %1\n\ts_nop 0\n\t"
               "buffer_load_dwordx4 %2, %6, %0 offen nt lds\n\ts_add_u32 %0, %0, %7\n\t"
               "buffer_load_dwordx4 %3, %6, %0 offen offset:1024 nt lds\n\ts_add_u32 %0, %0, %7\n\t"
               "buffer_load_dwordx4 %4, %6, %0 offen offset:2048 nt lds\n\ts_add_u32 %0, %0, %7\n\t"
               "buffer_load_dwordx4 %5, %6, %0 offen offset:3072 nt lds\n\ts_add_u32 %0, %0, %8\n\ts_add_u32 %1, %1, 0x1000"
               : "+s"(so), "+s"(m0v) : "v"(v0), "v"(v1), "v"(v2), "v"(v3), "s"(r), "i"(GS - 1024), "i"(GS + 3072) : "memory", "scc");
}
struct Ld { unsigned cnt, nxt, last, vfull; };
__device__ __forceinline__ void pace(Ld& s, unsigned w4, unsigned to) {
  unsigned a, b, v;
  asm volatile("0:\n\t"
               "s_memtime vcc\n\t"
               "s_getreg_b32 %0, hwreg(HW_REG_IB_STS)\n\t"
               "s_and_b32 %1, %0, 15\n\t"
               "s_bfe_u32 %0, %0, 0x20016\n\t"
               "s_lshl4_add_u32 %0, %0, %1\n\t"
               "s_sub_u32 %1, %5, %0\n\t"
               "s_cmp_lt_i32 %1, %3\n\t"
               "s_cbranch_scc1 1f\n\t"
               "s_lshr_b32 %1, %1, 4\n\t"
               "v_mov_b32 %2, %1\n\t"
               "ds_write_b32 %6, %2\n\t"
               "s_lshl_b32 %1, %1, 4\n\t"
               "s_add_u32 %3, %1, 16\n"
               "1:\n\t"
               "s_waitcnt lgkmcnt(0)\n\t"
               "s_cmp_le_u32 %0, %7\n\t"
               "s_cbranch_scc1 2f\n\t"
               "s_sub_u32 %1, vcc_lo, %4\n\t"
               "s_cmp_ge_u32 %1, %8\n\t"
               "s_cselect_b32 %1, %9, 0\n\t"
               "s_cmp_le_u32 %0, %1\n\t"
               "s_cbranch_scc0 0b\n"
               "2:\n\t"
               "s_mov_b32 %4, vcc_lo"
               : "=&s"(a), "=&s"(b), "=&v"(v), "+s"(s.nxt), "+s"(s.last)
               : "s"(s.cnt), "v"(s.vfull), "s"(w4), "s"(to), "i"(WHI - 4) : "memory", "scc", "vcc");
}
template <int GS, int F0, int NF>
__device__ __forceinline__ void stream(LAS unsigned char* L, Ld& s, rsrc_t r, int v0, int v1, int v2, int v3, int so, int rot, unsigned lds0, unsigned& w4, unsigned& to) {
  volatile LAS unsigned* fl = (volatile LAS unsigned*)(L + L_FL);
  const int base = so;
#pragma nounroll
  for (int f = F0; f < F0 + NF; ++f) {
    unsigned m0v = lds0 + (f & 7) * 16384;
    if (NF == 8) so = base + ((f - F0 + rot) & 7) * 16384;
    if (F0 + NF > 8 && f >= 8) {
      const unsigned now = (unsigned)__builtin_amdgcn_s_memrealtime(), ref = __builtin_amdgcn_readfirstlane(fl[F_REF]);
      const int lag = (CTL && ref) ? (int)(now - ref) - (F0 < 20 ? TOFF + (f - 5) * TAU : TOFF + 15 * TAU + (f - 20) * TAU2) : 0;
      w4 = lag > TTH ? WHI - 4 : lag < -TTH ? WLO - 4 : WIN - 4;
      to = lag < -TTH ? TOLO : TOMID;
      lds_wait<LSLP>(L, F_D + (f < 12 ? (f & ~1) : f) - 8, f < 12 ? 4u : f < 20 ? 1u : 8u);
    }
#pragma nounroll
    for (int i = 0; i < 4; ++i) {
      pace(s, w4, to);
      dma4<GS>(r, v0, v1, v2, v3, so, m0v);
      s.cnt += 4;
    }
  }
}

__global__ void __launch_bounds__(NT)
fused_decode(const unsigned short* __restrict__ x, const void* __restrict__ kc, const void* __restrict__ vc, const void* __restrict__ wq,
             const void* __restrict__ wo, const void* __restrict__ w1, const void* __restrict__ b1, const void* __restrict__ w2,
             const void* __restrict__ b2, void* __restrict__ out, void* ws) {
  unsigned long long wq_early;
  asm volatile("s_load_dwordx2 %0, %1, 0x18" : "=s"(wq_early) : "s"(__builtin_amdgcn_kernarg_segment_ptr()));
  SB();
  __shared__ __attribute__((aligned(16))) unsigned char smem[LDS_BYTES];
  LAS unsigned char* const L = (LAS unsigned char*)smem;
  const int tid = threadIdx.x, lane = tid & 63, wv = __builtin_amdgcn_readfirstlane(tid >> 6), c = blockIdx.x;
  const int g = lane >> 4, n16 = lane & 15;
  const int grp = c & 7, sp = c >> 3, ab = grp >> 2, ahkv = grp & 3;
  const int ksq = c >> 6, cgq = c & 63;
  const bool has1 = grp != (sp & 7);
  const int c1 = sp * 7 + grp - (grp > (sp & 7) ? 1 : 0);
  const int ks1 = c1 / 56, cg1 = c1 % 56;
  const int cgo = sp & 7;
  const int ho = grp * 4 + (sp >> 3);
  const int ks2 = (sp >> 3) * 8 + grp;
  const bool has2 = ks2 < 28;

  const unsigned vsig = (unsigned)(size_t)smem + (lane == 0 ? L_FL : L_SINK + lane * 4);
  if (tid < F_WORDS) ((volatile LAS unsigned*)(L + L_FL))[tid] = 0u;
  SB();
  asm volatile("s_waitcnt lgkmcnt(0)" : "+s"(wq_early) :: "memory");
  __builtin_amdgcn_s_barrier();
  FENCE();
  if (wv >= 1 && wv < NWAVES - 1) {
    const int w = wv - 1;
    STAMP(0);
    const rsrc_t rws = mk(ws, WS_END);
    const rsrc_t rws2 = mk(ws, has2 ? WS_END : 0u);
    unsigned long long xl = (unsigned long long)x;
    const rsrc_t rxl = mk((const void*)xl, w < 2 ? 4096u : 0u);
    const v4u xa = __builtin_amdgcn_raw_buffer_load_b128(rxl, lane * 16, w * 2048, 0);
    const v4u xb = __builtin_amdgcn_raw_buffer_load_b128(rxl, lane * 16, w * 2048 + 1024, 0);
    FENCE();
    if (w < 2) {
      float xf[16];
#pragma unroll
      for (int i = 0; i < 4; ++i) { xf[2 * i] = blo(xa[i]); xf[2 * i + 1] = bhi(xa[i]); xf[8 + 2 * i] = blo(xb[i]); xf[9 + 2 * i] = bhi(xb[i]); }
      const float piv = asf(__builtin_amdgcn_readfirstlane(asu(xf[0])));
      float s1 = 0.f, s2 = 0.f;
#pragma unroll
      for (int i = 0; i < 16; ++i) { const float d = xf[i] - piv; s1 += d; s2 = fmaf(d, d, s2); }
      float a1 = wave_sum(s1) * (1.0f / 1024), a2 = wave_sum(s2) * (1.0f / 1024);
      float mean = rb(piv + a1), inv = rcp(rb(__builtin_amdgcn_sqrtf(rb(rb(fmaxf(a2 - a1 * a1, 0.f)) + 1e-5f))));
      v4u pa, pb;
      s1 = 0.f; s2 = 0.f;
#pragma unroll
      for (int i = 0; i < 8; ++i) {
        const unsigned t = pk(xf[2 * i] - mean, xf[2 * i + 1] - mean), u = pk(blo(t) * inv, bhi(t) * inv);
        if (i < 4) pa[i] = u; else pb[i - 4] = u;
        xf[2 * i] = blo(u); xf[2 * i + 1] = bhi(u);
        s1 += xf[2 * i] + xf[2 * i + 1]; s2 = fmaf(xf[2 * i], xf[2 * i], fmaf(xf[2 * i + 1], xf[2 * i + 1], s2));
      }
      *(LAS v4u*)(L + L_XS + w * 2048 + lane * 16) = pa;
      *(LAS v4u*)(L + L_XS + w * 2048 + 1024 + lane * 16) = pb;
      a1 = wave_sum(s1) * (1.0f / 1024); a2 = wave_sum(s2) * (1.0f / 1024);
      mean = rb(a1); inv = rcp(rb(__builtin_amdgcn_sqrtf(rb(rb(fmaxf(a2 - a1 * a1, 0.f)) + 1e-5f))));
#pragma unroll
      for (int i = 0; i < 8; ++i) {
        const unsigned t = pk(xf[2 * i] - mean, xf[2 * i + 1] - mean), u = pk(blo(t) * inv, bhi(t) * inv);
        if (i < 4) pa[i] = u; else pb[i - 4] = u;
      }
      *(LAS v4u*)(L + L_YS + w * 2048 + lane * 16) = pa;
      *(LAS v4u*)(L + L_YS + w * 2048 + 1024 + lane * 16) = pb;
      lds_signal<F_LN>(vsig);
    }
    const int q4 = n16 >> 2, p4 = lane & 3;
    lds_wait<LSLP>(L, F_LN, 3);
    const unsigned tag = __builtin_amdgcn_readfirstlane(((volatile LAS unsigned*)(L + L_FL))[F_TAG]);
    const unsigned ta = tag & 0xffu, tb = (tag >> 8) & 0xffu;
    STAMP(1);
    {
      const bool isq = w < 4;
      const LAS unsigned char* ap = L + (isq ? L_XS + ksq * 512 : L_YS + (ks1 & 3) * 512) + (lane & 1) * 2048 + g * 16;
      v4u A[8];
#pragma unroll
      for (int s = 0; s < 8; ++s) A[s] = *(const LAS v4u*)(ap + s * 64);
      lds_wait<LSLP>(L, F_FULL, isq ? 2 : 4);
      const LAS unsigned char* bp = L + (isq ? 0 : 32768) + (8 * g + q4) * 128 + (((w & 3) ^ ((q4 >> 1) | ((g & 1) << 1))) * 32) + p4 * 8;
      v4u Bf[8];
#pragma unroll
      for (int s = 0; s < 8; ++s) Bf[s] = trfrag(bp, s * 4096, s * 4096 + 512);
      lds_signal<F_DQ>(vsig + (isq ? 0 : (F_DH - F_DQ) * 4));
      f4 d0 = mfma32(Bf[0], A[0], f4{0.f, 0.f, 0.f, 0.f}), d1 = mfma32(Bf[1], A[1], f4{0.f, 0.f, 0.f, 0.f});
#pragma unroll
      for (int s = 2; s < 8; s += 2) { d0 = mfma32(Bf[s], A[s], d0); d1 = mfma32(Bf[s + 1], A[s + 1], d1); }
      const rsrc_t rp = mk(ws, (isq || has1) ? WS_END : 0u);
      const int pso = (isq ? WS_QP + cgq * 2048 + ksq * 128 : WS_HP + cg1 * 2048 + ks1 * 128) + (w & 3) * 512;
      stc4(rp, n16 < 2 ? g * 32 + n16 * 16 : OOB, pso, tg4(d0 + d1, ta, tb));
    }
    STAMP(2);
    v4u K[16];
    {
      const LAS unsigned char* kp = L + ((4 + w) & 7) * 16384 + n16 * 256;
      lds_wait<1>(L, F_FULL, 5 + w);
#pragma unroll
      for (int s = 0; s < 4; ++s) {
        const LAS unsigned char* kq = kp + (((4 * s + g) ^ n16) * 16);
#pragma unroll
        for (int t = 0; t < 4; ++t) K[4 * t + s] = *(const LAS v4u*)(kq + t * 4096);
      }
      lds_signal<F_DK>(vsig + w * 4);
    }
    STAMP(3);
    lds_wait<1>(L, F_Q, 1);
    {
      const LAS unsigned char* qb = L + L_QS + (n16 >> 1) * 272 + g * 16;
      v4u qf[4];
#pragma unroll
      for (int s = 0; s < 4; ++s) qf[s] = *(const LAS v4u*)(qb + s * 64);
      f4 sc[4];
#pragma unroll
      for (int t = 0; t < 4; ++t) sc[t] = mfma32(K[4 * t], qf[0], f4{0.f, 0.f, 0.f, 0.f});
#pragma unroll
      for (int s = 1; s < 4; ++s)
#pragma unroll
        for (int t = 0; t < 4; ++t) sc[t] = mfma32(K[4 * t + s], qf[s], sc[t]);
#pragma unroll
      for (int t = 0; t < 4; ++t)
#pragma unroll
        for (int r = 0; r < 4; r += 2) {
          const unsigned u = pk(sc[t][r], sc[t][r + 1]), v = pk(blo(u) * 0.088388347648318440f, bhi(u) * 0.088388347648318440f);
          sc[t][r] = blo(v); sc[t][r + 1] = bhi(v);
        }
      float mw = fmaxf(fmaxf(sc[0].x, sc[0].y), fmaxf(sc[0].z, sc[0].w));
#pragma unroll
      for (int t = 1; t < 4; ++t) mw = fmaxf(mw, fmaxf(fmaxf(sc[t].x, sc[t].y), fmaxf(sc[t].z, sc[t].w)));
      mw = x32_max(x16_max(mw)) * LOG2E;
      float lw = 0.f;
      const unsigned pm = (lane & 1) ? 0xffff0000u : 0u;
      unsigned P[8];
#pragma unroll
      for (int t = 0; t < 4; ++t)
#pragma unroll
        for (int r = 0; r < 4; r += 2) {
          const float v0 = ex2(fmaf(sc[t][r], LOG2E, -mw)), v1 = ex2(fmaf(sc[t][r + 1], LOG2E, -mw));
          lw += v0 + v1;
          const unsigned h = pk(v0, v1);
          P[2 * t + (r >> 1)] = pk(v0 - asf((h << 16) & pm), v1 - asf(h & pm));
        }
      *(LAS v4u*)(L + L_P + w * 2048 + lane * 16) = v4u{P[0], P[1], P[2], P[3]};
      *(LAS v4u*)(L + L_P + w * 2048 + 1024 + lane * 16) = v4u{P[4], P[5], P[6], P[7]};
      lw = x32_sum(x16_sum(lw));
      if (lane < 16 && !(lane & 1)) *(LAS f2*)(L + L_ST + (w * 8 + (lane >> 1)) * 8) = f2{mw, lw};
      lds_signal<F_P>(vsig);
    }
    STAMP(4);
    lds_wait<1>(L, F_P, 8);
    float o0 = 0.f, o1 = 0.f;
    {
      f4 st[8];
#pragma unroll
      for (int j = 0; j < 8; ++j) st[j] = *(const LAS f4*)(L + L_ST + (j * 8 + 2 * g) * 8);
      float M0 = st[0].x, M1 = st[0].z;
#pragma unroll
      for (int j = 1; j < 8; ++j) { M0 = fmaxf(M0, st[j].x); M1 = fmaxf(M1, st[j].z); }
      M0 = __builtin_ceilf(M0); M1 = __builtin_ceilf(M1);
#pragma unroll
      for (int j = 0; j < 8; ++j) { st[j].x = ex2(st[j].x - M0); st[j].z = ex2(st[j].z - M1); }
      STAMP(5);
      int ovs = (lane & 2) ? OOB : (2 * g + (lane & 1)) * 32768 + (n16 >> 2) * 16;
      asm volatile("" : "+v"(ovs));
      const LAS unsigned char* vp = L + (4 * g + q4) * 256 + ((w ^ (4 * (g & 1) + q4)) * 32) + p4 * 8;
#pragma unroll
      for (int j = 0; j < 8; ++j) {
        const v4u pa = *(const LAS v4u*)(L + L_P + j * 2048 + lane * 16), pb = *(const LAS v4u*)(L + L_P + j * 2048 + 1024 + lane * 16);
        if (j < 7) lds_wait<LSLP>(L, F_FULL, 13 + j); else lds_wait<0>(L, F_FULL, 20);
        const LAS unsigned char* vq = vp + ((4 + j) & 7) * 16384;
        const v4u va = trfrag(vq, 0, 4096), vb = trfrag(vq, 8192, 12288);
        if (j == 0) lds_signal<F_DV>(vsig); else if (j == 1) lds_signal<F_DV + 1>(vsig); else if (j == 2) lds_signal<F_DV + 2>(vsig); else if (j == 3) lds_signal<F_DV + 3>(vsig);
        f4 d = mfma32(pa, va, f4{0.f, 0.f, 0.f, 0.f});
        d = mfma32(pb, vb, d);
        o0 = fmaf(st[j].x, d.x + d.y, o0);
        o1 = fmaf(st[j].z, d.z + d.w, o1);
      }
      const bool odd = (lane & 1) != 0;
      const float rcv = dpp<XOR1>(odd ? o0 : o1);
      const unsigned gr = odd ? pk(rcv, o1) : pk(o0, rcv);
      const unsigned gn = dppu<XOR2>(gr);
      stc4(rws, ovs, WS_OP + ((ahkv * 64 + w) * 32 + sp) * 128 + ab * 64, v4u{gr, tag, gn, tag});
    }
    STAMP(6);
    const LAS unsigned char* wp = L + (8 * g + q4) * 256 + ((w ^ (q4 | ((g & 1) << 2))) * 32) + p4 * 8;
    const int fvo = (g >> 1) * 2048 + (g & 1) * 32;
    v4u MPv;
    {
      v4u A[4];
      lds_wait<1>(L, F_H, 1);
#pragma unroll
      for (int s = 0; s < 4; ++s) A[s] = *(const LAS v4u*)(L + L_HS + (lane & 1) * 272 + s * 64 + g * 16);
      lds_wait<LSLP>(L, F_FULL, 22);
      f4 d = {0.f, 0.f, 0.f, 0.f};
#pragma unroll
      for (int s = 0; s < 4; ++s) d = mfma32(trfrag(wp + 4 * 16384, s * 8192, s * 8192 + 1024), A[s], d);
      MPv = tg4(d, ta, tb);
      if (!MPLATE) stc4(rws2, n16 < 2 ? fvo + n16 * 16 : OOB, WS_MP + ((cgo * 16 + 2 * w) * 32 + ks2) * 64, MPv);
    }
    STAMP(7);
    {
      const int ovo = ((n16 >> 2) & 1) * 2048 + g * 512 + (n16 >> 3) * 64 + (lane & 3) * 16, oso = WS_OP + (ho * 8 + w) * 4096;
      v4u G[4];
      lds_wait<0>(L, F_FULL, OGATE);
#pragma unroll
      for (int j = 0; j < 4; ++j) G[j] = ldc4(rws, ovo + j * 128, oso);
      SB();
      if (MPLATE) stc4(rws2, n16 < 2 ? fvo + n16 * 16 : OOB, WS_MP + ((cgo * 16 + 2 * w) * 32 + ks2) * 64, MPv);
      SB();
      lds_wait<0>(L, F_W, 1);
      v2u WF = *(const LAS v2u*)(L + L_WT + (lane & 3) * 32 + g * 8);
      float invl = ((const LAS float*)(L + L_WT + 128))[(lane >> 1) & 1];
      LAS unsigned char* osw = L + (n16 == g ? L_OS + g * 272 + w * 32 : L_P + w * 2048 + lane * 32);
      const LAS unsigned char* osr = L + L_OS + (lane & 3) * 272 + g * 16;
      const LAS unsigned char* wop = wp + 6 * 16384;
      int fv2 = (n16 & 13) == 0 ? fvo + (n16 >> 1) * 16 : OOB, fso = WS_FP + ((cgo * 16 + 2 * w) * 32 + ho) * 64;
      unsigned vta = ta, vtb = tb, vtag = tag;
      asm volatile("" : "+v"(WF), "+v"(invl), "+v"(osw), "+v"(osr), "+v"(wop), "+v"(fv2), "+s"(fso), "+v"(vta), "+v"(vtb), "+v"(vtag));
      lds_wait<0>(L, F_FULL, 24);
      v4u Bo[4];
#pragma unroll
      for (int s = 0; s < 4; ++s) Bo[s] = trfrag(wop, s * 8192, s * 8192 + 1024);
      SB();
      STAMP(8);
      const f4 z = {0.f, 0.f, 0.f, 0.f};
      f4 e0, e1, e2, e3;
#define COMBINE() do { \
        e0 = mfma16(v2u{plo(G[0].x, G[1].x), plo(G[2].x, G[3].x)}, WF, z); e1 = mfma16(v2u{phi(G[0].x, G[1].x), phi(G[2].x, G[3].x)}, WF, z); \
        e2 = mfma16(v2u{plo(G[0].z, G[1].z), plo(G[2].z, G[3].z)}, WF, z); e3 = mfma16(v2u{phi(G[0].z, G[1].z), phi(G[2].z, G[3].z)}, WF, z); } while (0)
#define OBAD(j, acc) xoror(G[j].y, vtag, xoror(G[j].w, vtag, acc))
      COMBINE();
      SB();
      if (__builtin_expect(__any((OBAD(0, OBAD(1, 0u)) | OBAD(2, OBAD(3, 0u))) != 0u), 0)) {
#pragma nounroll
        for (unsigned spin = 0; spin < SPIN_CAP; ++spin) {
          const bool m0 = __any(OBAD(0, 0u) != 0u), m1 = __any(OBAD(1, 0u) != 0u), m2 = __any(OBAD(2, 0u) != 0u), m3 = __any(OBAD(3, 0u) != 0u);
          if (!(m0 || m1 || m2 || m3)) break;
          FENCE();
          if (m0) G[0] = ldc4(rws, ovo, oso);
          if (m1) G[1] = ldc4(rws, ovo + 128, oso);
          if (m2) G[2] = ldc4(rws, ovo + 256, oso);
          if (m3) G[3] = ldc4(rws, ovo + 384, oso);
        }
        COMBINE();
      }
      STAMP(9);
      *(LAS v4u*)osw = v4u{pk(e0.x, e1.x), pk(e2.x, e3.x), pk(e0.y, e1.y), pk(e2.y, e3.y)};
      *(LAS v4u*)(osw + 16) = v4u{pk(e0.z, e1.z), pk(e2.z, e3.z), pk(e0.w, e1.w), pk(e2.w, e3.w)};
      LGKM0();
      __builtin_amdgcn_s_barrier();
      FENCE();
      v4u a0 = *(const LAS v4u*)osr, a1 = *(const LAS v4u*)(osr + 64), a2 = *(const LAS v4u*)(osr + 128), a3 = *(const LAS v4u*)(osr + 192);
      SB();
      f4 d = mfma32(Bo[0], a0, z), d2 = mfma32(Bo[1], a1, z);
      d = mfma32(Bo[2], a2, d); d2 = mfma32(Bo[3], a3, d2);
      d += d2;
      d.x += dpp<XOR1>(d.x); d.y += dpp<XOR1>(d.y); d.z += dpp<XOR1>(d.z); d.w += dpp<XOR1>(d.w);
      d *= invl;
      stc4(rws, fv2, fso, v4u{(asu(d.x) & 0xffffff00u) | vta, (asu(d.y) & 0xffffff00u) | vtb, (asu(d.z) & 0xffffff00u) | vta, (asu(d.w) & 0xffffff00u) | vtb});
    }
    STAMP(10);
  } else if (wv == NWAVES - 1) {
    STAMP(0);
    volatile LAS unsigned* fl = (volatile LAS unsigned*)(L + L_FL);
    const rsrc_t rws = mk(ws, WS_END), rws1 = mk(ws, has1 ? WS_END : 0u), rws2 = mk(ws, has2 ? WS_END : 0u), rout = mk(out, 4096);
    const unsigned eraw = __builtin_amdgcn_raw_buffer_load_b32(rws, 0, WS_E, AUX_SC1);
    const unsigned b1p = __builtin_amdgcn_raw_buffer_load_b32(mk(b1, has1 ? 7168u : 0u), (n16 >> 2) * 8 + (lane & 1) * 4, c1 * 32, 0);
    const int col2 = ((lane >> 1) & 1) * 8 + ((g >> 1) | ((g & 1) << 1)) * 2;
    const int fo = (lane & 1) * 2048 + col2;
    const unsigned short xfin = __builtin_amdgcn_raw_buffer_load_b16(mk(x, c < 128 ? 4096u : 0u), fo, c * 16, 0);
    const unsigned short b2fin = __builtin_amdgcn_raw_buffer_load_b16(mk(b2, c < 128 ? 2048u : 0u), col2, c * 16, 0);
    const v4u codepf = __builtin_amdgcn_raw_buffer_load_b128(mk((const void*)((__builtin_amdgcn_s_getpc() & ~255ull) - PFB * 1024u), PF * 1024u), lane * 16, sp * 1024, 0);
    FENCE();
    const unsigned tag = __builtin_amdgcn_readfirstlane(eraw) * 0x9E3779B1u + 0x7F4A7C15u;
    const unsigned ta = tag & 0xffu, tb = (tag >> 8) & 0xffu;
    fl[F_TAG] = tag;
    lds_signal<F_LN>(vsig);
    asm volatile("" :: "v"(codepf));
    lds_wait<1>(L, F_FULL, 4);
    const unsigned t4 = (unsigned)__builtin_amdgcn_s_memrealtime();
    lds_wait<LSLP>(L, F_FULL, S1GATE);
    {
      const int fvo = (lane < 16 && !(lane & 1)) ? (lane >> 2) * 16 : OOB, fb = (lane >> 1) & 1;
      bool needq = true, needh = has1;
#pragma nounroll
      for (unsigned spin = 0; (needq || needh) && spin < SPIN_CAP; ++spin) {
        const v2u GQ = ldc2(rws, needq ? lane * 8 : OOB, WS_QP + c * 512), GH = ldc2(rws1, needh ? lane * 8 : OOB, WS_HP + c1 * 512);
        if (needq && !__any((((GQ.x ^ ta) | (GQ.y ^ tb)) & 0xffu) != 0u)) {
          const unsigned gr = pk(x32_sum(x16_sum(untg(GQ.x))), x32_sum(x16_sum(untg(GQ.y)))), gn = dppu<XOR1>(gr), ty = (tag << 12) | (t4 & 0xfffu);
          stc4(rws, fvo + fb * 16384, WS_QF + c * 64, v4u{gr, ty, gn, ty});
          needq = false;
          STAMP(13);
        }
        if (needh && !__any((((GH.x ^ ta) | (GH.y ^ tb)) & 0xffu) != 0u)) {
          float h0 = rb(rb(x32_sum(x16_sum(untg(GH.x)))) + blo(b1p)), h1 = rb(rb(x32_sum(x16_sum(untg(GH.y)))) + bhi(b1p));
          constexpr float GC = -2.0f * 0.7978845608028654f * 1.4426950408889634f;
          h0 = h0 * rcp(1.0f + ex2(GC * (h0 + 0.044715f * h0 * h0 * h0)));
          h1 = h1 * rcp(1.0f + ex2(GC * (h1 + 0.044715f * h1 * h1 * h1)));
          const unsigned gr = pk(h0, h1), gn = dppu<XOR1>(gr);
          stc4(rws, fvo + fb * 14336, WS_HF + c1 * 64, v4u{gr, tag, gn, tag});
          needh = false;
          STAMP(14);
        }
        __builtin_amdgcn_s_sleep(PSLP);
      }
    }
    lds_wait<LSLP>(L, F_FULL, QGATE);
    {
      v4u Q[4];
      const unsigned t20 = tag & 0xfffffu;
#pragma nounroll
      for (unsigned spin = 0; spin < SPIN_CAP; ++spin) {
        unsigned bad = 0;
#pragma unroll
        for (int i = 0; i < 4; ++i) Q[i] = ldc4(rws, lane * 16, WS_QF + grp * 4096 + i * 1024);
#pragma unroll
        for (int i = 0; i < 4; ++i) bad |= ((Q[i].y >> 12) ^ t20) | ((Q[i].w >> 12) ^ t20);
        if (!__any(bad != 0u)) break;
        __builtin_amdgcn_s_sleep(PSLP);
      }
#pragma unroll
      for (int i = 0; i < 4; ++i) *(LAS v2u*)(L + L_QS + (2 * i + (lane >> 5)) * 272 + (lane & 31) * 8) = v2u{Q[i].x, Q[i].z};
      lds_signal<F_Q>(vsig);
      STAMP(15);
      if (CTL) {
        int d = 0;
#pragma unroll
        for (int i = 0; i < 4; ++i) d += (int)((Q[i].y - t4) << 20) >> 20;
        fl[F_REF] = (t4 + (int)(wave_sum((float)d) * (1.0f / 256))) | 1u;
      }
    }
    lds_wait<1>(L, F_P, 8);
    {
      f2 sv[8];
#pragma unroll
      for (int j = 0; j < 8; ++j) sv[j] = *(const LAS f2*)(L + L_ST + (j * 8 + (lane & 7)) * 8);
      float M = sv[0].x, Ls = 0.f;
#pragma unroll
      for (int j = 1; j < 8; ++j) M = fmaxf(M, sv[j].x);
      M = __builtin_ceilf(M);
#pragma unroll
      for (int j = 0; j < 8; ++j) Ls = fmaf(ex2(sv[j].x - M), sv[j].y, Ls);
      stc4(rws, lane < 8 ? (lane >> 2) * 2048 + (lane & 3) * 16 : OOB, WS_ST + grp * 4096 + sp * 64, v4u{asu(M), tag, asu(Ls), tag});
      const unsigned tag2 = has2 ? tag : 0u;
      v4u H;
#pragma nounroll
      for (unsigned spin = 0; spin < SPIN_CAP; ++spin) {
        H = ldc4(rws2, (lane >> 5) * 14336 + (lane & 31) * 16, WS_HF + ks2 * 512);
        if (!__any(((H.y ^ tag2) | (H.w ^ tag2)) != 0u)) break;
        __builtin_amdgcn_s_sleep(PSLP);
      }
      *(LAS v2u*)(L + L_HS + (lane >> 5) * 272 + (lane & 31) * 8) = v2u{H.x, H.z};
      lds_signal<F_H>(vsig);
      STAMP(16);
    }
    lds_wait<LSLP>(L, F_FULL, SGATE);
    {
      v4u SG;
#pragma nounroll
      for (unsigned spin = 0; spin < SPIN_CAP; ++spin) {
        SG = ldc4(rws, (lane >> 5) * 16384 + (lane & 31) * 64, WS_ST + (ho >> 2) * 2048 + (ho & 3) * 16);
        if (!__any(((SG.y ^ tag) | (SG.w ^ tag)) != 0u)) break;
        __builtin_amdgcn_s_sleep(PSLP);
      }
      const float m = asf(SG.x), M = x16_max(row_max(m));
      const float e = rb(ex2(m - M)), Ls = x16_sum(row_sum(e * asf(SG.z)));
      ((LAS unsigned short*)(L + L_WT))[lane] = (unsigned short)(asu(e) >> 16);
      ((LAS float*)(L + L_WT + 128))[lane >> 5] = rcp(Ls) * (1.0f + 1.0f / 131072);
      lds_signal<F_W>(vsig);
      STAMP(17);
    }
    {
      const rsrc_t rwf = mk(ws, c < 128 ? WS_END : 0u);
      const unsigned mlive = lane < 48 ? 0xffffffffu : 0u;
      __builtin_amdgcn_s_barrier();
      FENCE();
      if (c < 128) {
        v4u M0 = ldc4(rwf, lane * 16, WS_MP + c * 2048), M1 = ldc4(rwf, lane * 16, WS_MP + c * 2048 + 1024);
        v4u F0, F1;
        if (!FLATE) { F0 = ldc4(rwf, lane * 16, WS_FP + c * 2048); F1 = ldc4(rwf, lane * 16, WS_FP + c * 2048 + 1024); }
        SB();
        if (__any((bad4(M0, ta, tb) | (bad4(M1, ta, tb) & mlive)) != 0u)) {
#pragma nounroll
          for (unsigned spin = 0; spin < SPIN_CAP; ++spin) {
            FENCE();
            M0 = ldc4(rwf, lane * 16, WS_MP + c * 2048); M1 = ldc4(rwf, lane * 16, WS_MP + c * 2048 + 1024);
            if (!__any((bad4(M0, ta, tb) | (bad4(M1, ta, tb) & mlive)) != 0u)) break;
          }
        }
        const float m = rb(rb(red4(M0, M1 & mlive)) + blo(b2fin));
        float gt = rb(m * rcp(1.0f + ex2(-1.4426950408889634f * m))), xv = blo(xfin);
        asm volatile("" : "+v"(gt), "+v"(xv));
        if (FLATE) { if (FSLP0) __builtin_amdgcn_s_sleep(FSLP0); FENCE(); F0 = ldc4(rwf, lane * 16, WS_FP + c * 2048); F1 = ldc4(rwf, lane * 16, WS_FP + c * 2048 + 1024); }
#pragma nounroll
        for (unsigned spin = 0; spin < SPIN_CAP; ++spin) {
          const bool m0 = __any(bad4(F0, ta, tb) != 0u), m1 = __any(bad4(F1, ta, tb) != 0u);
          if (!(m0 || m1)) break;
          FENCE();
          if (FSLP) __builtin_amdgcn_s_sleep(FSLP);
          if (m0) F0 = ldc4(rwf, lane * 16, WS_FP + c * 2048);
          if (m1) F1 = ldc4(rwf, lane * 16, WS_FP + c * 2048 + 1024);
        }
        STAMP(25);
        const float o = xv + rb(rb(red4(F0, F1)) * gt);
        __builtin_amdgcn_raw_buffer_store_b16((unsigned short)pk(o, o), rout, (lane & 12) == 0 ? fo : OOB, c * 16, OAUX);
        __builtin_amdgcn_raw_buffer_store_b32(tag, rws, (c | lane) == 0 ? 0 : OOB, WS_E, EAUX);
        STAMP(26);
      }
    }
  } else {
    __builtin_amdgcn_s_setprio(3);
    STAMP(0);
    const rsrc_t rq = mk((const void*)wq_early, WQB);
    const int r8 = lane >> 3, c8 = lane & 7;
    const int f0 = r8 & 2;
    const int vq0 = r8 * 8192 + (c8 ^ f0) * 16, vq1 = vq0 ^ 64;
    int so = ksq * 256 * 8192 + cgq * 128;
    unsigned m0v = (unsigned)(size_t)smem;
#pragma unroll
    for (int i = 0; i < WIN / 4; ++i) dma4<8 * 8192>(rq, vq0, vq1, vq0, vq1, so, m0v);
    SB();
    volatile LAS unsigned* fl = (volatile LAS unsigned*)(L + L_FL);
    const unsigned lds0 = (unsigned)(size_t)smem;
    Ld st = {WIN, 16, 0, lds0 + L_FL + F_FULL * 4};
    unsigned w4 = WIN - 4, to = 0x7fffffffu;
#pragma nounroll
    for (int i = WIN / 4; i < 8; ++i) { pace(st, w4, to); dma4<8 * 8192>(rq, vq0, vq1, vq0, vq1, so, m0v); st.cnt += 4; }
    to = TOMID;
    {
      const int v0 = r8 * 7168 + (c8 ^ f0) * 16;
      stream<8 * 7168, 2, 2>(L, st, mk(w1, has1 ? W1B : 0u), v0, v0 ^ 64, v0, v0 ^ 64, ks1 * 256 * 7168 + cg1 * 128, 0, lds0, w4, to);
    }
    const int kvb = (grp * 16384 + sp * 512) * 256;
    {
      const int v0 = g * 256 + (n16 ^ g) * 16;
      stream<1024, 4, 8>(L, st, mk(kc, KVB), v0, v0 ^ 64, v0 ^ 128, v0 ^ 192, kvb, sp, lds0, w4, to);
    }
    {
      const int v0 = g * 256 + (n16 ^ (2 * g)) * 16;
      stream<1024, 12, 8>(L, st, mk(vc, KVB), v0, v0 ^ 128, v0, v0 ^ 128, kvb, sp, lds0, w4, to);
    }
    {
      const int v0 = g * 2048 + (n16 ^ (2 * g)) * 16;
      stream<8192, 20, 2>(L, st, mk(w2, has2 ? W1B : 0u), v0, v0, v0 ^ 128, v0 ^ 128, ks2 * 128 * 2048 + cgo * 256, 0, lds0, w4, to);
      stream<8192, 22, 2>(L, st, mk(wo, WQB), v0, v0, v0 ^ 128, v0 ^ 128, ho * 128 * 2048 + cgo * 256, 0, lds0, w4, to);
    }
    pace(st, 0u, 0x7fffffffu);
    asm volatile("s_waitcnt vmcnt(0)" ::: "memory");
    fl[F_FULL] = 24;
  }
}

}

extern "C" void kernel_launch(void* const* d_in, const int* in_sizes, int n_in, void* d_out, int out_size, void* d_ws, size_t ws_size,
                              hipStream_t stream) {
  (void)in_sizes; (void)n_in; (void)out_size; (void)ws_size;
  fused_decode<<<256, NT, 0, stream>>>((const unsigned short*)d_in[0], d_in[1], d_in[2], d_in[3], d_in[4], d_in[6], d_in[7], d_in[8], d_in[9],
                                       d_out, d_ws);
}
```

```cpp
#include <hip/hip_runtime.h>
#include <hip/hip_bf16.h>

namespace {

typedef unsigned v4u __attribute__((ext_vector_type(4)));
typedef unsigned v2u __attribute__((ext_vector_type(2)));
typedef float f4 __attribute__((ext_vector_type(4)));
typedef float f2 __attribute__((ext_vector_type(2)));
typedef __bf16 bf8 __attribute__((ext_vector_type(8)));
typedef __bf16 bf2 __attribute__((ext_vector_type(2)));
typedef short s4 __attribute__((ext_vector_type(4)));
typedef __amdgpu_buffer_rsrc_t rsrc_t;
#define LAS __attribute__((address_space(3)))

constexpr int NWAVES = 10;
constexpr int NT = 64 * NWAVES;
constexpr unsigned KVB = 2u * 4 * 16384 * 128 * 2, WQB = 1024u * 4096 * 2, W1B = 1024u * 3584 * 2;
constexpr unsigned WS_E = 0;
constexpr unsigned WS_QP = 4096;
constexpr unsigned WS_HP = WS_QP + 131072;
constexpr unsigned WS_QF = WS_HP + 114688;
constexpr unsigned WS_HF = WS_QF + 32768;
constexpr unsigned WS_ST = WS_HF + 28672;
constexpr unsigned WS_OP = WS_ST + 32768;
constexpr unsigned WS_FP = WS_OP + 1048576;
constexpr unsigned WS_MP = WS_FP + 262144;
constexpr unsigned WS_END = WS_MP + 262144;
constexpr int L_P = 131072, L_XS = L_P, L_YS = L_P + 4096, L_QS = L_P + 16384, L_HS = L_QS + 8 * 272, L_OS = L_HS + 2 * 272,
              L_ST = L_OS + 4 * 272, L_WT = L_ST + 512, L_FL = L_WT + 256, L_SINK = L_FL + 128, L_STAMP = L_SINK + 512, L_END = L_STAMP + NWAVES * 256;
constexpr int LDS_BYTES = L_END;
static_assert(L_END <= 163840 && (L_HS % 16) == 0 && (L_OS % 16) == 0 && (L_ST % 16) == 0 && (L_WT % 16) == 0 && (L_FL % 16) == 0, "lds map");
enum { F_FULL = 0, F_LN = 1, F_Q = 2, F_P = 3, F_H = 4, F_REF = 5, F_TAG = 6, F_W = 7, F_D = 8, F_DQ = F_D, F_DH = F_D + 2, F_DK = F_D + 4, F_DV = F_D + 12, F_WORDS = 32 };
constexpr int OOB = 0x7ff00000;
#define AUXL 16
#define AUXS 16
constexpr int AUX_SC1 = 16;
constexpr unsigned SPIN_CAP = 1u << 15, LSPIN_CAP = 1u << 20;
constexpr float LOG2E = 1.4426950408889634f;
#define LSLP 2
#define QGATE 10
#define OGATE 23
#define PSLP 2
#define PF 14
#define WLO 16
#define WHI 36
#define TAU 56
#define TOFF 15
#define TTH 20
#define TOMID 330
#define TOLO 700
#define TAU2 46
#define PROP 0
#define TPROP 16
#define TOHI 200
#define S1GATE 5
#define SGATE 19
#define MPLATE 0
#define PFB 3
#define OPF 1
#define OAUX 16
#define EAUX 16
#define FLATE 1
#define FSLP0 0
#define FSLP 0
#define WBO WHI
#define TTH2 TTH
#define CTL 1
#define WIN 24

__device__ __forceinline__ rsrc_t mk(const void* p, unsigned bytes) { return __builtin_amdgcn_make_buffer_rsrc((void*)p, 0, bytes, 0x00020000); }
__device__ __forceinline__ v4u ldc4(rsrc_t r, int vo, int so) { return __builtin_amdgcn_raw_buffer_load_b128(r, vo, so, AUXL); }
__device__ __forceinline__ v2u ldc2(rsrc_t r, int vo, int so) { return __builtin_amdgcn_raw_buffer_load_b64(r, vo, so, AUXL); }
__device__ __forceinline__ void stc4(rsrc_t r, int vo, int so, v4u v) { __builtin_amdgcn_raw_buffer_store_b128(v, r, vo, so, AUXS); }
__device__ __forceinline__ float asf(unsigned u) { return __builtin_bit_cast(float, u); }
__device__ __forceinline__ unsigned asu(float f) { return __builtin_bit_cast(unsigned, f); }
__device__ __forceinline__ unsigned pk(float a, float b) { f2 v = {a, b}; return __builtin_bit_cast(unsigned, __builtin_convertvector(v, bf2)); }
__device__ __forceinline__ float blo(unsigned u) { return asf(u << 16); }
__device__ __forceinline__ float bhi(unsigned u) { return asf(u & 0xffff0000u); }
__device__ __forceinline__ float rb(float a) { return blo(pk(a, a)); }
__device__ __forceinline__ unsigned plo(unsigned a, unsigned b) { return __builtin_amdgcn_perm(b, a, 0x05040100u); }
__device__ __forceinline__ unsigned phi(unsigned a, unsigned b) { return __builtin_amdgcn_perm(b, a, 0x07060302u); }
__device__ __forceinline__ f4 mfma32(v4u a, v4u b, f4 c) { return __builtin_amdgcn_mfma_f32_16x16x32_bf16(__builtin_bit_cast(bf8, a), __builtin_bit_cast(bf8, b), c, 0, 0, 0); }
__device__ __forceinline__ f4 mfma16(v2u a, v2u b, f4 c) { return __builtin_amdgcn_mfma_f32_16x16x16bf16_1k(__builtin_bit_cast(s4, a), __builtin_bit_cast(s4, b), c, 0, 0, 0); }
template <int C> __device__ __forceinline__ float dpp(float x) { return asf(__builtin_amdgcn_mov_dpp(asu(x), C, 0xf, 0xf, true)); }
template <int C> __device__ __forceinline__ unsigned dppu(unsigned x) { return __builtin_amdgcn_mov_dpp(x, C, 0xf, 0xf, true); }
constexpr int XOR1 = 0xB1, XOR2 = 0x4E, ROR4 = 0x124, ROR8 = 0x128;
__device__ __forceinline__ float x16_sum(float x) { auto s = __builtin_amdgcn_permlane16_swap(asu(x), asu(x), false, false); return asf(s[0]) + asf(s[1]); }
__device__ __forceinline__ float x32_sum(float x) { auto s = __builtin_amdgcn_permlane32_swap(asu(x), asu(x), false, false); return asf(s[0]) + asf(s[1]); }
__device__ __forceinline__ float x16_max(float x) { auto s = __builtin_amdgcn_permlane16_swap(asu(x), asu(x), false, false); return fmaxf(asf(s[0]), asf(s[1])); }
__device__ __forceinline__ float x32_max(float x) { auto s = __builtin_amdgcn_permlane32_swap(asu(x), asu(x), false, false); return fmaxf(asf(s[0]), asf(s[1])); }
__device__ __forceinline__ float row_sum(float x) { x += dpp<XOR1>(x); x += dpp<XOR2>(x); x += dpp<ROR4>(x); x += dpp<ROR8>(x); return x; }
__device__ __forceinline__ float row_max(float x) { x = fmaxf(x, dpp<XOR1>(x)); x = fmaxf(x, dpp<XOR2>(x)); x = fmaxf(x, dpp<ROR4>(x)); x = fmaxf(x, dpp<ROR8>(x)); return x; }
__device__ __forceinline__ float wave_sum(float x) { return x32_sum(x16_sum(row_sum(x))); }
__device__ __forceinline__ float ex2(float x) { return __builtin_amdgcn_exp2f(x); }
__device__ __forceinline__ float rcp(float x) { return __builtin_amdgcn_rcpf(x); }
__device__ __forceinline__ unsigned tg(float v, unsigned t8) { return ((asu(v) + 0x80u) & 0xffffff00u) | t8; }
__device__ __forceinline__ v4u tg4(f4 v, unsigned ta, unsigned tb) { return v4u{tg(v.x, ta), tg(v.y, tb), tg(v.z, ta), tg(v.w, tb)}; }
__device__ __forceinline__ float untg(unsigned u) { return asf(u & 0xffffff00u); }
__device__ __forceinline__ unsigned xoror(unsigned a, unsigned b, unsigned c) { return __builtin_amdgcn_bitop3_b32(a, b, c, 0xBE); }
__device__ __forceinline__ unsigned bad4(v4u u, unsigned ta, unsigned tb) { return xoror(u.x, ta, xoror(u.y, tb, xoror(u.z, ta, u.w ^ tb))) & 0xffu; }
__device__ __forceinline__ float red4(v4u a, v4u b) {
  const auto s01 = __builtin_amdgcn_permlane32_swap(asu(untg(a.x) + untg(b.x)), asu(untg(a.y) + untg(b.y)), false, false);
  const auto s23 = __builtin_amdgcn_permlane32_swap(asu(untg(a.z) + untg(b.z)), asu(untg(a.w) + untg(b.w)), false, false);
  const auto t = __builtin_amdgcn_permlane16_swap(asu(asf(s01[0]) + asf(s01[1])), asu(asf(s23[0]) + asf(s23[1])), false, false);
  float v = asf(t[0]) + asf(t[1]);
  v += dpp<ROR4>(v); v += dpp<ROR8>(v);
  return v;
}
__device__ __forceinline__ v4u trfrag(const LAS unsigned char* a, int off0, int off1) {
  const v2u lo = __builtin_bit_cast(v2u, __builtin_amdgcn_ds_read_tr16_b64_v4i16((LAS s4*)(a + off0)));
  const v2u hi = __builtin_bit_cast(v2u, __builtin_amdgcn_ds_read_tr16_b64_v4i16((LAS s4*)(a + off1)));
  return v4u{lo.x, lo.y, hi.x, hi.y};
}

#define SB() __builtin_amdgcn_sched_barrier(0)
#define FENCE() do { SB(); asm volatile("" ::: "memory"); SB(); } while (0)
#define LGKM0() do { SB(); asm volatile("s_waitcnt lgkmcnt(0)" ::: "memory"); SB(); } while (0)
#define STAMP(i)

template <int SLEEP> __device__ __forceinline__ void lds_wait(LAS unsigned char* L, int word, unsigned want) {
  FENCE();
  const volatile LAS unsigned* p = (const volatile LAS unsigned*)(L + L_FL) + word;
#pragma nounroll
  for (unsigned i = 0; i < LSPIN_CAP; ++i) {
    if ((unsigned)__builtin_amdgcn_readfirstlane(*p) >= want) break;
    if (SLEEP) __builtin_amdgcn_s_sleep(SLEEP);
  }
  FENCE();
}
template <int WORD> __device__ __forceinline__ void lds_signal(unsigned vsig) {
  SB();
  asm volatile("s_waitcnt lgkmcnt(0)\n\tds_add_u32 %0, %1 offset:%2" :: "v"(vsig), "v"(1u), "i"(WORD * 4) : "memory");
  SB();
}
template <int GS> __device__ __forceinline__ void dma4(rsrc_t r, int v0, int v1, int v2, int v3, int& so, unsigned& m0v) {
  asm volatile("s_mov_b32 m0, %1\n\ts_nop 0\n\t"
               "buffer_load_dwordx4 %2, %6, %0 offen nt lds\n\ts_add_u32 %0, %0, %7\n\t"
               "buffer_load_dwordx4 %3, %6, %0 offen offset:1024 nt lds\n\ts_add_u32 %0, %0, %7\n\t"
               "buffer_load_dwordx4 %4, %6, %0 offen offset:2048 nt lds\n\ts_add_u32 %0, %0, %7\n\t"
               "buffer_load_dwordx4 %5, %6, %0 offen offset:3072 nt lds\n\ts_add_u32 %0, %0, %8\n\ts_add_u32 %1, %1, 0x1000"
               : "+s"(so), "+s"(m0v) : "v"(v0), "v"(v1), "v"(v2), "v"(v3), "s"(r), "i"(GS - 1024), "i"(GS + 3072) : "memory", "scc");
}
struct Ld { unsigned cnt, nxt, last, vfull; };
__device__ __forceinline__ void pace(Ld& s, unsigned w4, unsigned to) {
  unsigned a, b, v;
  asm volatile("0:\n\t"
               "s_memtime vcc\n\t"
               "s_getreg_b32 %0, hwreg(HW_REG_IB_STS)\n\t"
               "s_and_b32 %1, %0, 15\n\t"
               "s_bfe_u32 %0, %0, 0x20016\n\t"
               "s_lshl4_add_u32 %0, %0, %1\n\t"
               "s_sub_u32 %1, %5, %0\n\t"
               "s_cmp_lt_i32 %1, %3\n\t"
               "s_cbranch_scc1 1f\n\t"
               "s_lshr_b32 %1, %1, 4\n\t"
               "v_mov_b32 %2, %1\n\t"
               "ds_write_b32 %6, %2\n\t"
               "s_lshl_b32 %1, %1, 4\n\t"
               "s_add_u32 %3, %1, 16\n"
               "1:\n\t"
               "s_waitcnt lgkmcnt(0)\n\t"
               "s_cmp_le_u32 %0, %7\n\t"
               "s_cbranch_scc1 2f\n\t"
               "s_sub_u32 %1, vcc_lo, %4\n\t"
               "s_cmp_ge_u32 %1, %8\n\t"
               "s_cselect_b32 %1, %9, 0\n\t"
               "s_cmp_le_u32 %0, %1\n\t"
               "s_cbranch_scc0 0b\n"
               "2:\n\t"
               "s_mov_b32 %4, vcc_lo"
               : "=&s"(a), "=&s"(b), "=&v"(v), "+s"(s.nxt), "+s"(s.last)
               : "s"(s.cnt), "v"(s.vfull), "s"(w4), "s"(to), "i"(WHI - 4) : "memory", "scc", "vcc");
}
template <int GS, int F0, int NF>
__device__ __forceinline__ void stream(LAS unsigned char* L, Ld& s, rsrc_t r, int v0, int v1, int v2, int v3, int so, int rot, unsigned lds0, unsigned& w4, unsigned& to) {
  volatile LAS unsigned* fl = (volatile LAS unsigned*)(L + L_FL);
  const int base = so;
#pragma nounroll
  for (int f = F0; f < F0 + NF; ++f) {
    unsigned m0v = lds0 + (f & 7) * 16384;
    if (NF == 8) so = base + ((f - F0 + rot) & 7) * 16384;
    if (F0 + NF > 8 && f >= 8) {
      const unsigned now = (unsigned)__builtin_amdgcn_s_memrealtime(), ref = __builtin_amdgcn_readfirstlane(fl[F_REF]);
      const int lag = (CTL && ref) ? (int)(now - ref) - (F0 < 20 ? TOFF + (f - 5) * TAU : TOFF + 15 * TAU + (f - 20) * TAU2) : 0;
      w4 = lag > TTH2 ? WBO - 4 : lag < -TTH ? WLO - 4 : WIN - 4;
      to = lag < -TTH ? TOLO : TOMID;
      lds_wait<LSLP>(L, F_D + (f < 12 ? (f & ~1) : f) - 8, f < 12 ? 4u : f < 20 ? 1u : 8u);
    }
#pragma nounroll
    for (int i = 0; i < 4; ++i) {
      pace(s, w4, to);
      dma4<GS>(r, v0, v1, v2, v3, so, m0v);
      s.cnt += 4;
    }
  }
}

__global__ void __launch_bounds__(NT)
fused_decode(const unsigned short* __restrict__ x, const void* __restrict__ kc, const void* __restrict__ vc, const void* __restrict__ wq,
             const void* __restrict__ wo, const void* __restrict__ w1, const void* __restrict__ b1, const void* __restrict__ w2,
             const void* __restrict__ b2, void* __restrict__ out, void* ws) {
  unsigned long long wq_early;
  asm volatile("s_load_dwordx2 %0, %1, 0x18" : "=s"(wq_early) : "s"(__builtin_amdgcn_kernarg_segment_ptr()));
  SB();
  __shared__ __attribute__((aligned(16))) unsigned char smem[LDS_BYTES];
  LAS unsigned char* const L = (LAS unsigned char*)smem;
  const int tid = threadIdx.x, lane = tid & 63, wv = __builtin_amdgcn_readfirstlane(tid >> 6), c = blockIdx.x;
  const int g = lane >> 4, n16 = lane & 15;
  const int grp = c & 7, sp = c >> 3, ab = grp >> 2, ahkv = grp & 3;
  const int ksq = c >> 6, cgq = c & 63;
  const bool has1 = grp != (sp & 7);
  const int c1 = sp * 7 + grp - (grp > (sp & 7) ? 1 : 0);
  const int ks1 = c1 / 56, cg1 = c1 % 56;
  const int cgo = sp & 7;
  const int ho = grp * 4 + (sp >> 3);
  const int ks2 = (sp >> 3) * 8 + grp;
  const bool has2 = ks2 < 28;

  const unsigned vsig = (unsigned)(size_t)smem + (lane == 0 ? L_FL : L_SINK + lane * 4);
  if (tid < F_WORDS) ((volatile LAS unsigned*)(L + L_FL))[tid] = 0u;
  const int r8 = lane >> 3, c8 = lane & 7;
  const int f0 = r8 & 2;
  int vq0 = r8 * 8192 + (c8 ^ f0) * 16, vq1 = vq0 ^ 64;
  int so = ksq * 256 * 8192 + cgq * 128;
  unsigned m0v = (unsigned)(size_t)smem;
  SB();
  asm volatile("s_waitcnt lgkmcnt(0)" : "+s"(wq_early), "+v"(vq0), "+v"(vq1), "+s"(so) :: "memory");
  const rsrc_t rq = mk((const void*)wq_early, WQB);
  if (wv == 0) {
    __builtin_amdgcn_s_setprio(3);
#pragma unroll
    for (int i = 0; i < WIN / 4; ++i) dma4<8 * 8192>(rq, vq0, vq1, vq0, vq1, so, m0v);
  }
  SB();
  __builtin_amdgcn_s_barrier();
  FENCE();
  if (wv >= 1 && wv < NWAVES - 1) {
    const int w = wv - 1;
    STAMP(0);
    const rsrc_t rws = mk(ws, WS_END);
    const rsrc_t rws2 = mk(ws, has2 ? WS_END : 0u);
    unsigned long long xl = (unsigned long long)x;
    const rsrc_t rxl = mk((const void*)xl, w < 2 ? 4096u : 0u);
    const v4u xa = __builtin_amdgcn_raw_buffer_load_b128(rxl, lane * 16, w * 2048, 0);
    const v4u xb = __builtin_amdgcn_raw_buffer_load_b128(rxl, lane * 16, w * 2048 + 1024, 0);
    FENCE();
    if (w < 2) {
      float xf[16];
#pragma unroll
      for (int i = 0; i < 4; ++i) { xf[2 * i] = blo(xa[i]); xf[2 * i + 1] = bhi(xa[i]); xf[8 + 2 * i] = blo(xb[i]); xf[9 + 2 * i] = bhi(xb[i]); }
      const float piv = asf(__builtin_amdgcn_readfirstlane(asu(xf[0])));
      float s1 = 0.f, s2 = 0.f;
#pragma unroll
      for (int i = 0; i < 16; ++i) { const float d = xf[i] - piv; s1 += d; s2 = fmaf(d, d, s2); }
      float a1 = wave_sum(s1) * (1.0f / 1024), a2 = wave_sum(s2) * (1.0f / 1024);
      float mean = rb(piv + a1), inv = rcp(rb(__builtin_amdgcn_sqrtf(rb(rb(fmaxf(a2 - a1 * a1, 0.f)) + 1e-5f))));
      v4u pa, pb;
      s1 = 0.f; s2 = 0.f;
#pragma unroll
      for (int i = 0; i < 8; ++i) {
        const unsigned t = pk(xf[2 * i] - mean, xf[2 * i + 1] - mean), u = pk(blo(t) * inv, bhi(t) * inv);
        if (i < 4) pa[i] = u; else pb[i - 4] = u;
        xf[2 * i] = blo(u); xf[2 * i + 1] = bhi(u);
        s1 += xf[2 * i] + xf[2 * i + 1]; s2 = fmaf(xf[2 * i], xf[2 * i], fmaf(xf[2 * i + 1], xf[2 * i + 1], s2));
      }
      *(LAS v4u*)(L + L_XS + w * 2048 + lane * 16) = pa;
      *(LAS v4u*)(L + L_XS + w * 2048 + 1024 + lane * 16) = pb;
      a1 = wave_sum(s1) * (1.0f / 1024); a2 = wave_sum(s2) * (1.0f / 1024);
      mean = rb(a1); inv = rcp(rb(__builtin_amdgcn_sqrtf(rb(rb(fmaxf(a2 - a1 * a1, 0.f)) + 1e-5f))));
#pragma unroll
      for (int i = 0; i < 8; ++i) {
        const unsigned t = pk(xf[2 * i] - mean, xf[2 * i + 1] - mean), u = pk(blo(t) * inv, bhi(t) * inv);
        if (i < 4) pa[i] = u; else pb[i - 4] = u;
      }
      *(LAS v4u*)(L + L_YS + w * 2048 + lane * 16) = pa;
      *(LAS v4u*)(L + L_YS + w * 2048 + 1024 + lane * 16) = pb;
      lds_signal<F_LN>(vsig);
    }
    const int q4 = n16 >> 2, p4 = lane & 3;
    lds_wait<LSLP>(L, F_LN, 3);
    const unsigned tag = __builtin_amdgcn_readfirstlane(((volatile LAS unsigned*)(L + L_FL))[F_TAG]);
    const unsigned ta = tag & 0xffu, tb = (tag >> 8) & 0xffu;
    STAMP(1);
    {
      const bool isq = w < 4;
      const LAS unsigned char* ap = L + (isq ? L_XS + ksq * 512 : L_YS + (ks1 & 3) * 512) + (lane & 1) * 2048 + g * 16;
      v4u A[8];
#pragma unroll
      for (int s = 0; s < 8; ++s) A[s] = *(const LAS v4u*)(ap + s * 64);
      lds_wait<LSLP>(L, F_FULL, isq ? 2 : 4);
      const LAS unsigned char* bp = L + (isq ? 0 : 32768) + (8 * g + q4) * 128 + (((w & 3) ^ ((q4 >> 1) | ((g & 1) << 1))) * 32) + p4 * 8;
      v4u Bf[8];
#pragma unroll
      for (int s = 0; s < 8; ++s) Bf[s] = trfrag(bp, s * 4096, s * 4096 + 512);
      lds_signal<F_DQ>(vsig + (isq ? 0 : (F_DH - F_DQ) * 4));
      f4 d0 = mfma32(Bf[0], A[0], f4{0.f, 0.f, 0.f, 0.f}), d1 = mfma32(Bf[1], A[1], f4{0.f, 0.f, 0.f, 0.f});
#pragma unroll
      for (int s = 2; s < 8; s += 2) { d0 = mfma32(Bf[s], A[s], d0); d1 = mfma32(Bf[s + 1], A[s + 1], d1); }
      const rsrc_t rp = mk(ws, (isq || has1) ? WS_END : 0u);
      const int pso = (isq ? WS_QP + cgq * 2048 + ksq * 128 : WS_HP + cg1 * 2048 + ks1 * 128) + (w & 3) * 512;
      stc4(rp, n16 < 2 ? g * 32 + n16 * 16 : OOB, pso, tg4(d0 + d1, ta, tb));
    }
    STAMP(2);
    v4u K[16];
    {
      const LAS unsigned char* kp = L + ((4 + w) & 7) * 16384 + n16 * 256;
      lds_wait<1>(L, F_FULL, 5 + w);
#pragma unroll
      for (int s = 0; s < 4; ++s) {
        const LAS unsigned char* kq = kp + (((4 * s + g) ^ n16) * 16);
#pragma unroll
        for (int t = 0; t < 4; ++t) K[4 * t + s] = *(const LAS v4u*)(kq + t * 4096);
      }
      lds_signal<F_DK>(vsig + w * 4);
    }
    STAMP(3);
    lds_wait<1>(L, F_Q, 1);
    {
      const LAS unsigned char* qb = L + L_QS + (n16 >> 1) * 272 + g * 16;
      v4u qf[4];
#pragma unroll
      for (int s = 0; s < 4; ++s) qf[s] = *(const LAS v4u*)(qb + s * 64);
      f4 sc[4];
#pragma unroll
      for (int t = 0; t < 4; ++t) sc[t] = mfma32(K[4 * t], qf[0], f4{0.f, 0.f, 0.f, 0.f});
#pragma unroll
      for (int s = 1; s < 4; ++s)
#pragma unroll
        for (int t = 0; t < 4; ++t) sc[t] = mfma32(K[4 * t + s], qf[s], sc[t]);
#pragma unroll
      for (int t = 0; t < 4; ++t)
#pragma unroll
        for (int r = 0; r < 4; r += 2) {
          const unsigned u = pk(sc[t][r], sc[t][r + 1]), v = pk(blo(u) * 0.088388347648318440f, bhi(u) * 0.088388347648318440f);
          sc[t][r] = blo(v); sc[t][r + 1] = bhi(v);
        }
      float mw = fmaxf(fmaxf(sc[0].x, sc[0].y), fmaxf(sc[0].z, sc[0].w));
#pragma unroll
      for (int t = 1; t < 4; ++t) mw = fmaxf(mw, fmaxf(fmaxf(sc[t].x, sc[t].y), fmaxf(sc[t].z, sc[t].w)));
      mw = x32_max(x16_max(mw)) * LOG2E;
      float lw = 0.f;
      const unsigned pm = (lane & 1) ? 0xffff0000u : 0u;
      unsigned P[8];
#pragma unroll
      for (int t = 0; t < 4; ++t)
#pragma unroll
        for (int r = 0; r < 4; r += 2) {
          const float v0 = ex2(fmaf(sc[t][r], LOG2E, -mw)), v1 = ex2(fmaf(sc[t][r + 1], LOG2E, -mw));
          lw += v0 + v1;
          const unsigned h = pk(v0, v1);
          P[2 * t + (r >> 1)] = pk(v0 - asf((h << 16) & pm), v1 - asf(h & pm));
        }
      *(LAS v4u*)(L + L_P + w * 2048 + lane * 16) = v4u{P[0], P[1], P[2], P[3]};
      *(LAS v4u*)(L + L_P + w * 2048 + 1024 + lane * 16) = v4u{P[4], P[5], P[6], P[7]};
      lw = x32_sum(x16_sum(lw));
      if (lane < 16 && !(lane & 1)) *(LAS f2*)(L + L_ST + (w * 8 + (lane >> 1)) * 8) = f2{mw, lw};
      lds_signal<F_P>(vsig);
    }
    STAMP(4);
    lds_wait<1>(L, F_P, 8);
    float o0 = 0.f, o1 = 0.f;
    {
      f4 st[8];
#pragma unroll
      for (int j = 0; j < 8; ++j) st[j] = *(const LAS f4*)(L + L_ST + (j * 8 + 2 * g) * 8);
      float M0 = st[0].x, M1 = st[0].z;
#pragma unroll
      for (int j = 1; j < 8; ++j) { M0 = fmaxf(M0, st[j].x); M1 = fmaxf(M1, st[j].z); }
      M0 = __builtin_ceilf(M0); M1 = __builtin_ceilf(M1);
#pragma unroll
      for (int j = 0; j < 8; ++j) { st[j].x = ex2(st[j].x - M0); st[j].z = ex2(st[j].z - M1); }
      STAMP(5);
      int ovs = (lane & 2) ? OOB : (2 * g + (lane & 1)) * 32768 + (n16 >> 2) * 16;
      asm volatile("" : "+v"(ovs));
      const LAS unsigned char* vp = L + (4 * g + q4) * 256 + ((w ^ (4 * (g & 1) + q4)) * 32) + p4 * 8;
#pragma unroll
      for (int j = 0; j < 8; ++j) {
        const v4u pa = *(const LAS v4u*)(L + L_P + j * 2048 + lane * 16), pb = *(const LAS v4u*)(L + L_P + j * 2048 + 1024 + lane * 16);
        if (j < 7) lds_wait<LSLP>(L, F_FULL, 13 + j); else lds_wait<0>(L, F_FULL, 20);
        const LAS unsigned char* vq = vp + ((4 + j) & 7) * 16384;
        const v4u va = trfrag(vq, 0, 4096), vb = trfrag(vq, 8192, 12288);
        if (j == 0) lds_signal<F_DV>(vsig); else if (j == 1) lds_signal<F_DV + 1>(vsig); else if (j == 2) lds_signal<F_DV + 2>(vsig); else if (j == 3) lds_signal<F_DV + 3>(vsig);
        f4 d = mfma32(pa, va, f4{0.f, 0.f, 0.f, 0.f});
        d = mfma32(pb, vb, d);
        o0 = fmaf(st[j].x, d.x + d.y, o0);
        o1 = fmaf(st[j].z, d.z + d.w, o1);
      }
      const bool odd = (lane & 1) != 0;
      const float rcv = dpp<XOR1>(odd ? o0 : o1);
      const unsigned gr = odd ? pk(rcv, o1) : pk(o0, rcv);
      const unsigned gn = dppu<XOR2>(gr);
      stc4(rws, ovs, WS_OP + ((ahkv * 64 + w) * 32 + sp) * 128 + ab * 64, v4u{gr, tag, gn, tag});
    }
    STAMP(6);
    const LAS unsigned char* wp = L + (8 * g + q4) * 256 + ((w ^ (q4 | ((g & 1) << 2))) * 32) + p4 * 8;
    const int fvo = (g >> 1) * 2048 + (g & 1) * 32;
    v4u MPv;
    {
      v4u A[4];
      lds_wait<1>(L, F_H, 1);
#pragma unroll
      for (int s = 0; s < 4; ++s) A[s] = *(const LAS v4u*)(L + L_HS + (lane & 1) * 272 + s * 64 + g * 16);
      lds_wait<LSLP>(L, F_FULL, 22);
      f4 d = {0.f, 0.f, 0.f, 0.f};
#pragma unroll
      for (int s = 0; s < 4; ++s) d = mfma32(trfrag(wp + 4 * 16384, s * 8192, s * 8192 + 1024), A[s], d);
      MPv = tg4(d, ta, tb);
      if (!MPLATE) stc4(rws2, n16 < 2 ? fvo + n16 * 16 : OOB, WS_MP + ((cgo * 16 + 2 * w) * 32 + ks2) * 64, MPv);
    }
    STAMP(7);
    {
      const int ovo = ((n16 >> 2) & 1) * 2048 + g * 512 + (n16 >> 3) * 64 + (lane & 3) * 16, oso = WS_OP + (ho * 8 + w) * 4096;
      v4u G[4];
      lds_wait<0>(L, F_FULL, OGATE);
#pragma unroll
      for (int j = 0; j < 4; ++j) G[j] = ldc4(rws, ovo + j * 128, oso);
      SB();
      if (MPLATE) stc4(rws2, n16 < 2 ? fvo + n16 * 16 : OOB, WS_MP + ((cgo * 16 + 2 * w) * 32 + ks2) * 64, MPv);
      SB();
      lds_wait<0>(L, F_W, 1);
      v2u WF = *(const LAS v2u*)(L + L_WT + (lane & 3) * 32 + g * 8);
      float invl = ((const LAS float*)(L + L_WT + 128))[(lane >> 1) & 1];
      LAS unsigned char* osw = L + (n16 == g ? L_OS + g * 272 + w * 32 : L_QS + lane * 32);
      const LAS unsigned char* osr = L + L_OS + (lane & 3) * 272 + g * 16;
      const LAS unsigned char* wop = wp + 6 * 16384;
      int fv2 = (n16 & 13) == 0 ? fvo + (n16 >> 1) * 16 : OOB, fso = WS_FP + ((cgo * 16 + 2 * w) * 32 + ho) * 64;
      unsigned vta = ta, vtb = tb, vtag = tag;
      asm volatile("" : "+v"(WF), "+v"(invl), "+v"(osw), "+v"(osr), "+v"(wop), "+v"(fv2), "+s"(fso), "+v"(vta), "+v"(vtb), "+v"(vtag));
      lds_wait<0>(L, F_FULL, 24);
      v4u Bo[4];
#pragma unroll
      for (int s = 0; s < 4; ++s) Bo[s] = trfrag(wop, s * 8192, s * 8192 + 1024);
      SB();
      STAMP(8);
      const f4 z = {0.f, 0.f, 0.f, 0.f};
      f4 e0, e1, e2, e3;
#define COMBINE() do { \
        e0 = mfma16(v2u{plo(G[0].x, G[1].x), plo(G[2].x, G[3].x)}, WF, z); e1 = mfma16(v2u{phi(G[0].x, G[1].x), phi(G[2].x, G[3].x)}, WF, z); \
        e2 = mfma16(v2u{plo(G[0].z, G[1].z), plo(G[2].z, G[3].z)}, WF, z); e3 = mfma16(v2u{phi(G[0].z, G[1].z), phi(G[2].z, G[3].z)}, WF, z); } while (0)
#define OBAD(j, acc) xoror(G[j].y, vtag, xoror(G[j].w, vtag, acc))
      COMBINE();
      SB();
      if (__builtin_expect(__any((OBAD(0, OBAD(1, 0u)) | OBAD(2, OBAD(3, 0u))) != 0u), 0)) {
#pragma nounroll
        for (unsigned spin = 0; spin < SPIN_CAP; ++spin) {
          const bool m0 = __any(OBAD(0, 0u) != 0u), m1 = __any(OBAD(1, 0u) != 0u), m2 = __any(OBAD(2, 0u) != 0u), m3 = __any(OBAD(3, 0u) != 0u);
          if (!(m0 || m1 || m2 || m3)) break;
          FENCE();
          if (m0) G[0] = ldc4(rws, ovo, oso);
          if (m1) G[1] = ldc4(rws, ovo + 128, oso);
          if (m2) G[2] = ldc4(rws, ovo + 256, oso);
          if (m3) G[3] = ldc4(rws, ovo + 384, oso);
        }
        COMBINE();
      }
      STAMP(9);
      *(LAS v4u*)osw = v4u{pk(e0.x, e1.x), pk(e2.x, e3.x), pk(e0.y, e1.y), pk(e2.y, e3.y)};
      *(LAS v4u*)(osw + 16) = v4u{pk(e0.z, e1.z), pk(e2.z, e3.z), pk(e0.w, e1.w), pk(e2.w, e3.w)};
      LGKM0();
      __builtin_amdgcn_s_barrier();
      FENCE();
      v4u a0 = *(const LAS v4u*)osr, a1 = *(const LAS v4u*)(osr + 64), a2 = *(const LAS v4u*)(osr + 128), a3 = *(const LAS v4u*)(osr + 192);
      SB();
      f4 d = mfma32(Bo[0], a0, z), d2 = mfma32(Bo[1], a1, z);
      d = mfma32(Bo[2], a2, d); d2 = mfma32(Bo[3], a3, d2);
      d += d2;
      d.x += dpp<XOR1>(d.x); d.y += dpp<XOR1>(d.y); d.z += dpp<XOR1>(d.z); d.w += dpp<XOR1>(d.w);
      d *= invl;
      stc4(rws, fv2, fso, v4u{(asu(d.x) & 0xffffff00u) | vta, (asu(d.y) & 0xffffff00u) | vtb, (asu(d.z) & 0xffffff00u) | vta, (asu(d.w) & 0xffffff00u) | vtb});
    }
    STAMP(10);
  } else if (wv == NWAVES - 1) {
    STAMP(0);
    volatile LAS unsigned* fl = (volatile LAS unsigned*)(L + L_FL);
    const rsrc_t rws = mk(ws, WS_END), rws1 = mk(ws, has1 ? WS_END : 0u), rws2 = mk(ws, has2 ? WS_END : 0u), rout = mk(out, 4096);
    const unsigned eraw = __builtin_amdgcn_raw_buffer_load_b32(rws, 0, WS_E, AUX_SC1);
    const unsigned b1p = __builtin_amdgcn_raw_buffer_load_b32(mk(b1, has1 ? 7168u : 0u), (n16 >> 2) * 8 + (lane & 1) * 4, c1 * 32, 0);
    const int col2 = ((lane >> 1) & 1) * 8 + ((g >> 1) | ((g & 1) << 1)) * 2;
    const int fo = (lane & 1) * 2048 + col2;
    const unsigned short xfin = __builtin_amdgcn_raw_buffer_load_b16(mk(x, c < 128 ? 4096u : 0u), fo, c * 16, 0);
    const unsigned short b2fin = __builtin_amdgcn_raw_buffer_load_b16(mk(b2, c < 128 ? 2048u : 0u), col2, c * 16, 0);
    const v4u codepf = __builtin_amdgcn_raw_buffer_load_b128(mk((const void*)((__builtin_amdgcn_s_getpc() & ~255ull) - PFB * 1024u), PF * 1024u), lane * 16, sp * 1024, 0);
    FENCE();
    const unsigned tag = __builtin_amdgcn_readfirstlane(eraw) * 0x9E3779B1u + 0x7F4A7C15u;
    const unsigned ta = tag & 0xffu, tb = (tag >> 8) & 0xffu;
    fl[F_TAG] = tag;
    lds_signal<F_LN>(vsig);
    asm volatile("" :: "v"(codepf));
    lds_wait<1>(L, F_FULL, 4);
    const unsigned t4 = (unsigned)__builtin_amdgcn_s_memrealtime();
    lds_wait<LSLP>(L, F_FULL, S1GATE);
    {
      const int fvo = (lane < 16 && !(lane & 1)) ? (lane >> 2) * 16 : OOB, fb = (lane >> 1) & 1;
      bool needq = true, needh = has1;
#pragma nounroll
      for (unsigned spin = 0; (needq || needh) && spin < SPIN_CAP; ++spin) {
        const v2u GQ = ldc2(rws, needq ? lane * 8 : OOB, WS_QP + c * 512), GH = ldc2(rws1, needh ? lane * 8 : OOB, WS_HP + c1 * 512);
        if (needq && !__any((((GQ.x ^ ta) | (GQ.y ^ tb)) & 0xffu) != 0u)) {
          const unsigned gr = pk(x32_sum(x16_sum(untg(GQ.x))), x32_sum(x16_sum(untg(GQ.y)))), gn = dppu<XOR1>(gr), ty = (tag << 12) | (t4 & 0xfffu);
          stc4(rws, fvo + fb * 16384, WS_QF + c * 64, v4u{gr, ty, gn, ty});
          needq = false;
          STAMP(13);
        }
        if (needh && !__any((((GH.x ^ ta) | (GH.y ^ tb)) & 0xffu) != 0u)) {
          float h0 = rb(rb(x32_sum(x16_sum(untg(GH.x)))) + blo(b1p)), h1 = rb(rb(x32_sum(x16_sum(untg(GH.y)))) + bhi(b1p));
          constexpr float GC = -2.0f * 0.7978845608028654f * 1.4426950408889634f;
          h0 = h0 * rcp(1.0f + ex2(GC * (h0 + 0.044715f * h0 * h0 * h0)));
          h1 = h1 * rcp(1.0f + ex2(GC * (h1 + 0.044715f * h1 * h1 * h1)));
          const unsigned gr = pk(h0, h1), gn = dppu<XOR1>(gr);
          stc4(rws, fvo + fb * 14336, WS_HF + c1 * 64, v4u{gr, tag, gn, tag});
          needh = false;
          STAMP(14);
        }
        __builtin_amdgcn_s_sleep(PSLP);
      }
    }
    lds_wait<LSLP>(L, F_FULL, QGATE);
    {
      v4u Q[4];
      const unsigned t20 = tag & 0xfffffu;
#pragma nounroll
      for (unsigned spin = 0; spin < SPIN_CAP; ++spin) {
        unsigned bad = 0;
#pragma unroll
        for (int i = 0; i < 4; ++i) Q[i] = ldc4(rws, lane * 16, WS_QF + grp * 4096 + i * 1024);
#pragma unroll
        for (int i = 0; i < 4; ++i) bad |= ((Q[i].y >> 12) ^ t20) | ((Q[i].w >> 12) ^ t20);
        if (!__any(bad != 0u)) break;
        __builtin_amdgcn_s_sleep(PSLP);
      }
#pragma unroll
      for (int i = 0; i < 4; ++i) *(LAS v2u*)(L + L_QS + (2 * i + (lane >> 5)) * 272 + (lane & 31) * 8) = v2u{Q[i].x, Q[i].z};
      lds_signal<F_Q>(vsig);
      STAMP(15);
      if (CTL) {
        int d = 0;
#pragma unroll
        for (int i = 0; i < 4; ++i) d += (int)((Q[i].y - t4) << 20) >> 20;
        fl[F_REF] = (t4 + (int)(wave_sum((float)d) * (1.0f / 256))) | 1u;
      }
    }
    lds_wait<1>(L, F_P, 8);
    {
      f2 sv[8];
#pragma unroll
      for (int j = 0; j < 8; ++j) sv[j] = *(const LAS f2*)(L + L_ST + (j * 8 + (lane & 7)) * 8);
      float M = sv[0].x, Ls = 0.f;
#pragma unroll
      for (int j = 1; j < 8; ++j) M = fmaxf(M, sv[j].x);
      M = __builtin_ceilf(M);
#pragma unroll
      for (int j = 0; j < 8; ++j) Ls = fmaf(ex2(sv[j].x - M), sv[j].y, Ls);
      stc4(rws, lane < 8 ? (lane >> 2) * 2048 + (lane & 3) * 16 : OOB, WS_ST + grp * 4096 + sp * 64, v4u{asu(M), tag, asu(Ls), tag});
      const unsigned tag2 = has2 ? tag : 0u;
      v4u H;
#pragma nounroll
      for (unsigned spin = 0; spin < SPIN_CAP; ++spin) {
        H = ldc4(rws2, (lane >> 5) * 14336 + (lane & 31) * 16, WS_HF + ks2 * 512);
        if (!__any(((H.y ^ tag2) | (H.w ^ tag2)) != 0u)) break;
        __builtin_amdgcn_s_sleep(PSLP);
      }
      *(LAS v2u*)(L + L_HS + (lane >> 5) * 272 + (lane & 31) * 8) = v2u{H.x, H.z};
      lds_signal<F_H>(vsig);
      STAMP(16);
    }
    lds_wait<LSLP>(L, F_FULL, SGATE);
    {
      v4u SG;
#pragma nounroll
      for (unsigned spin = 0; spin < SPIN_CAP; ++spin) {
        SG = ldc4(rws, (lane >> 5) * 16384 + (lane & 31) * 64, WS_ST + (ho >> 2) * 2048 + (ho & 3) * 16);
        if (!__any(((SG.y ^ tag) | (SG.w ^ tag)) != 0u)) break;
        __builtin_amdgcn_s_sleep(PSLP);
      }
      const float m = asf(SG.x), M = x16_max(row_max(m));
      const float e = rb(ex2(m - M)), Ls = x16_sum(row_sum(e * asf(SG.z)));
      ((LAS unsigned short*)(L + L_WT))[lane] = (unsigned short)(asu(e) >> 16);
      ((LAS float*)(L + L_WT + 128))[lane >> 5] = rcp(Ls) * (1.0f + 1.0f / 131072);
      lds_signal<F_W>(vsig);
      STAMP(17);
    }
    {
      const rsrc_t rwf = mk(ws, c < 128 ? WS_END : 0u);
      const unsigned mlive = lane < 48 ? 0xffffffffu : 0u;
      __builtin_amdgcn_s_barrier();
      FENCE();
      if (c < 128) {
        int so = WS_MP + c * 2048, ov = OOB, ev = OOB, pass = 0;
        unsigned live = mlive;
        float gt = 0.f;
        const float xv = blo(xfin), bv = blo(b2fin);
        asm volatile("" : "+s"(pass));
#pragma nounroll
        for (;;) {
          v4u A0 = ldc4(rwf, lane * 16, so), A1 = ldc4(rwf, lane * 16, so + 1024);
#pragma nounroll
          for (unsigned spin = 0; spin < SPIN_CAP; ++spin) {
            const bool m0 = __any(bad4(A0, ta, tb) != 0u), m1 = __any((bad4(A1, ta, tb) & live) != 0u);
            if (!(m0 || m1)) break;
            FENCE();
            if (m0) A0 = ldc4(rwf, lane * 16, so);
            if (m1) A1 = ldc4(rwf, lane * 16, so + 1024);
          }
          if (pass) { STAMP(25); }
          const float r = rb(red4(A0, A1 & live));
          const float o = xv + rb(r * gt);
          __builtin_amdgcn_raw_buffer_store_b16((unsigned short)pk(o, o), rout, ov, c * 16, OAUX);
          __builtin_amdgcn_raw_buffer_store_b32(tag, rws, ev, WS_E, EAUX);
          SB();
          if (pass) break;
          const float m = rb(r + bv);
          gt = rb(m * rcp(1.0f + ex2(-1.4426950408889634f * m)));
          pass = 1; so = WS_FP + c * 2048; live = 0xffffffffu;
          ov = (lane & 12) == 0 ? fo : OOB; ev = (c | lane) == 0 ? 0 : OOB;
          asm volatile("" : "+s"(pass), "+s"(so), "+v"(live), "+v"(ov), "+v"(ev), "+v"(gt));
        }
        STAMP(26);
      }
    }
  } else {
    STAMP(0);
    volatile LAS unsigned* fl = (volatile LAS unsigned*)(L + L_FL);
    const unsigned lds0 = (unsigned)(size_t)smem;
    Ld st = {WIN, 16, 0, lds0 + L_FL + F_FULL * 4};
    unsigned w4 = WIN - 4, to = 0x7fffffffu;
#pragma nounroll
    for (int i = WIN / 4; i < 8; ++i) { pace(st, w4, to); dma4<8 * 8192>(rq, vq0, vq1, vq0, vq1, so, m0v); st.cnt += 4; }
    to = TOMID;
    {
      const int v0 = r8 * 7168 + (c8 ^ f0) * 16;
      stream<8 * 7168, 2, 2>(L, st, mk(w1, has1 ? W1B : 0u), v0, v0 ^ 64, v0, v0 ^ 64, ks1 * 256 * 7168 + cg1 * 128, 0, lds0, w4, to);
    }
    const int kvb = (grp * 16384 + sp * 512) * 256;
    {
      const int v0 = g * 256 + (n16 ^ g) * 16;
      stream<1024, 4, 8>(L, st, mk(kc, KVB), v0, v0 ^ 64, v0 ^ 128, v0 ^ 192, kvb, sp, lds0, w4, to);
    }
    {
      const int v0 = g * 256 + (n16 ^ (2 * g)) * 16;
      stream<1024, 12, 8>(L, st, mk(vc, KVB), v0, v0 ^ 128, v0, v0 ^ 128, kvb, sp, lds0, w4, to);
    }
    {
      const int v0 = g * 2048 + (n16 ^ (2 * g)) * 16;
      stream<8192, 20, 2>(L, st, mk(w2, has2 ? W1B : 0u), v0, v0, v0 ^ 128, v0 ^ 128, ks2 * 128 * 2048 + cgo * 256, 0, lds0, w4, to);
      stream<8192, 22, 2>(L, st, mk(wo, WQB), v0, v0, v0 ^ 128, v0 ^ 128, ho * 128 * 2048 + cgo * 256, 0, lds0, w4, to);
    }
    pace(st, 0u, 0x7fffffffu);
    asm volatile("s_waitcnt vmcnt(0)" ::: "memory");
    fl[F_FULL] = 24;
  }
}

}

extern "C" void kernel_launch(void* const* d_in, const int* in_sizes, int n_in, void* d_out, int out_size, void* d_ws, size_t ws_size,
                              hipStream_t stream) {
  (void)in_sizes; (void)n_in; (void)out_size; (void)ws_size;
  fused_decode<<<256, NT, 0, stream>>>((const unsigned short*)d_in[0], d_in[1], d_in[2], d_in[3], d_in[4], d_in[6], d_in[7], d_in[8], d_in[9],
                                       d_out, d_ws);
}
```

```cpp
#include <hip/hip_runtime.h>
#include <hip/hip_bf16.h>

namespace {

typedef unsigned v4u __attribute__((ext_vector_type(4)));
typedef unsigned v2u __attribute__((ext_vector_type(2)));
typedef float f4 __attribute__((ext_vector_type(4)));
typedef float f2 __attribute__((ext_vector_type(2)));
typedef __bf16 bf8 __attribute__((ext_vector_type(8)));
typedef __bf16 bf2 __attribute__((ext_vector_type(2)));
typedef short s4 __attribute__((ext_vector_type(4)));
typedef __amdgpu_buffer_rsrc_t rsrc_t;
#define LAS __attribute__((address_space(3)))

constexpr int NWAVES = 10;
constexpr int NT = 64 * NWAVES;
constexpr unsigned KVB = 2u * 4 * 16384 * 128 * 2, WQB = 1024u * 4096 * 2, W1B = 1024u * 3584 * 2;
constexpr unsigned WS_E = 0;
constexpr unsigned WS_QP = 4096;
constexpr unsigned WS_HP = WS_QP + 131072;
constexpr unsigned WS_QF = WS_HP + 114688;
constexpr unsigned WS_HF = WS_QF + 32768;
constexpr unsigned WS_ST = WS_HF + 28672;
constexpr unsigned WS_OP = WS_ST + 32768;
constexpr unsigned WS_FP = WS_OP + 1048576;
constexpr unsigned WS_MP = WS_FP + 262144;
constexpr unsigned WS_END = WS_MP + 262144;
constexpr int L_P = 131072, L_XS = L_P, L_YS = L_P + 4096, L_QS = L_P + 16384, L_HS = L_QS + 8 * 272, L_OS = L_HS + 2 * 272,
              L_ST = L_OS + 4 * 272, L_WT = L_ST + 512, L_FL = L_WT + 256, L_SINK = L_FL + 128, L_STAMP = L_SINK + 512, L_END = L_STAMP + NWAVES * 256;
constexpr int LDS_BYTES = L_END;
static_assert(L_END <= 163840 && (L_HS % 16) == 0 && (L_OS % 16) == 0 && (L_ST % 16) == 0 && (L_WT % 16) == 0 && (L_FL % 16) == 0, "lds map");
enum { F_FULL = 0, F_LN = 1, F_Q = 2, F_P = 3, F_H = 4, F_REF = 5, F_TAG = 6, F_W = 7, F_D = 8, F_DQ = F_D, F_DH = F_D + 2, F_DK = F_D + 4, F_DV = F_D + 12, F_WORDS = 32 };
constexpr int OOB = 0x7ff00000;
#define AUXL 16
#define AUXS 16
constexpr int AUX_SC1 = 16;
constexpr unsigned SPIN_CAP = 1u << 15, LSPIN_CAP = 1u << 20;
constexpr float LOG2E = 1.4426950408889634f;
#define LSLP 2
#define QGATE 10
#define OGATE 23
#define PSLP 2
#define PF 14
#define WLO 16
#define WHI 36
#define TAU 56
#define TOFF 15
#define TTH 20
#define TOMID 330
#define TOLO 700
#define TAU2 46
#define PROP 0
#define TPROP 16
#define TOHI 200
#define S1GATE 5
#define SGATE 19
#define MPLATE 0
#define PFB 3
#define OPF 1
#define OAUX 16
#define EAUX 16
#define FLATE 1
#define FSLP0 0
#define FSLP 0
#define WBO WHI
#define TTH2 TTH
#define CTL 1
#define WIN 24

__device__ __forceinline__ rsrc_t mk(const void* p, unsigned bytes) { return __builtin_amdgcn_make_buffer_rsrc((void*)p, 0, bytes, 0x00020000); }
__device__ __forceinline__ v4u ldc4(rsrc_t r, int vo, int so) { return __builtin_amdgcn_raw_buffer_load_b128(r, vo, so, AUXL); }
__device__ __forceinline__ v2u ldc2(rsrc_t r, int vo, int so) { return __builtin_amdgcn_raw_buffer_load_b64(r, vo, so, AUXL); }
__device__ __forceinline__ void stc4(rsrc_t r, int vo, int so, v4u v) { __builtin_amdgcn_raw_buffer_store_b128(v, r, vo, so, AUXS); }
__device__ __forceinline__ float asf(unsigned u) { return __builtin_bit_cast(float, u); }
__device__ __forceinline__ unsigned asu(float f) { return __builtin_bit_cast(unsigned, f); }
__device__ __forceinline__ unsigned pk(float a, float b) { f2 v = {a, b}; return __builtin_bit_cast(unsigned, __builtin_convertvector(v, bf2)); }
__device__ __forceinline__ float blo(unsigned u) { return asf(u << 16); }
__device__ __forceinline__ float bhi(unsigned u) { return asf(u & 0xffff0000u); }
__device__ __forceinline__ float rb(float a) { return blo(pk(a, a)); }
__device__ __forceinline__ unsigned plo(unsigned a, unsigned b) { return __builtin_amdgcn_perm(b, a, 0x05040100u); }
__device__ __forceinline__ unsigned phi(unsigned a, unsigned b) { return __builtin_amdgcn_perm(b, a, 0x07060302u); }
__device__ __forceinline__ f4 mfma32(v4u a, v4u b, f4 c) { return __builtin_amdgcn_mfma_f32_16x16x32_bf16(__builtin_bit_cast(bf8, a), __builtin_bit_cast(bf8, b), c, 0, 0, 0); }
__device__ __forceinline__ f4 mfma16(v2u a, v2u b, f4 c) { return __builtin_amdgcn_mfma_f32_16x16x16bf16_1k(__builtin_bit_cast(s4, a), __builtin_bit_cast(s4, b), c, 0, 0, 0); }
template <int C> __device__ __forceinline__ float dpp(float x) { return asf(__builtin_amdgcn_mov_dpp(asu(x), C, 0xf, 0xf, true)); }
template <int C> __device__ __forceinline__ unsigned dppu(unsigned x) { return __builtin_amdgcn_mov_dpp(x, C, 0xf, 0xf, true); }
constexpr int XOR1 = 0xB1, XOR2 = 0x4E, ROR4 = 0x124, ROR8 = 0x128;
__device__ __forceinline__ float x16_sum(float x) { auto s = __builtin_amdgcn_permlane16_swap(asu(x), asu(x), false, false); return asf(s[0]) + asf(s[1]); }
__device__ __forceinline__ float x32_sum(float x) { auto s = __builtin_amdgcn_permlane32_swap(asu(x), asu(x), false, false); return asf(s[0]) + asf(s[1]); }
__device__ __forceinline__ float x16_max(float x) { auto s = __builtin_amdgcn_permlane16_swap(asu(x), asu(x), false, false); return fmaxf(asf(s[0]), asf(s[1])); }
__device__ __forceinline__ float x32_max(float x) { auto s = __builtin_amdgcn_permlane32_swap(asu(x), asu(x), false, false); return fmaxf(asf(s[0]), asf(s[1])); }
__device__ __forceinline__ float row_sum(float x) { x += dpp<XOR1>(x); x += dpp<XOR2>(x); x += dpp<ROR4>(x); x += dpp<ROR8>(x); return x; }
__device__ __forceinline__ float row_max(float x) { x = fmaxf(x, dpp<XOR1>(x)); x = fmaxf(x, dpp<XOR2>(x)); x = fmaxf(x, dpp<ROR4>(x)); x = fmaxf(x, dpp<ROR8>(x)); return x; }
__device__ __forceinline__ float wave_sum(float x) { return x32_sum(x16_sum(row_sum(x))); }
__device__ __forceinline__ float ex2(float x) { return __builtin_amdgcn_exp2f(x); }
__device__ __forceinline__ float rcp(float x) { return __builtin_amdgcn_rcpf(x); }
__device__ __forceinline__ unsigned tg(float v, unsigned t8) { return ((asu(v) + 0x80u) & 0xffffff00u) | t8; }
__device__ __forceinline__ v4u tg4(f4 v, unsigned ta, unsigned tb) { return v4u{tg(v.x, ta), tg(v.y, tb), tg(v.z, ta), tg(v.w, tb)}; }
__device__ __forceinline__ float untg(unsigned u) { return asf(u & 0xffffff00u); }
__device__ __forceinline__ unsigned xoror(unsigned a, unsigned b, unsigned c) { return __builtin_amdgcn_bitop3_b32(a, b, c, 0xBE); }
__device__ __forceinline__ unsigned bad4(v4u u, unsigned ta, unsigned tb) { return xoror(u.x, ta, xoror(u.y, tb, xoror(u.z, ta, u.w ^ tb))) & 0xffu; }
__device__ __forceinline__ float red4(v4u a, v4u b) {
  const auto s01 = __builtin_amdgcn_permlane32_swap(asu(untg(a.x) + untg(b.x)), asu(untg(a.y) + untg(b.y)), false, false);
  const auto s23 = __builtin_amdgcn_permlane32_swap(asu(untg(a.z) + untg(b.z)), asu(untg(a.w) + untg(b.w)), false, false);
  const auto t = __builtin_amdgcn_permlane16_swap(asu(asf(s01[0]) + asf(s01[1])), asu(asf(s23[0]) + asf(s23[1])), false, false);
  float v = asf(t[0]) + asf(t[1]);
  v += dpp<ROR4>(v); v += dpp<ROR8>(v);
  return v;
}
__device__ __forceinline__ v4u trfrag(const LAS unsigned char* a, int off0, int off1) {
  const v2u lo = __builtin_bit_cast(v2u, __builtin_amdgcn_ds_read_tr16_b64_v4i16((LAS s4*)(a + off0)));
  const v2u hi = __builtin_bit_cast(v2u, __builtin_amdgcn_ds_read_tr16_b64_v4i16((LAS s4*)(a + off1)));
  return v4u{lo.x, lo.y, hi.x, hi.y};
}

#define SB() __builtin_amdgcn_sched_barrier(0)
#define FENCE() do { SB(); asm volatile("" ::: "memory"); SB(); } while (0)
#define LGKM0() do { SB(); asm volatile("s_waitcnt lgkmcnt(0)" ::: "memory"); SB(); } while (0)
#define STAMP(i)

template <int SLEEP> __device__ __forceinline__ void lds_wait(LAS unsigned char* L, int word, unsigned want) {
  FENCE();
  const volatile LAS unsigned* p = (const volatile LAS unsigned*)(L + L_FL) + word;
#pragma nounroll
  for (unsigned i = 0; i < LSPIN_CAP; ++i) {
    if ((unsigned)__builtin_amdgcn_readfirstlane(*p) >= want) break;
    if (SLEEP) __builtin_amdgcn_s_sleep(SLEEP);
  }
  FENCE();
}
template <int WORD> __device__ __forceinline__ void lds_signal(unsigned vsig) {
  SB();
  asm volatile("s_waitcnt lgkmcnt(0)\n\tds_add_u32 %0, %1 offset:%2" :: "v"(vsig), "v"(1u), "i"(WORD * 4) : "memory");
  SB();
}
template <int GS> __device__ __forceinline__ void dma4(rsrc_t r, int v0, int v1, int v2, int v3, int& so, unsigned& m0v) {
  asm volatile("s_mov_b32 m0, %1\n\ts_nop 0\n\t"
               "buffer_load_dwordx4 %2, %6, %0 offen nt lds\n\ts_add_u32 %0, %0, %7\n\t"
               "buffer_load_dwordx4 %3, %6, %0 offen offset:1024 nt lds\n\ts_add_u32 %0, %0, %7\n\t"
               "buffer_load_dwordx4 %4, %6, %0 offen offset:2048 nt lds\n\ts_add_u32 %0, %0, %7\n\t"
               "buffer_load_dwordx4 %5, %6, %0 offen offset:3072 nt lds\n\ts_add_u32 %0, %0, %8\n\ts_add_u32 %1, %1, 0x1000"
               : "+s"(so), "+s"(m0v) : "v"(v0), "v"(v1), "v"(v2), "v"(v3), "s"(r), "i"(GS - 1024), "i"(GS + 3072) : "memory", "scc");
}
struct Ld { unsigned cnt, nxt, last, vfull; };
__device__ __forceinline__ void pace(Ld& s, unsigned w4, unsigned to) {
  unsigned a, b, v;
  asm volatile("0:\n\t"
               "s_memtime vcc\n\t"
               "s_getreg_b32 %0, hwreg(HW_REG_IB_STS)\n\t"
               "s_and_b32 %1, %0, 15\n\t"
               "s_bfe_u32 %0, %0, 0x20016\n\t"
               "s_lshl4_add_u32 %0, %0, %1\n\t"
               "s_sub_u32 %1, %5, %0\n\t"
               "s_cmp_lt_i32 %1, %3\n\t"
               "s_cbranch_scc1 1f\n\t"
               "s_lshr_b32 %1, %1, 4\n\t"
               "v_mov_b32 %2, %1\n\t"
               "ds_write_b32 %6, %2\n\t"
               "s_lshl_b32 %1, %1, 4\n\t"
               "s_add_u32 %3, %1, 16\n"
               "1:\n\t"
               "s_waitcnt lgkmcnt(0)\n\t"
               "s_cmp_le_u32 %0, %7\n\t"
               "s_cbranch_scc1 2f\n\t"
               "s_sub_u32 %1, vcc_lo, %4\n\t"
               "s_cmp_ge_u32 %1, %8\n\t"
               "s_cselect_b32 %1, %9, 0\n\t"
               "s_cmp_le_u32 %0, %1\n\t"
               "s_cbranch_scc0 0b\n"
               "2:\n\t"
               "s_mov_b32 %4, vcc_lo"
               : "=&s"(a), "=&s"(b), "=&v"(v), "+s"(s.nxt), "+s"(s.last)
               : "s"(s.cnt), "v"(s.vfull), "s"(w4), "s"(to), "i"(WHI - 4) : "memory", "scc", "vcc");
}
template <int GS, int F0, int NF>
__device__ __forceinline__ void stream(LAS unsigned char* L, Ld& s, rsrc_t r, int v0, int v1, int v2, int v3, int so, int rot, unsigned lds0, unsigned& w4, unsigned& to) {
  volatile LAS unsigned* fl = (volatile LAS unsigned*)(L + L_FL);
  const int base = so;
#pragma nounroll
  for (int f = F0; f < F0 + NF; ++f) {
    unsigned m0v = lds0 + (f & 7) * 16384;
    if (NF == 8) so = base + ((f - F0 + rot) & 7) * 16384;
    if (F0 + NF > 8 && f >= 8) {
      const unsigned now = (unsigned)__builtin_amdgcn_s_memrealtime(), ref = __builtin_amdgcn_readfirstlane(fl[F_REF]);
      const int lag = (CTL && ref) ? (int)(now - ref) - (F0 < 20 ? TOFF + (f - 5) * TAU : TOFF + 15 * TAU + (f - 20) * TAU2) : 0;
      w4 = lag > TTH2 ? WBO - 4 : lag < -TTH ? WLO - 4 : WIN - 4;
      to = lag < -TTH ? TOLO : TOMID;
      lds_wait<LSLP>(L, F_D + (f < 12 ? (f & ~1) : f) - 8, f < 12 ? 4u : f < 20 ? 1u : 8u);
    }
#pragma nounroll
    for (int i = 0; i < 4; ++i) {
      pace(s, w4, to);
      dma4<GS>(r, v0, v1, v2, v3, so, m0v);
      s.cnt += 4;
    }
  }
}

__global__ void __launch_bounds__(NT)
fused_decode(const unsigned short* __restrict__ x, const void* __restrict__ kc, const void* __restrict__ vc, const void* __restrict__ wq,
             const void* __restrict__ wo, const void* __restrict__ w1, const void* __restrict__ b1, const void* __restrict__ w2,
             const void* __restrict__ b2, void* __restrict__ out, void* ws) {
  unsigned long long pt0l; asm volatile("s_memrealtime %0" : "=s"(pt0l)); SB();
  unsigned long long wq_early;
  asm volatile("s_load_dwordx2 %0, %1, 0x18" : "=s"(wq_early) : "s"(__builtin_amdgcn_kernarg_segment_ptr()));
  SB();
  __shared__ __attribute__((aligned(16))) unsigned char smem[LDS_BYTES];
  LAS unsigned char* const L = (LAS unsigned char*)smem;
  const int tid = threadIdx.x, lane = tid & 63, wv = __builtin_amdgcn_readfirstlane(tid >> 6), c = blockIdx.x;
  const int g = lane >> 4, n16 = lane & 15;
  const int grp = c & 7, sp = c >> 3, ab = grp >> 2, ahkv = grp & 3;
  const int ksq = c >> 6, cgq = c & 63;
  const bool has1 = grp != (sp & 7);
  const int c1 = sp * 7 + grp - (grp > (sp & 7) ? 1 : 0);
  const int ks1 = c1 / 56, cg1 = c1 % 56;
  const int cgo = sp & 7;
  const int ho = grp * 4 + (sp >> 3);
  const int ks2 = (sp >> 3) * 8 + grp;
  const bool has2 = ks2 < 28;

  const unsigned vsig = (unsigned)(size_t)smem + (lane == 0 ? L_FL : L_SINK + lane * 4);
  if (tid < F_WORDS) ((volatile LAS unsigned*)(L + L_FL))[tid] = 0u;
  const int r8 = lane >> 3, c8 = lane & 7;
  const int f0 = r8 & 2;
  int vq0 = r8 * 8192 + (c8 ^ f0) * 16, vq1 = vq0 ^ 64;
  int so = ksq * 256 * 8192 + cgq * 128;
  unsigned m0v = (unsigned)(size_t)smem;
  SB();
  asm volatile("s_waitcnt lgkmcnt(0)" : "+s"(wq_early), "+v"(vq0), "+v"(vq1), "+s"(so), "+s"(pt0l) :: "memory");
  const unsigned pt0 = (unsigned)pt0l, pt1 = (unsigned)__builtin_amdgcn_s_memrealtime();
  SB();
  const rsrc_t rq = mk((const void*)wq_early, WQB);
  if (wv == 0) {
    __builtin_amdgcn_s_setprio(3);
#pragma unroll
    for (int i = 0; i < WIN / 4; ++i) dma4<8 * 8192>(rq, vq0, vq1, vq0, vq1, so, m0v);
  }
  if (wv == 0) { ((volatile LAS unsigned*)(L + L_STAMP))[0] = pt0; ((volatile LAS unsigned*)(L + L_STAMP))[1] = pt1; LGKM0(); }
  SB();
  __builtin_amdgcn_s_barrier();
  FENCE();
  if (wv >= 1 && wv < NWAVES - 1) {
    const int w = wv - 1;
    STAMP(0);
    const rsrc_t rws = mk(ws, WS_END);
    const rsrc_t rws2 = mk(ws, has2 ? WS_END : 0u);
    unsigned long long xl = (unsigned long long)x;
    const rsrc_t rxl = mk((const void*)xl, w < 2 ? 4096u : 0u);
    const v4u xa = __builtin_amdgcn_raw_buffer_load_b128(rxl, lane * 16, w * 2048, 0);
    const v4u xb = __builtin_amdgcn_raw_buffer_load_b128(rxl, lane * 16, w * 2048 + 1024, 0);
    FENCE();
    if (w < 2) {
      float xf[16];
#pragma unroll
      for (int i = 0; i < 4; ++i) { xf[2 * i] = blo(xa[i]); xf[2 * i + 1] = bhi(xa[i]); xf[8 + 2 * i] = blo(xb[i]); xf[9 + 2 * i] = bhi(xb[i]); }
      const float piv = asf(__builtin_amdgcn_readfirstlane(asu(xf[0])));
      float s1 = 0.f, s2 = 0.f;
#pragma unroll
      for (int i = 0; i < 16; ++i) { const float d = xf[i] - piv; s1 += d; s2 = fmaf(d, d, s2); }
      float a1 = wave_sum(s1) * (1.0f / 1024), a2 = wave_sum(s2) * (1.0f / 1024);
      float mean = rb(piv + a1), inv = rcp(rb(__builtin_amdgcn_sqrtf(rb(rb(fmaxf(a2 - a1 * a1, 0.f)) + 1e-5f))));
      v4u pa, pb;
      s1 = 0.f; s2 = 0.f;
#pragma unroll
      for (int i = 0; i < 8; ++i) {
        const unsigned t = pk(xf[2 * i] - mean, xf[2 * i + 1] - mean), u = pk(blo(t) * inv, bhi(t) * inv);
        if (i < 4) pa[i] = u; else pb[i - 4] = u;
        xf[2 * i] = blo(u); xf[2 * i + 1] = bhi(u);
        s1 += xf[2 * i] + xf[2 * i + 1]; s2 = fmaf(xf[2 * i], xf[2 * i], fmaf(xf[2 * i + 1], xf[2 * i + 1], s2));
      }
      *(LAS v4u*)(L + L_XS + w * 2048 + lane * 16) = pa;
      *(LAS v4u*)(L + L_XS + w * 2048 + 1024 + lane * 16) = pb;
      a1 = wave_sum(s1) * (1.0f / 1024); a2 = wave_sum(s2) * (1.0f / 1024);
      mean = rb(a1); inv = rcp(rb(__builtin_amdgcn_sqrtf(rb(rb(fmaxf(a2 - a1 * a1, 0.f)) + 1e-5f))));
#pragma unroll
      for (int i = 0; i < 8; ++i) {
        const unsigned t = pk(xf[2 * i] - mean, xf[2 * i + 1] - mean), u = pk(blo(t) * inv, bhi(t) * inv);
        if (i < 4) pa[i] = u; else pb[i - 4] = u;
      }
      *(LAS v4u*)(L + L_YS + w * 2048 + lane * 16) = pa;
      *(LAS v4u*)(L + L_YS + w * 2048 + 1024 + lane * 16) = pb;
      lds_signal<F_LN>(vsig);
    }
    const int q4 = n16 >> 2, p4 = lane & 3;
    lds_wait<LSLP>(L, F_LN, 3);
    const unsigned tag = __builtin_amdgcn_readfirstlane(((volatile LAS unsigned*)(L + L_FL))[F_TAG]);
    const unsigned ta = tag & 0xffu, tb = (tag >> 8) & 0xffu;
    STAMP(1);
    {
      const bool isq = w < 4;
      const LAS unsigned char* ap = L + (isq ? L_XS + ksq * 512 : L_YS + (ks1 & 3) * 512) + (lane & 1) * 2048 + g * 16;
      v4u A[8];
#pragma unroll
      for (int s = 0; s < 8; ++s) A[s] = *(const LAS v4u*)(ap + s * 64);
      lds_wait<LSLP>(L, F_FULL, isq ? 2 : 4);
      const LAS unsigned char* bp = L + (isq ? 0 : 32768) + (8 * g + q4) * 128 + (((w & 3) ^ ((q4 >> 1) | ((g & 1) << 1))) * 32) + p4 * 8;
      v4u Bf[8];
#pragma unroll
      for (int s = 0; s < 8; ++s) Bf[s] = trfrag(bp, s * 4096, s * 4096 + 512);
      lds_signal<F_DQ>(vsig + (isq ? 0 : (F_DH - F_DQ) * 4));
      f4 d0 = mfma32(Bf[0], A[0], f4{0.f, 0.f, 0.f, 0.f}), d1 = mfma32(Bf[1], A[1], f4{0.f, 0.f, 0.f, 0.f});
#pragma unroll
      for (int s = 2; s < 8; s += 2) { d0 = mfma32(Bf[s], A[s], d0); d1 = mfma32(Bf[s + 1], A[s + 1], d1); }
      const rsrc_t rp = mk(ws, (isq || has1) ? WS_END : 0u);
      const int pso = (isq ? WS_QP + cgq * 2048 + ksq * 128 : WS_HP + cg1 * 2048 + ks1 * 128) + (w & 3) * 512;
      stc4(rp, n16 < 2 ? g * 32 + n16 * 16 : OOB, pso, tg4(d0 + d1, ta, tb));
    }
    STAMP(2);
    v4u K[16];
    {
      const LAS unsigned char* kp = L + ((4 + w) & 7) * 16384 + n16 * 256;
      lds_wait<1>(L, F_FULL, 5 + w);
#pragma unroll
      for (int s = 0; s < 4; ++s) {
        const LAS unsigned char* kq = kp + (((4 * s + g) ^ n16) * 16);
#pragma unroll
        for (int t = 0; t < 4; ++t) K[4 * t + s] = *(const LAS v4u*)(kq + t * 4096);
      }
      lds_signal<F_DK>(vsig + w * 4);
    }
    STAMP(3);
    lds_wait<1>(L, F_Q, 1);
    {
      const LAS unsigned char* qb = L + L_QS + (n16 >> 1) * 272 + g * 16;
      v4u qf[4];
#pragma unroll
      for (int s = 0; s < 4; ++s) qf[s] = *(const LAS v4u*)(qb + s * 64);
      f4 sc[4];
#pragma unroll
      for (int t = 0; t < 4; ++t) sc[t] = mfma32(K[4 * t], qf[0], f4{0.f, 0.f, 0.f, 0.f});
#pragma unroll
      for (int s = 1; s < 4; ++s)
#pragma unroll
        for (int t = 0; t < 4; ++t) sc[t] = mfma32(K[4 * t + s], qf[s], sc[t]);
#pragma unroll
      for (int t = 0; t < 4; ++t)
#pragma unroll
        for (int r = 0; r < 4; r += 2) {
          const unsigned u = pk(sc[t][r], sc[t][r + 1]), v = pk(blo(u) * 0.088388347648318440f, bhi(u) * 0.088388347648318440f);
          sc[t][r] = blo(v); sc[t][r + 1] = bhi(v);
        }
      float mw = fmaxf(fmaxf(sc[0].x, sc[0].y), fmaxf(sc[0].z, sc[0].w));
#pragma unroll
      for (int t = 1; t < 4; ++t) mw = fmaxf(mw, fmaxf(fmaxf(sc[t].x, sc[t].y), fmaxf(sc[t].z, sc[t].w)));
      mw = x32_max(x16_max(mw)) * LOG2E;
      float lw = 0.f;
      const unsigned pm = (lane & 1) ? 0xffff0000u : 0u;
      unsigned P[8];
#pragma unroll
      for (int t = 0; t < 4; ++t)
#pragma unroll
        for (int r = 0; r < 4; r += 2) {
          const float v0 = ex2(fmaf(sc[t][r], LOG2E, -mw)), v1 = ex2(fmaf(sc[t][r + 1], LOG2E, -mw));
          lw += v0 + v1;
          const unsigned h = pk(v0, v1);
          P[2 * t + (r >> 1)] = pk(v0 - asf((h << 16) & pm), v1 - asf(h & pm));
        }
      *(LAS v4u*)(L + L_P + w * 2048 + lane * 16) = v4u{P[0], P[1], P[2], P[3]};
      *(LAS v4u*)(L + L_P + w * 2048 + 1024 + lane * 16) = v4u{P[4], P[5], P[6], P[7]};
      lw = x32_sum(x16_sum(lw));
      if (lane < 16 && !(lane & 1)) *(LAS f2*)(L + L_ST + (w * 8 + (lane >> 1)) * 8) = f2{mw, lw};
      lds_signal<F_P>(vsig);
    }
    STAMP(4);
    lds_wait<1>(L, F_P, 8);
    float o0 = 0.f, o1 = 0.f;
    {
      f4 st[8];
#pragma unroll
      for (int j = 0; j < 8; ++j) st[j] = *(const LAS f4*)(L + L_ST + (j * 8 + 2 * g) * 8);
      float M0 = st[0].x, M1 = st[0].z;
#pragma unroll
      for (int j = 1; j < 8; ++j) { M0 = fmaxf(M0, st[j].x); M1 = fmaxf(M1, st[j].z); }
      M0 = __builtin_ceilf(M0); M1 = __builtin_ceilf(M1);
#pragma unroll
      for (int j = 0; j < 8; ++j) { st[j].x = ex2(st[j].x - M0); st[j].z = ex2(st[j].z - M1); }
      STAMP(5);
      int ovs = (lane & 2) ? OOB : (2 * g + (lane & 1)) * 32768 + (n16 >> 2) * 16;
      asm volatile("" : "+v"(ovs));
      const LAS unsigned char* vp = L + (4 * g + q4) * 256 + ((w ^ (4 * (g & 1) + q4)) * 32) + p4 * 8;
#pragma unroll
      for (int j = 0; j < 8; ++j) {
        const v4u pa = *(const LAS v4u*)(L + L_P + j * 2048 + lane * 16), pb = *(const LAS v4u*)(L + L_P + j * 2048 + 1024 + lane * 16);
        if (j < 7) lds_wait<LSLP>(L, F_FULL, 13 + j); else lds_wait<0>(L, F_FULL, 20);
        const LAS unsigned char* vq = vp + ((4 + j) & 7) * 16384;
        const v4u va = trfrag(vq, 0, 4096), vb = trfrag(vq, 8192, 12288);
        if (j == 0) lds_signal<F_DV>(vsig); else if (j == 1) lds_signal<F_DV + 1>(vsig); else if (j == 2) lds_signal<F_DV + 2>(vsig); else if (j == 3) lds_signal<F_DV + 3>(vsig);
        f4 d = mfma32(pa, va, f4{0.f, 0.f, 0.f, 0.f});
        d = mfma32(pb, vb, d);
        o0 = fmaf(st[j].x, d.x + d.y, o0);
        o1 = fmaf(st[j].z, d.z + d.w, o1);
      }
      const bool odd = (lane & 1) != 0;
      const float rcv = dpp<XOR1>(odd ? o0 : o1);
      const unsigned gr = odd ? pk(rcv, o1) : pk(o0, rcv);
      const unsigned gn = dppu<XOR2>(gr);
      stc4(rws, ovs, WS_OP + ((ahkv * 64 + w) * 32 + sp) * 128 + ab * 64, v4u{gr, tag, gn, tag});
    }
    STAMP(6);
    const LAS unsigned char* wp = L + (8 * g + q4) * 256 + ((w ^ (q4 | ((g & 1) << 2))) * 32) + p4 * 8;
    const int fvo = (g >> 1) * 2048 + (g & 1) * 32;
    v4u MPv;
    {
      v4u A[4];
      lds_wait<1>(L, F_H, 1);
#pragma unroll
      for (int s = 0; s < 4; ++s) A[s] = *(const LAS v4u*)(L + L_HS + (lane & 1) * 272 + s * 64 + g * 16);
      lds_wait<LSLP>(L, F_FULL, 22);
      f4 d = {0.f, 0.f, 0.f, 0.f};
#pragma unroll
      for (int s = 0; s < 4; ++s) d = mfma32(trfrag(wp + 4 * 16384, s * 8192, s * 8192 + 1024), A[s], d);
      MPv = tg4(d, ta, tb);
      if (!MPLATE) stc4(rws2, n16 < 2 ? fvo + n16 * 16 : OOB, WS_MP + ((cgo * 16 + 2 * w) * 32 + ks2) * 64, MPv);
    }
    STAMP(7);
    {
      const int ovo = ((n16 >> 2) & 1) * 2048 + g * 512 + (n16 >> 3) * 64 + (lane & 3) * 16, oso = WS_OP + (ho * 8 + w) * 4096;
      v4u G[4];
      lds_wait<0>(L, F_FULL, OGATE);
#pragma unroll
      for (int j = 0; j < 4; ++j) G[j] = ldc4(rws, ovo + j * 128, oso);
      SB();
      if (MPLATE) stc4(rws2, n16 < 2 ? fvo + n16 * 16 : OOB, WS_MP + ((cgo * 16 + 2 * w) * 32 + ks2) * 64, MPv);
      SB();
      lds_wait<0>(L, F_W, 1);
      v2u WF = *(const LAS v2u*)(L + L_WT + (lane & 3) * 32 + g * 8);
      float invl = ((const LAS float*)(L + L_WT + 128))[(lane >> 1) & 1];
      LAS unsigned char* osw = L + (n16 == g ? L_OS + g * 272 + w * 32 : L_QS + lane * 32);
      const LAS unsigned char* osr = L + L_OS + (lane & 3) * 272 + g * 16;
      const LAS unsigned char* wop = wp + 6 * 16384;
      int fv2 = (n16 & 13) == 0 ? fvo + (n16 >> 1) * 16 : OOB, fso = WS_FP + ((cgo * 16 + 2 * w) * 32 + ho) * 64;
      unsigned vta = ta, vtb = tb, vtag = tag;
      asm volatile("" : "+v"(WF), "+v"(invl), "+v"(osw), "+v"(osr), "+v"(wop), "+v"(fv2), "+s"(fso), "+v"(vta), "+v"(vtb), "+v"(vtag));
      lds_wait<0>(L, F_FULL, 24);
      v4u Bo[4];
#pragma unroll
      for (int s = 0; s < 4; ++s) Bo[s] = trfrag(wop, s * 8192, s * 8192 + 1024);
      SB();
      STAMP(8);
      const f4 z = {0.f, 0.f, 0.f, 0.f};
      f4 e0, e1, e2, e3;
#define COMBINE() do { \
        e0 = mfma16(v2u{plo(G[0].x, G[1].x), plo(G[2].x, G[3].x)}, WF, z); e1 = mfma16(v2u{phi(G[0].x, G[1].x), phi(G[2].x, G[3].x)}, WF, z); \
        e2 = mfma16(v2u{plo(G[0].z, G[1].z), plo(G[2].z, G[3].z)}, WF, z); e3 = mfma16(v2u{phi(G[0].z, G[1].z), phi(G[2].z, G[3].z)}, WF, z); } while (0)
#define OBAD(j, acc) xoror(G[j].y, vtag, xoror(G[j].w, vtag, acc))
      COMBINE();
      SB();
      if (__builtin_expect(__any((OBAD(0, OBAD(1, 0u)) | OBAD(2, OBAD(3, 0u))) != 0u), 0)) {
#pragma nounroll
        for (unsigned spin = 0; spin < SPIN_CAP; ++spin) {
          const bool m0 = __any(OBAD(0, 0u) != 0u), m1 = __any(OBAD(1, 0u) != 0u), m2 = __any(OBAD(2, 0u) != 0u), m3 = __any(OBAD(3, 0u) != 0u);
          if (!(m0 || m1 || m2 || m3)) break;
          FENCE();
          if (m0) G[0] = ldc4(rws, ovo, oso);
          if (m1) G[1] = ldc4(rws, ovo + 128, oso);
          if (m2) G[2] = ldc4(rws, ovo + 256, oso);
          if (m3) G[3] = ldc4(rws, ovo + 384, oso);
        }
        COMBINE();
      }
      STAMP(9);
      *(LAS v4u*)osw = v4u{pk(e0.x, e1.x), pk(e2.x, e3.x), pk(e0.y, e1.y), pk(e2.y, e3.y)};
      *(LAS v4u*)(osw + 16) = v4u{pk(e0.z, e1.z), pk(e2.z, e3.z), pk(e0.w, e1.w), pk(e2.w, e3.w)};
      LGKM0();
      __builtin_amdgcn_s_barrier();
      FENCE();
      v4u a0 = *(const LAS v4u*)osr, a1 = *(const LAS v4u*)(osr + 64), a2 = *(const LAS v4u*)(osr + 128), a3 = *(const LAS v4u*)(osr + 192);
      SB();
      f4 d = mfma32(Bo[0], a0, z), d2 = mfma32(Bo[1], a1, z);
      d = mfma32(Bo[2], a2, d); d2 = mfma32(Bo[3], a3, d2);
      d += d2;
      d.x += dpp<XOR1>(d.x); d.y += dpp<XOR1>(d.y); d.z += dpp<XOR1>(d.z); d.w += dpp<XOR1>(d.w);
      d *= invl;
      stc4(rws, fv2, fso, v4u{(asu(d.x) & 0xffffff00u) | vta, (asu(d.y) & 0xffffff00u) | vtb, (asu(d.z) & 0xffffff00u) | vta, (asu(d.w) & 0xffffff00u) | vtb});
    }
    STAMP(10);
  } else if (wv == NWAVES - 1) {
    STAMP(0);
    volatile LAS unsigned* fl = (volatile LAS unsigned*)(L + L_FL);
    const rsrc_t rws = mk(ws, WS_END), rws1 = mk(ws, has1 ? WS_END : 0u), rws2 = mk(ws, has2 ? WS_END : 0u), rout = mk(out, 4096);
    const unsigned eraw = __builtin_amdgcn_raw_buffer_load_b32(rws, 0, WS_E, AUX_SC1);
    const unsigned b1p = __builtin_amdgcn_raw_buffer_load_b32(mk(b1, has1 ? 7168u : 0u), (n16 >> 2) * 8 + (lane & 1) * 4, c1 * 32, 0);
    const int col2 = ((lane >> 1) & 1) * 8 + ((g >> 1) | ((g & 1) << 1)) * 2;
    const int fo = (lane & 1) * 2048 + col2;
    const unsigned short xfin = __builtin_amdgcn_raw_buffer_load_b16(mk(x, c < 128 ? 4096u : 0u), fo, c * 16, 0);
    const unsigned short b2fin = __builtin_amdgcn_raw_buffer_load_b16(mk(b2, c < 128 ? 2048u : 0u), col2, c * 16, 0);
    const v4u codepf = __builtin_amdgcn_raw_buffer_load_b128(mk((const void*)((__builtin_amdgcn_s_getpc() & ~255ull) - PFB * 1024u), PF * 1024u), lane * 16, sp * 1024, 0);
    FENCE();
    const unsigned tag = __builtin_amdgcn_readfirstlane(eraw) * 0x9E3779B1u + 0x7F4A7C15u;
    const unsigned ta = tag & 0xffu, tb = (tag >> 8) & 0xffu;
    fl[F_TAG] = tag;
    lds_signal<F_LN>(vsig);
    asm volatile("" :: "v"(codepf));
    lds_wait<0>(L, F_FULL, 2);
    const unsigned ptf2 = (unsigned)__builtin_amdgcn_s_memrealtime();
    lds_wait<1>(L, F_FULL, 4);
    const unsigned t4 = (unsigned)__builtin_amdgcn_s_memrealtime();
    lds_wait<LSLP>(L, F_FULL, S1GATE);
    {
      const int fvo = (lane < 16 && !(lane & 1)) ? (lane >> 2) * 16 : OOB, fb = (lane >> 1) & 1;
      bool needq = true, needh = has1;
#pragma nounroll
      for (unsigned spin = 0; (needq || needh) && spin < SPIN_CAP; ++spin) {
        const v2u GQ = ldc2(rws, needq ? lane * 8 : OOB, WS_QP + c * 512), GH = ldc2(rws1, needh ? lane * 8 : OOB, WS_HP + c1 * 512);
        if (needq && !__any((((GQ.x ^ ta) | (GQ.y ^ tb)) & 0xffu) != 0u)) {
          const unsigned gr = pk(x32_sum(x16_sum(untg(GQ.x))), x32_sum(x16_sum(untg(GQ.y)))), gn = dppu<XOR1>(gr), ty = (tag << 12) | (t4 & 0xfffu);
          stc4(rws, fvo + fb * 16384, WS_QF + c * 64, v4u{gr, ty, gn, ty});
          needq = false;
          STAMP(13);
        }
        if (needh && !__any((((GH.x ^ ta) | (GH.y ^ tb)) & 0xffu) != 0u)) {
          float h0 = rb(rb(x32_sum(x16_sum(untg(GH.x)))) + blo(b1p)), h1 = rb(rb(x32_sum(x16_sum(untg(GH.y)))) + bhi(b1p));
          constexpr float GC = -2.0f * 0.7978845608028654f * 1.4426950408889634f;
          h0 = h0 * rcp(1.0f + ex2(GC * (h0 + 0.044715f * h0 * h0 * h0)));
          h1 = h1 * rcp(1.0f + ex2(GC * (h1 + 0.044715f * h1 * h1 * h1)));
          const unsigned gr = pk(h0, h1), gn = dppu<XOR1>(gr);
          stc4(rws, fvo + fb * 14336, WS_HF + c1 * 64, v4u{gr, tag, gn, tag});
          needh = false;
          STAMP(14);
        }
        __builtin_amdgcn_s_sleep(PSLP);
      }
    }
    lds_wait<LSLP>(L, F_FULL, QGATE);
    {
      v4u Q[4];
      const unsigned t20 = tag & 0xfffffu;
#pragma nounroll
      for (unsigned spin = 0; spin < SPIN_CAP; ++spin) {
        unsigned bad = 0;
#pragma unroll
        for (int i = 0; i < 4; ++i) Q[i] = ldc4(rws, lane * 16, WS_QF + grp * 4096 + i * 1024);
#pragma unroll
        for (int i = 0; i < 4; ++i) bad |= ((Q[i].y >> 12) ^ t20) | ((Q[i].w >> 12) ^ t20);
        if (!__any(bad != 0u)) break;
        __builtin_amdgcn_s_sleep(PSLP);
      }
#pragma unroll
      for (int i = 0; i < 4; ++i) *(LAS v2u*)(L + L_QS + (2 * i + (lane >> 5)) * 272 + (lane & 31) * 8) = v2u{Q[i].x, Q[i].z};
      lds_signal<F_Q>(vsig);
      STAMP(15);
      if (CTL) {
        int d = 0;
#pragma unroll
        for (int i = 0; i < 4; ++i) d += (int)((Q[i].y - t4) << 20) >> 20;
        fl[F_REF] = (t4 + (int)(wave_sum((float)d) * (1.0f / 256))) | 1u;
      }
    }
    lds_wait<1>(L, F_P, 8);
    {
      f2 sv[8];
#pragma unroll
      for (int j = 0; j < 8; ++j) sv[j] = *(const LAS f2*)(L + L_ST + (j * 8 + (lane & 7)) * 8);
      float M = sv[0].x, Ls = 0.f;
#pragma unroll
      for (int j = 1; j < 8; ++j) M = fmaxf(M, sv[j].x);
      M = __builtin_ceilf(M);
#pragma unroll
      for (int j = 0; j < 8; ++j) Ls = fmaf(ex2(sv[j].x - M), sv[j].y, Ls);
      stc4(rws, lane < 8 ? (lane >> 2) * 2048 + (lane & 3) * 16 : OOB, WS_ST + grp * 4096 + sp * 64, v4u{asu(M), tag, asu(Ls), tag});
      const unsigned tag2 = has2 ? tag : 0u;
      v4u H;
#pragma nounroll
      for (unsigned spin = 0; spin < SPIN_CAP; ++spin) {
        H = ldc4(rws2, (lane >> 5) * 14336 + (lane & 31) * 16, WS_HF + ks2 * 512);
        if (!__any(((H.y ^ tag2) | (H.w ^ tag2)) != 0u)) break;
        __builtin_amdgcn_s_sleep(PSLP);
      }
      *(LAS v2u*)(L + L_HS + (lane >> 5) * 272 + (lane & 31) * 8) = v2u{H.x, H.z};
      lds_signal<F_H>(vsig);
      STAMP(16);
    }
    lds_wait<LSLP>(L, F_FULL, SGATE);
    {
      v4u SG;
#pragma nounroll
      for (unsigned spin = 0; spin < SPIN_CAP; ++spin) {
        SG = ldc4(rws, (lane >> 5) * 16384 + (lane & 31) * 64, WS_ST + (ho >> 2) * 2048 + (ho & 3) * 16);
        if (!__any(((SG.y ^ tag) | (SG.w ^ tag)) != 0u)) break;
        __builtin_amdgcn_s_sleep(PSLP);
      }
      const float m = asf(SG.x), M = x16_max(row_max(m));
      const float e = rb(ex2(m - M)), Ls = x16_sum(row_sum(e * asf(SG.z)));
      ((LAS unsigned short*)(L + L_WT))[lane] = (unsigned short)(asu(e) >> 16);
      ((LAS float*)(L + L_WT + 128))[lane >> 5] = rcp(Ls) * (1.0f + 1.0f / 131072);
      lds_signal<F_W>(vsig);
      STAMP(17);
    }
    {
      const rsrc_t rwf = mk(ws, c < 128 ? WS_END : 0u);
      const unsigned mlive = lane < 48 ? 0xffffffffu : 0u;
      __builtin_amdgcn_s_barrier();
      FENCE();
      if (c < 128) {
        int so = WS_MP + c * 2048, ov = OOB, ev = OOB, pass = 0;
        unsigned live = mlive;
        float gt = 0.f;
        const float xv = blo(xfin), bv = blo(b2fin);
        asm volatile("" : "+s"(pass));
#pragma nounroll
        for (;;) {
          v4u A0 = ldc4(rwf, lane * 16, so), A1 = ldc4(rwf, lane * 16, so + 1024);
#pragma nounroll
          for (unsigned spin = 0; spin < SPIN_CAP; ++spin) {
            const bool m0 = __any(bad4(A0, ta, tb) != 0u), m1 = __any((bad4(A1, ta, tb) & live) != 0u);
            if (!(m0 || m1)) break;
            FENCE();
            if (m0) A0 = ldc4(rwf, lane * 16, so);
            if (m1) A1 = ldc4(rwf, lane * 16, so + 1024);
          }
          if (pass) { STAMP(25); }
          const float r = rb(red4(A0, A1 & live));
          const float o = xv + rb(r * gt);
          __builtin_amdgcn_raw_buffer_store_b16((unsigned short)pk(o, o), rout, ov, c * 16, OAUX);
          __builtin_amdgcn_raw_buffer_store_b32(tag, rws, ev, WS_E, EAUX);
          SB();
          if (pass) break;
          const float m = rb(r + bv);
          gt = rb(m * rcp(1.0f + ex2(-1.4426950408889634f * m)));
          pass = 1; so = WS_FP + c * 2048; live = 0xffffffffu;
          ov = (lane & 12) == 0 ? fo : OOB; ev = (c | lane) == 0 ? 0 : OOB;
          asm volatile("" : "+s"(pass), "+s"(so), "+v"(live), "+v"(ov), "+v"(ev), "+v"(gt));
        }
        { const unsigned l0 = __builtin_amdgcn_readfirstlane(((volatile LAS unsigned*)(L + L_STAMP))[0]), l1 = __builtin_amdgcn_readfirstlane(((volatile LAS unsigned*)(L + L_STAMP))[1]); int d = 6 * (int)(pt0 - l0); if (d > 6000) d = 6000; if (d < 0) d = 0; const unsigned t1 = (unsigned)__builtin_amdgcn_s_memrealtime(); for (int it = 0; it < 100000; ++it) if ((unsigned)__builtin_amdgcn_s_memrealtime() - t1 >= (unsigned)d) break; }
      }
    }
  } else {
    STAMP(0);
    volatile LAS unsigned* fl = (volatile LAS unsigned*)(L + L_FL);
    const unsigned lds0 = (unsigned)(size_t)smem;
    Ld st = {WIN, 16, 0, lds0 + L_FL + F_FULL * 4};
    unsigned w4 = WIN - 4, to = 0x7fffffffu;
#pragma nounroll
    for (int i = WIN / 4; i < 8; ++i) { pace(st, w4, to); dma4<8 * 8192>(rq, vq0, vq1, vq0, vq1, so, m0v); st.cnt += 4; }
    to = TOMID;
    {
      const int v0 = r8 * 7168 + (c8 ^ f0) * 16;
      stream<8 * 7168, 2, 2>(L, st, mk(w1, has1 ? W1B : 0u), v0, v0 ^ 64, v0, v0 ^ 64, ks1 * 256 * 7168 + cg1 * 128, 0, lds0, w4, to);
    }
    const int kvb = (grp * 16384 + sp * 512) * 256;
    {
      const int v0 = g * 256 + (n16 ^ g) * 16;
      stream<1024, 4, 8>(L, st, mk(kc, KVB), v0, v0 ^ 64, v0 ^ 128, v0 ^ 192, kvb, sp, lds0, w4, to);
    }
    {
      const int v0 = g * 256 + (n16 ^ (2 * g)) * 16;
      stream<1024, 12, 8>(L, st, mk(vc, KVB), v0, v0 ^ 128, v0, v0 ^ 128, kvb, sp, lds0, w4, to);
    }
    {
      const int v0 = g * 2048 + (n16 ^ (2 * g)) * 16;
      stream<8192, 20, 2>(L, st, mk(w2, has2 ? W1B : 0u), v0, v0, v0 ^ 128, v0 ^ 128, ks2 * 128 * 2048 + cgo * 256, 0, lds0, w4, to);
      stream<8192, 22, 2>(L, st, mk(wo, WQB), v0, v0, v0 ^ 128, v0 ^ 128, ho * 128 * 2048 + cgo * 256, 0, lds0, w4, to);
    }
    pace(st, 0u, 0x7fffffffu);
    asm volatile("s_waitcnt vmcnt(0)" ::: "memory");
    fl[F_FULL] = 24;
  }
}

}

extern "C" void kernel_launch(void* const* d_in, const int* in_sizes, int n_in, void* d_out, int out_size, void* d_ws, size_t ws_size,
                              hipStream_t stream) {
  (void)in_sizes; (void)n_in; (void)out_size; (void)ws_size;
  fused_decode<<<256, NT, 0, stream>>>((const unsigned short*)d_in[0], d_in[1], d_in[2], d_in[3], d_in[4], d_in[6], d_in[7], d_in[8], d_in[9],
                                       d_out, d_ws);
}
```

```cpp
#include <hip/hip_runtime.h>
#include <hip/hip_bf16.h>

namespace {

typedef unsigned v4u __attribute__((ext_vector_type(4)));
typedef unsigned v2u __attribute__((ext_vector_type(2)));
typedef float f4 __attribute__((ext_vector_type(4)));
typedef float f2 __attribute__((ext_vector_type(2)));
typedef __bf16 bf8 __attribute__((ext_vector_type(8)));
typedef __bf16 bf2 __attribute__((ext_vector_type(2)));
typedef short s4 __attribute__((ext_vector_type(4)));
typedef __amdgpu_buffer_rsrc_t rsrc_t;
#define LAS __attribute__((address_space(3)))

constexpr int NWAVES = 10;
constexpr int NT = 64 * NWAVES;
constexpr unsigned KVB = 2u * 4 * 16384 * 128 * 2, WQB = 1024u * 4096 * 2, W1B = 1024u * 3584 * 2;
constexpr unsigned WS_E = 0;
constexpr unsigned WS_QP = 4096;
constexpr unsigned WS_HP = WS_QP + 131072;
constexpr unsigned WS_QF = WS_HP + 114688;
constexpr unsigned WS_HF = WS_QF + 32768;
constexpr unsigned WS_ST = WS_HF + 28672;
constexpr unsigned WS_OP = WS_ST + 32768;
constexpr unsigned WS_FP = WS_OP + 1048576;
constexpr unsigned WS_MP = WS_FP + 262144;
constexpr unsigned WS_END = WS_MP + 262144;
constexpr int L_P = 131072, L_XS = L_P, L_YS = L_P + 4096, L_QS = L_P + 16384, L_HS = L_QS + 8 * 272, L_OS = L_HS + 2 * 272,
              L_ST = L_OS + 4 * 272, L_WT = L_ST + 512, L_FL = L_WT + 256, L_SINK = L_FL + 128, L_STAMP = L_SINK + 512, L_END = L_STAMP + NWAVES * 256;
constexpr int LDS_BYTES = L_END;
static_assert(L_END <= 163840 && (L_HS % 16) == 0 && (L_OS % 16) == 0 && (L_ST % 16) == 0 && (L_WT % 16) == 0 && (L_FL % 16) == 0, "lds map");
enum { F_FULL = 0, F_LN = 1, F_Q = 2, F_P = 3, F_H = 4, F_REF = 5, F_TAG = 6, F_W = 7, F_D = 8, F_DQ = F_D, F_DH = F_D + 2, F_DK = F_D + 4, F_DV = F_D + 12, F_WORDS = 32 };
constexpr int OOB = 0x7ff00000;
#define AUXL 16
#define AUXS 16
constexpr int AUX_SC1 = 16;
constexpr unsigned SPIN_CAP = 1u << 15, LSPIN_CAP = 1u << 20;
constexpr float LOG2E = 1.4426950408889634f;
#define LSLP 2
#define QGATE 10
#define OGATE 23
#define PSLP 2
#define PF 14
#define WLO 16
#define WHI 36
#define TAU 56
#define TOFF 15
#define TTH 20
#define TOMID 330
#define TOLO 700
#define TAU2 46
#define PROP 0
#define TPROP 16
#define TOHI 200
#define S1GATE 5
#define SGATE 19
#define MPLATE 0
#define PFB 3
#define OPF 1
#define OAUX 16
#define EAUX 16
#define FLATE 1
#define FSLP0 0
#define FSLP 0
#define WBO WHI
#define TTH2 TTH
#define PFBYTES 15000
#define PFW 9
#define BLOOP 1
#define WARM 1
#define CTL 1
#define WIN 24

__device__ __forceinline__ rsrc_t mk(const void* p, unsigned bytes) { return __builtin_amdgcn_make_buffer_rsrc((void*)p, 0, bytes, 0x00020000); }
__device__ __forceinline__ v4u ldc4(rsrc_t r, int vo, int so) { return __builtin_amdgcn_raw_buffer_load_b128(r, vo, so, AUXL); }
__device__ __forceinline__ v2u ldc2(rsrc_t r, int vo, int so) { return __builtin_amdgcn_raw_buffer_load_b64(r, vo, so, AUXL); }
__device__ __forceinline__ void stc4(rsrc_t r, int vo, int so, v4u v) { __builtin_amdgcn_raw_buffer_store_b128(v, r, vo, so, AUXS); }
__device__ __forceinline__ float asf(unsigned u) { return __builtin_bit_cast(float, u); }
__device__ __forceinline__ unsigned asu(float f) { return __builtin_bit_cast(unsigned, f); }
__device__ __forceinline__ unsigned pk(float a, float b) { f2 v = {a, b}; return __builtin_bit_cast(unsigned, __builtin_convertvector(v, bf2)); }
__device__ __forceinline__ float blo(unsigned u) { return asf(u << 16); }
__device__ __forceinline__ float bhi(unsigned u) { return asf(u & 0xffff0000u); }
__device__ __forceinline__ float rb(float a) { return blo(pk(a, a)); }
__device__ __forceinline__ unsigned plo(unsigned a, unsigned b) { return __builtin_amdgcn_perm(b, a, 0x05040100u); }
__device__ __forceinline__ unsigned phi(unsigned a, unsigned b) { return __builtin_amdgcn_perm(b, a, 0x07060302u); }
__device__ __forceinline__ f4 mfma32(v4u a, v4u b, f4 c) { return __builtin_amdgcn_mfma_f32_16x16x32_bf16(__builtin_bit_cast(bf8, a), __builtin_bit_cast(bf8, b), c, 0, 0, 0); }
__device__ __forceinline__ f4 mfma16(v2u a, v2u b, f4 c) { return __builtin_amdgcn_mfma_f32_16x16x16bf16_1k(__builtin_bit_cast(s4, a), __builtin_bit_cast(s4, b), c, 0, 0, 0); }
template <int C> __device__ __forceinline__ float dpp(float x) { return asf(__builtin_amdgcn_mov_dpp(asu(x), C, 0xf, 0xf, true)); }
template <int C> __device__ __forceinline__ unsigned dppu(unsigned x) { return __builtin_amdgcn_mov_dpp(x, C, 0xf, 0xf, true); }
constexpr int XOR1 = 0xB1, XOR2 = 0x4E, ROR4 = 0x124, ROR8 = 0x128;
__device__ __forceinline__ float x16_sum(float x) { auto s = __builtin_amdgcn_permlane16_swap(asu(x), asu(x), false, false); return asf(s[0]) + asf(s[1]); }
__device__ __forceinline__ float x32_sum(float x) { auto s = __builtin_amdgcn_permlane32_swap(asu(x), asu(x), false, false); return asf(s[0]) + asf(s[1]); }
__device__ __forceinline__ float x16_max(float x) { auto s = __builtin_amdgcn_permlane16_swap(asu(x), asu(x), false, false); return fmaxf(asf(s[0]), asf(s[1])); }
__device__ __forceinline__ float x32_max(float x) { auto s = __builtin_amdgcn_permlane32_swap(asu(x), asu(x), false, false); return fmaxf(asf(s[0]), asf(s[1])); }
__device__ __forceinline__ float row_sum(float x) { x += dpp<XOR1>(x); x += dpp<XOR2>(x); x += dpp<ROR4>(x); x += dpp<ROR8>(x); return x; }
__device__ __forceinline__ float row_max(float x) { x = fmaxf(x, dpp<XOR1>(x)); x = fmaxf(x, dpp<XOR2>(x)); x = fmaxf(x, dpp<ROR4>(x)); x = fmaxf(x, dpp<ROR8>(x)); return x; }
__device__ __forceinline__ float wave_sum(float x) { return x32_sum(x16_sum(row_sum(x))); }
__device__ __forceinline__ float ex2(float x) { return __builtin_amdgcn_exp2f(x); }
__device__ __forceinline__ float rcp(float x) { return __builtin_amdgcn_rcpf(x); }
__device__ __forceinline__ unsigned tg(float v, unsigned t8) { return ((asu(v) + 0x80u) & 0xffffff00u) | t8; }
__device__ __forceinline__ v4u tg4(f4 v, unsigned ta, unsigned tb) { return v4u{tg(v.x, ta), tg(v.y, tb), tg(v.z, ta), tg(v.w, tb)}; }
__device__ __forceinline__ float untg(unsigned u) { return asf(u & 0xffffff00u); }
__device__ __forceinline__ unsigned xoror(unsigned a, unsigned b, unsigned c) { return __builtin_amdgcn_bitop3_b32(a, b, c, 0xBE); }
__device__ __forceinline__ unsigned bad4(v4u u, unsigned ta, unsigned tb) { return xoror(u.x, ta, xoror(u.y, tb, xoror(u.z, ta, u.w ^ tb))) & 0xffu; }
__device__ __forceinline__ float red4(v4u a, v4u b) {
  const auto s01 = __builtin_amdgcn_permlane32_swap(asu(untg(a.x) + untg(b.x)), asu(untg(a.y) + untg(b.y)), false, false);
  const auto s23 = __builtin_amdgcn_permlane32_swap(asu(untg(a.z) + untg(b.z)), asu(untg(a.w) + untg(b.w)), false, false);
  const auto t = __builtin_amdgcn_permlane16_swap(asu(asf(s01[0]) + asf(s01[1])), asu(asf(s23[0]) + asf(s23[1])), false, false);
  float v = asf(t[0]) + asf(t[1]);
  v += dpp<ROR4>(v); v += dpp<ROR8>(v);
  return v;
}
__device__ __forceinline__ v4u trfrag(const LAS unsigned char* a, int off0, int off1) {
  const v2u lo = __builtin_bit_cast(v2u, __builtin_amdgcn_ds_read_tr16_b64_v4i16((LAS s4*)(a + off0)));
  const v2u hi = __builtin_bit_cast(v2u, __builtin_amdgcn_ds_read_tr16_b64_v4i16((LAS s4*)(a + off1)));
  return v4u{lo.x, lo.y, hi.x, hi.y};
}

#define SB() __builtin_amdgcn_sched_barrier(0)
#define FENCE() do { SB(); asm volatile("" ::: "memory"); SB(); } while (0)
#define PAD(ch, k, n) do { if (WARM) { SB(); asm volatile(".Lpad" #ch "_" #k ":\n\ts_cbranch_execz .Lpad" #ch "_" #n); SB(); } } while (0)
#define HOP(ch) do { if (WARM) { unsigned long long ex_; SB(); asm volatile("s_mov_b64 %0, exec\n\ts_mov_b64 exec, 0\n\ts_branch .Lpad" #ch "_0\n.Lpad" #ch "_e:\n\ts_mov_b64 exec, %0" : "=&s"(ex_)); SB(); } } while (0)
#define LGKM0() do { SB(); asm volatile("s_waitcnt lgkmcnt(0)" ::: "memory"); SB(); } while (0)
#define STAMP(i)

template <int SLEEP> __device__ __forceinline__ void lds_wait(LAS unsigned char* L, int word, unsigned want) {
  FENCE();
  const volatile LAS unsigned* p = (const volatile LAS unsigned*)(L + L_FL) + word;
#pragma nounroll
  for (unsigned i = 0; i < LSPIN_CAP; ++i) {
    if ((unsigned)__builtin_amdgcn_readfirstlane(*p) >= want) break;
    if (SLEEP) __builtin_amdgcn_s_sleep(SLEEP);
  }
  FENCE();
}
template <int WORD> __device__ __forceinline__ void lds_signal(unsigned vsig) {
  SB();
  asm volatile("s_waitcnt lgkmcnt(0)\n\tds_add_u32 %0, %1 offset:%2" :: "v"(vsig), "v"(1u), "i"(WORD * 4) : "memory");
  SB();
}
template <int GS> __device__ __forceinline__ void dma4(rsrc_t r, int v0, int v1, int v2, int v3, int& so, unsigned& m0v) {
  asm volatile("s_mov_b32 m0, %1\n\ts_nop 0\n\t"
               "buffer_load_dwordx4 %2, %6, %0 offen nt lds\n\ts_add_u32 %0, %0, %7\n\t"
               "buffer_load_dwordx4 %3, %6, %0 offen offset:1024 nt lds\n\ts_add_u32 %0, %0, %7\n\t"
               "buffer_load_dwordx4 %4, %6, %0 offen offset:2048 nt lds\n\ts_add_u32 %0, %0, %7\n\t"
               "buffer_load_dwordx4 %5, %6, %0 offen offset:3072 nt lds\n\ts_add_u32 %0, %0, %8\n\ts_add_u32 %1, %1, 0x1000"
               : "+s"(so), "+s"(m0v) : "v"(v0), "v"(v1), "v"(v2), "v"(v3), "s"(r), "i"(GS - 1024), "i"(GS + 3072) : "memory", "scc");
}
struct Ld { unsigned cnt, nxt, last, vfull; };
__device__ __forceinline__ void pace(Ld& s, unsigned w4, unsigned to) {
  unsigned a, b, v;
  asm volatile("0:\n\t"
               "s_memtime vcc\n\t"
               "s_getreg_b32 %0, hwreg(HW_REG_IB_STS)\n\t"
               "s_and_b32 %1, %0, 15\n\t"
               "s_bfe_u32 %0, %0, 0x20016\n\t"
               "s_lshl4_add_u32 %0, %0, %1\n\t"
               "s_sub_u32 %1, %5, %0\n\t"
               "s_cmp_lt_i32 %1, %3\n\t"
               "s_cbranch_scc1 1f\n\t"
               "s_lshr_b32 %1, %1, 4\n\t"
               "v_mov_b32 %2, %1\n\t"
               "ds_write_b32 %6, %2\n\t"
               "s_lshl_b32 %1, %1, 4\n\t"
               "s_add_u32 %3, %1, 16\n"
               "1:\n\t"
               "s_waitcnt lgkmcnt(0)\n\t"
               "s_cmp_le_u32 %0, %7\n\t"
               "s_cbranch_scc1 2f\n\t"
               "s_sub_u32 %1, vcc_lo, %4\n\t"
               "s_cmp_ge_u32 %1, %8\n\t"
               "s_cselect_b32 %1, %9, 0\n\t"
               "s_cmp_le_u32 %0, %1\n\t"
               "s_cbranch_scc0 0b\n"
               "2:\n\t"
               "s_mov_b32 %4, vcc_lo"
               : "=&s"(a), "=&s"(b), "=&v"(v), "+s"(s.nxt), "+s"(s.last)
               : "s"(s.cnt), "v"(s.vfull), "s"(w4), "s"(to), "i"(WHI - 4) : "memory", "scc", "vcc");
}
template <int GS, int F0, int NF>
__device__ __forceinline__ void stream(LAS unsigned char* L, Ld& s, rsrc_t r, int v0, int v1, int v2, int v3, int so, int rot, unsigned lds0, unsigned& w4, unsigned& to) {
  volatile LAS unsigned* fl = (volatile LAS unsigned*)(L + L_FL);
  const int base = so;
#pragma nounroll
  for (int f = F0; f < F0 + NF; ++f) {
    unsigned m0v = lds0 + (f & 7) * 16384;
    if (NF == 8) so = base + ((f - F0 + rot) & 7) * 16384;
    if (F0 + NF > 8 && f >= 8) {
      const unsigned now = (unsigned)__builtin_amdgcn_s_memrealtime(), ref = __builtin_amdgcn_readfirstlane(fl[F_REF]);
      const int lag = (CTL && ref) ? (int)(now - ref) - (F0 < 20 ? TOFF + (f - 5) * TAU : TOFF + 15 * TAU + (f - 20) * TAU2) : 0;
      w4 = lag > TTH2 ? WBO - 4 : lag < -TTH ? WLO - 4 : WIN - 4;
      to = lag < -TTH ? TOLO : TOMID;
      lds_wait<LSLP>(L, F_D + (f < 12 ? (f & ~1) : f) - 8, f < 12 ? 4u : f < 20 ? 1u : 8u);
    }
#pragma nounroll
    for (int i = 0; i < 4; ++i) {
      pace(s, w4, to);
      dma4<GS>(r, v0, v1, v2, v3, so, m0v);
      s.cnt += 4;
    }
  }
}

__global__ void __launch_bounds__(NT)
fused_decode(const unsigned short* __restrict__ x, const void* __restrict__ kc, const void* __restrict__ vc, const void* __restrict__ wq,
             const void* __restrict__ wo, const void* __restrict__ w1, const void* __restrict__ b1, const void* __restrict__ w2,
             const void* __restrict__ b2, void* __restrict__ out, void* ws) {
  __shared__ __attribute__((aligned(16))) unsigned char smem[LDS_BYTES];
  LAS unsigned char* const L = (LAS unsigned char*)smem;
  const int tid = threadIdx.x, lane = tid & 63, wv = __builtin_amdgcn_readfirstlane(tid >> 6), c = blockIdx.x;
  int vq0, vq1, so;
  unsigned m0v;
  v4u rqw;
  if (wv == 0) {
    unsigned t0;
    asm volatile("s_load_dwordx2 s[36:37], %7, 0x18\n\t"
                 "s_getpc_b64 s[40:41]\n\t"
                 "s_setprio 3\n\t"
                 "s_andn2_b32 s40, s40, 63\n\t"
                 "s_lshl_b32 %4, %8, 7\n\t"
                 "s_movk_i32 s42, %9\n\t"
                 "s_mov_b32 s43, 0x20000\n\t"
                 "s_mov_b32 m0, %10\n\t"
                 "s_and_b32 s41, s41, 0xffff\n\t"
                 "v_lshlrev_b32 %0, 4, %6\n\t"
                 "s_and_b32 %2, %4, 0xfffffc00\n\t"
                 "buffer_load_dwordx4 %0, s[40:43], %2 offen lds\n\t"
                 "v_lshrrev_b32 %1, 3, %6\n\t"
                 "v_and_b32 %0, 0x70, %0\n\t"
                 "v_lshl_or_b32 %0, %1, 13, %0\n\t"
                 "v_and_b32 %1, 16, %6\n\t"
                 "v_lshlrev_b32 %1, 1, %1\n\t"
                 "v_xor_b32 %0, %0, %1\n\t"
                 "v_xor_b32 %1, 64, %0\n\t"
                 "s_lshl_b32 %2, %8, 15\n\t"
                 "s_and_b32 %2, %2, 0xffe00000\n\t"
                 "s_and_b32 %4, %4, 0x1f80\n\t"
                 "s_or_b32 %2, %2, %4\n\t"
                 "s_mov_b32 %3, %11\n\t"
                 "s_mov_b32 s38, %12\n\t"
                 "s_mov_b32 s39, s43\n\t"
                 "s_waitcnt lgkmcnt(0)\n\t"
                 "s_and_b32 s37, s37, 0xffff\n"
                 "1:\n\t"
                 "s_mov_b32 m0, %3\n\t"
                 "s_add_u32 %3, %3, 0x800\n\t"
                 "buffer_load_dwordx4 %0, s[36:39], %2 offen nt lds\n\t"
                 "s_add_u32 %2, %2, 0xfc00\n\t"
                 "buffer_load_dwordx4 %1, s[36:39], %2 offen offset:1024 nt lds\n\t"
                 "s_add_u32 %2, %2, 0x10400\n\t"
                 "s_cmp_lt_u32 %3, %13\n\t"
                 "s_cbranch_scc1 1b"
                 : "=&v"(vq0), "=&v"(vq1), "=&s"(so), "=&s"(m0v), "=&s"(t0), "={s[36:39]}"(rqw)
                 : "v"(tid), "s"(__builtin_amdgcn_kernarg_segment_ptr()), "s"(c), "i"(PFBYTES), "i"(L_P + 8192), "i"(0), "i"(WQB), "i"(WIN * 1024)
                 : "memory", "scc", "s40", "s41", "s42", "s43");
  }
  SB();
  const int g = lane >> 4, n16 = lane & 15;
  const int grp = c & 7, sp = c >> 3, ab = grp >> 2, ahkv = grp & 3;
  const int ksq = c >> 6, cgq = c & 63;
  const bool has1 = grp != (sp & 7);
  const int c1 = sp * 7 + grp - (grp > (sp & 7) ? 1 : 0);
  const int ks1 = c1 / 56, cg1 = c1 % 56;
  const int cgo = sp & 7;
  const int ho = grp * 4 + (sp >> 3);
  const int ks2 = (sp >> 3) * 8 + grp;
  const bool has2 = ks2 < 28;

  const unsigned vsig = (unsigned)(size_t)smem + (lane == 0 ? L_FL : L_SINK + lane * 4);
  if (wv == NWAVES - 1 && lane < F_WORDS) ((volatile LAS unsigned*)(L + L_FL))[lane] = 0u;
  LGKM0();
  __builtin_amdgcn_s_barrier();
  FENCE();
  if (wv >= 1 && wv < NWAVES - 1) {
    const int w = wv - 1;
    STAMP(0);
    const rsrc_t rws = mk(ws, WS_END);
    const rsrc_t rws2 = mk(ws, has2 ? WS_END : 0u);
    unsigned long long xl = (unsigned long long)x;
    const rsrc_t rxl = mk((const void*)xl, w < 2 ? 4096u : 0u);
    const v4u xa = __builtin_amdgcn_raw_buffer_load_b128(rxl, lane * 16, w * 2048, 0);
    const v4u xb = __builtin_amdgcn_raw_buffer_load_b128(rxl, lane * 16, w * 2048 + 1024, 0);
    FENCE();
    if (w < 2) {
      float xf[16];
#pragma unroll
      for (int i = 0; i < 4; ++i) { xf[2 * i] = blo(xa[i]); xf[2 * i + 1] = bhi(xa[i]); xf[8 + 2 * i] = blo(xb[i]); xf[9 + 2 * i] = bhi(xb[i]); }
      const float piv = asf(__builtin_amdgcn_readfirstlane(asu(xf[0])));
      float s1 = 0.f, s2 = 0.f;
#pragma unroll
      for (int i = 0; i < 16; ++i) { const float d = xf[i] - piv; s1 += d; s2 = fmaf(d, d, s2); }
      float a1 = wave_sum(s1) * (1.0f / 1024), a2 = wave_sum(s2) * (1.0f / 1024);
      float mean = rb(piv + a1), inv = rcp(rb(__builtin_amdgcn_sqrtf(rb(rb(fmaxf(a2 - a1 * a1, 0.f)) + 1e-5f))));
      v4u pa, pb;
      s1 = 0.f; s2 = 0.f;
#pragma unroll
      for (int i = 0; i < 8; ++i) {
        const unsigned t = pk(xf[2 * i] - mean, xf[2 * i + 1] - mean), u = pk(blo(t) * inv, bhi(t) * inv);
        if (i < 4) pa[i] = u; else pb[i - 4] = u;
        xf[2 * i] = blo(u); xf[2 * i + 1] = bhi(u);
        s1 += xf[2 * i] + xf[2 * i + 1]; s2 = fmaf(xf[2 * i], xf[2 * i], fmaf(xf[2 * i + 1], xf[2 * i + 1], s2));
      }
      *(LAS v4u*)(L + L_XS + w * 2048 + lane * 16) = pa;
      *(LAS v4u*)(L + L_XS + w * 2048 + 1024 + lane * 16) = pb;
      a1 = wave_sum(s1) * (1.0f / 1024); a2 = wave_sum(s2) * (1.0f / 1024);
      mean = rb(a1); inv = rcp(rb(__builtin_amdgcn_sqrtf(rb(rb(fmaxf(a2 - a1 * a1, 0.f)) + 1e-5f))));
#pragma unroll
      for (int i = 0; i < 8; ++i) {
        const unsigned t = pk(xf[2 * i] - mean, xf[2 * i + 1] - mean), u = pk(blo(t) * inv, bhi(t) * inv);
        if (i < 4) pa[i] = u; else pb[i - 4] = u;
      }
      *(LAS v4u*)(L + L_YS + w * 2048 + lane * 16) = pa;
      *(LAS v4u*)(L + L_YS + w * 2048 + 1024 + lane * 16) = pb;
      lds_signal<F_LN>(vsig);
    }
    const int q4 = n16 >> 2, p4 = lane & 3;
    lds_wait<LSLP>(L, F_LN, 3);
    if (w == 4) HOP(A);
    if (w == 5) HOP(B);
    if (w == 6) HOP(C);
    if (w == 7) HOP(D);
    const unsigned tag = __builtin_amdgcn_readfirstlane(((volatile LAS unsigned*)(L + L_FL))[F_TAG]);
    const unsigned ta = tag & 0xffu, tb = (tag >> 8) & 0xffu;
    STAMP(1);
    {
      const bool isq = w < 4;
      const LAS unsigned char* ap = L + (isq ? L_XS + ksq * 512 : L_YS + (ks1 & 3) * 512) + (lane & 1) * 2048 + g * 16;
      v4u A[8];
#pragma unroll
      for (int s = 0; s < 8; ++s) A[s] = *(const LAS v4u*)(ap + s * 64);
      lds_wait<LSLP>(L, F_FULL, isq ? 2 : 4);
      const LAS unsigned char* bp = L + (isq ? 0 : 32768) + (8 * g + q4) * 128 + (((w & 3) ^ ((q4 >> 1) | ((g & 1) << 1))) * 32) + p4 * 8;
      v4u Bf[8];
#pragma unroll
      for (int s = 0; s < 8; ++s) Bf[s] = trfrag(bp, s * 4096, s * 4096 + 512);
      lds_signal<F_DQ>(vsig + (isq ? 0 : (F_DH - F_DQ) * 4));
      f4 d0 = mfma32(Bf[0], A[0], f4{0.f, 0.f, 0.f, 0.f}), d1 = mfma32(Bf[1], A[1], f4{0.f, 0.f, 0.f, 0.f});
#pragma unroll
      for (int s = 2; s < 8; s += 2) { d0 = mfma32(Bf[s], A[s], d0); d1 = mfma32(Bf[s + 1], A[s + 1], d1); }
      const rsrc_t rp = mk(ws, (isq || has1) ? WS_END : 0u);
      const int pso = (isq ? WS_QP + cgq * 2048 + ksq * 128 : WS_HP + cg1 * 2048 + ks1 * 128) + (w & 3) * 512;
      stc4(rp, n16 < 2 ? g * 32 + n16 * 16 : OOB, pso, tg4(d0 + d1, ta, tb));
    }
    STAMP(2);
    v4u K[16];
    {
      const LAS unsigned char* kp = L + ((4 + w) & 7) * 16384 + n16 * 256;
      lds_wait<1>(L, F_FULL, 5 + w);
#pragma unroll
      for (int s = 0; s < 4; ++s) {
        const LAS unsigned char* kq = kp + (((4 * s + g) ^ n16) * 16);
#pragma unroll
        for (int t = 0; t < 4; ++t) K[4 * t + s] = *(const LAS v4u*)(kq + t * 4096);
      }
      lds_signal<F_DK>(vsig + w * 4);
    }
    STAMP(3);
    lds_wait<1>(L, F_Q, 1);
    {
      const LAS unsigned char* qb = L + L_QS + (n16 >> 1) * 272 + g * 16;
      v4u qf[4];
#pragma unroll
      for (int s = 0; s < 4; ++s) qf[s] = *(const LAS v4u*)(qb + s * 64);
      f4 sc[4];
#pragma unroll
      for (int t = 0; t < 4; ++t) sc[t] = mfma32(K[4 * t], qf[0], f4{0.f, 0.f, 0.f, 0.f});
#pragma unroll
      for (int s = 1; s < 4; ++s)
#pragma unroll
        for (int t = 0; t < 4; ++t) sc[t] = mfma32(K[4 * t + s], qf[s], sc[t]);
#pragma unroll
      for (int t = 0; t < 4; ++t)
#pragma unroll
        for (int r = 0; r < 4; r += 2) {
          const unsigned u = pk(sc[t][r], sc[t][r + 1]), v = pk(blo(u) * 0.088388347648318440f, bhi(u) * 0.088388347648318440f);
          sc[t][r] = blo(v); sc[t][r + 1] = bhi(v);
        }
      float mw = fmaxf(fmaxf(sc[0].x, sc[0].y), fmaxf(sc[0].z, sc[0].w));
#pragma unroll
      for (int t = 1; t < 4; ++t) mw = fmaxf(mw, fmaxf(fmaxf(sc[t].x, sc[t].y), fmaxf(sc[t].z, sc[t].w)));
      mw = x32_max(x16_max(mw)) * LOG2E;
      float lw = 0.f;
      const unsigned pm = (lane & 1) ? 0xffff0000u : 0u;
      unsigned P[8];
#pragma unroll
      for (int t = 0; t < 4; ++t)
#pragma unroll
        for (int r = 0; r < 4; r += 2) {
          const float v0 = ex2(fmaf(sc[t][r], LOG2E, -mw)), v1 = ex2(fmaf(sc[t][r + 1], LOG2E, -mw));
          lw += v0 + v1;
          const unsigned h = pk(v0, v1);
          P[2 * t + (r >> 1)] = pk(v0 - asf((h << 16) & pm), v1 - asf(h & pm));
        }
      *(LAS v4u*)(L + L_P + w * 2048 + lane * 16) = v4u{P[0], P[1], P[2], P[3]};
      *(LAS v4u*)(L + L_P + w * 2048 + 1024 + lane * 16) = v4u{P[4], P[5], P[6], P[7]};
      lw = x32_sum(x16_sum(lw));
      if (lane < 16 && !(lane & 1)) *(LAS f2*)(L + L_ST + (w * 8 + (lane >> 1)) * 8) = f2{mw, lw};
      lds_signal<F_P>(vsig);
    }
    STAMP(4);
    lds_wait<1>(L, F_P, 8);
    float o0 = 0.f, o1 = 0.f;
    {
      f4 st[8];
#pragma unroll
      for (int j = 0; j < 8; ++j) st[j] = *(const LAS f4*)(L + L_ST + (j * 8 + 2 * g) * 8);
      float M0 = st[0].x, M1 = st[0].z;
#pragma unroll
      for (int j = 1; j < 8; ++j) { M0 = fmaxf(M0, st[j].x); M1 = fmaxf(M1, st[j].z); }
      M0 = __builtin_ceilf(M0); M1 = __builtin_ceilf(M1);
#pragma unroll
      for (int j = 0; j < 8; ++j) { st[j].x = ex2(st[j].x - M0); st[j].z = ex2(st[j].z - M1); }
      STAMP(5);
      int ovs = (lane & 2) ? OOB : (2 * g + (lane & 1)) * 32768 + (n16 >> 2) * 16;
      asm volatile("" : "+v"(ovs));
      const LAS unsigned char* vp = L + (4 * g + q4) * 256 + ((w ^ (4 * (g & 1) + q4)) * 32) + p4 * 8;
#pragma unroll
      for (int j = 0; j < 8; ++j) {
        const v4u pa = *(const LAS v4u*)(L + L_P + j * 2048 + lane * 16), pb = *(const LAS v4u*)(L + L_P + j * 2048 + 1024 + lane * 16);
        if (j < 7) lds_wait<LSLP>(L, F_FULL, 13 + j); else { lds_wait<0>(L, F_FULL, 20); PAD(B, 0, 1); }
        const LAS unsigned char* vq = vp + ((4 + j) & 7) * 16384;
        const v4u va = trfrag(vq, 0, 4096), vb = trfrag(vq, 8192, 12288);
        if (j == 0) lds_signal<F_DV>(vsig); else if (j == 1) lds_signal<F_DV + 1>(vsig); else if (j == 2) lds_signal<F_DV + 2>(vsig); else if (j == 3) lds_signal<F_DV + 3>(vsig);
        f4 d = mfma32(pa, va, f4{0.f, 0.f, 0.f, 0.f});
        d = mfma32(pb, vb, d);
        o0 = fmaf(st[j].x, d.x + d.y, o0);
        o1 = fmaf(st[j].z, d.z + d.w, o1);
      }
      PAD(B, 1, 2);
      const bool odd = (lane & 1) != 0;
      const float rcv = dpp<XOR1>(odd ? o0 : o1);
      const unsigned gr = odd ? pk(rcv, o1) : pk(o0, rcv);
      const unsigned gn = dppu<XOR2>(gr);
      stc4(rws, ovs, WS_OP + ((ahkv * 64 + w) * 32 + sp) * 128 + ab * 64, v4u{gr, tag, gn, tag});
    }
    STAMP(6);
    const LAS unsigned char* wp = L + (8 * g + q4) * 256 + ((w ^ (q4 | ((g & 1) << 2))) * 32) + p4 * 8;
    const int fvo = (g >> 1) * 2048 + (g & 1) * 32;
    v4u MPv;
    {
      v4u A[4];
      lds_wait<1>(L, F_H, 1);
#pragma unroll
      for (int s = 0; s < 4; ++s) A[s] = *(const LAS v4u*)(L + L_HS + (lane & 1) * 272 + s * 64 + g * 16);
      lds_wait<LSLP>(L, F_FULL, 22);
      PAD(B, 2, 3);
      f4 d = {0.f, 0.f, 0.f, 0.f};
#pragma unroll
      for (int s = 0; s < 4; ++s) d = mfma32(trfrag(wp + 4 * 16384, s * 8192, s * 8192 + 1024), A[s], d);
      PAD(B, 3, 4);
      MPv = tg4(d, ta, tb);
      if (!MPLATE) stc4(rws2, n16 < 2 ? fvo + n16 * 16 : OOB, WS_MP + ((cgo * 16 + 2 * w) * 32 + ks2) * 64, MPv);
    }
    STAMP(7);
    {
      const int ovo = ((n16 >> 2) & 1) * 2048 + g * 512 + (n16 >> 3) * 64 + (lane & 3) * 16, oso = WS_OP + (ho * 8 + w) * 4096;
      v4u G[4];
      lds_wait<0>(L, F_FULL, OGATE);
      PAD(B, 4, 5);
#pragma unroll
      for (int j = 0; j < 4; ++j) G[j] = ldc4(rws, ovo + j * 128, oso);
      SB();
      if (MPLATE) stc4(rws2, n16 < 2 ? fvo + n16 * 16 : OOB, WS_MP + ((cgo * 16 + 2 * w) * 32 + ks2) * 64, MPv);
      SB();
      lds_wait<0>(L, F_W, 1);
      PAD(B, 5, e);
      v2u WF = *(const LAS v2u*)(L + L_WT + (lane & 3) * 32 + g * 8);
      float invl = ((const LAS float*)(L + L_WT + 128))[(lane >> 1) & 1];
      LAS unsigned char* osw = L + (n16 == g ? L_OS + g * 272 + w * 32 : L_QS + lane * 32);
      const LAS unsigned char* osr = L + L_OS + (lane & 3) * 272 + g * 16;
      const LAS unsigned char* wop = wp + 6 * 16384;
      int fv2 = (n16 & 13) == 0 ? fvo + (n16 >> 1) * 16 : OOB, fso = WS_FP + ((cgo * 16 + 2 * w) * 32 + ho) * 64;
      unsigned vta = ta, vtb = tb, vtag = tag;
      asm volatile("" : "+v"(WF), "+v"(invl), "+v"(osw), "+v"(osr), "+v"(wop), "+v"(fv2), "+s"(fso), "+v"(vta), "+v"(vtb), "+v"(vtag));
      lds_wait<0>(L, F_FULL, 24);
      PAD(A, 0, 1);
      v4u Bo[4];
#pragma unroll
      for (int s = 0; s < 4; ++s) Bo[s] = trfrag(wop, s * 8192, s * 8192 + 1024);
      SB();
      STAMP(8);
      const f4 z = {0.f, 0.f, 0.f, 0.f};
      f4 e0, e1, e2, e3;
#define COMBINE() do { \
        e0 = mfma16(v2u{plo(G[0].x, G[1].x), plo(G[2].x, G[3].x)}, WF, z); e1 = mfma16(v2u{phi(G[0].x, G[1].x), phi(G[2].x, G[3].x)}, WF, z); \
        e2 = mfma16(v2u{plo(G[0].z, G[1].z), plo(G[2].z, G[3].z)}, WF, z); e3 = mfma16(v2u{phi(G[0].z, G[1].z), phi(G[2].z, G[3].z)}, WF, z); } while (0)
#define OBAD(j, acc) xoror(G[j].y, vtag, xoror(G[j].w, vtag, acc))
      PAD(A, 1, 2);
      COMBINE();
      PAD(A, 2, 3);
      if (__builtin_expect(__any((OBAD(0, OBAD(1, 0u)) | OBAD(2, OBAD(3, 0u))) != 0u), 0)) {
#pragma nounroll
        for (unsigned spin = 0; spin < SPIN_CAP; ++spin) {
          const bool m0 = __any(OBAD(0, 0u) != 0u), m1 = __any(OBAD(1, 0u) != 0u), m2 = __any(OBAD(2, 0u) != 0u), m3 = __any(OBAD(3, 0u) != 0u);
          if (!(m0 || m1 || m2 || m3)) break;
          FENCE();
          if (m0) G[0] = ldc4(rws, ovo, oso);
          if (m1) G[1] = ldc4(rws, ovo + 128, oso);
          if (m2) G[2] = ldc4(rws, ovo + 256, oso);
          if (m3) G[3] = ldc4(rws, ovo + 384, oso);
        }
        COMBINE();
      }
      STAMP(9);
      *(LAS v4u*)osw = v4u{pk(e0.x, e1.x), pk(e2.x, e3.x), pk(e0.y, e1.y), pk(e2.y, e3.y)};
      *(LAS v4u*)(osw + 16) = v4u{pk(e0.z, e1.z), pk(e2.z, e3.z), pk(e0.w, e1.w), pk(e2.w, e3.w)};
      PAD(A, 3, 4);
      LGKM0();
      __builtin_amdgcn_s_barrier();
      FENCE();
      v4u a0 = *(const LAS v4u*)osr, a1 = *(const LAS v4u*)(osr + 64), a2 = *(const LAS v4u*)(osr + 128), a3 = *(const LAS v4u*)(osr + 192);
      SB();
      f4 d = mfma32(Bo[0], a0, z), d2 = mfma32(Bo[1], a1, z);
      d = mfma32(Bo[2], a2, d); d2 = mfma32(Bo[3], a3, d2);
      PAD(A, 4, 5);
      d += d2;
      d.x += dpp<XOR1>(d.x); d.y += dpp<XOR1>(d.y); d.z += dpp<XOR1>(d.z); d.w += dpp<XOR1>(d.w);
      d *= invl;
      stc4(rws, fv2, fso, v4u{(asu(d.x) & 0xffffff00u) | vta, (asu(d.y) & 0xffffff00u) | vtb, (asu(d.z) & 0xffffff00u) | vta, (asu(d.w) & 0xffffff00u) | vtb});
    }
    PAD(A, 5, e);
    STAMP(10);
  } else if (wv == NWAVES - 1) {
    STAMP(0);
    volatile LAS unsigned* fl = (volatile LAS unsigned*)(L + L_FL);
    const rsrc_t rws = mk(ws, WS_END), rws1 = mk(ws, has1 ? WS_END : 0u), rws2 = mk(ws, has2 ? WS_END : 0u), rout = mk(out, 4096);
    const unsigned eraw = __builtin_amdgcn_raw_buffer_load_b32(rws, 0, WS_E, AUX_SC1);
    const unsigned b1p = __builtin_amdgcn_raw_buffer_load_b32(mk(b1, has1 ? 7168u : 0u), (n16 >> 2) * 8 + (lane & 1) * 4, c1 * 32, 0);
    const int col2 = ((lane >> 1) & 1) * 8 + ((g >> 1) | ((g & 1) << 1)) * 2;
    const int fo = (lane & 1) * 2048 + col2;
    const unsigned short xfin = __builtin_amdgcn_raw_buffer_load_b16(mk(x, c < 128 ? 4096u : 0u), fo, c * 16, 0);
    const unsigned short b2fin = __builtin_amdgcn_raw_buffer_load_b16(mk(b2, c < 128 ? 2048u : 0u), col2, c * 16, 0);
    FENCE();
    const unsigned tag = __builtin_amdgcn_readfirstlane(eraw) * 0x9E3779B1u + 0x7F4A7C15u;
    const unsigned ta = tag & 0xffu, tb = (tag >> 8) & 0xffu;
    fl[F_TAG] = tag;
    lds_signal<F_LN>(vsig);
    lds_wait<1>(L, F_FULL, 4);
    const unsigned t4 = (unsigned)__builtin_amdgcn_s_memrealtime();
    lds_wait<LSLP>(L, F_FULL, S1GATE);
    {
      const int fvo = (lane < 16 && !(lane & 1)) ? (lane >> 2) * 16 : OOB, fb = (lane >> 1) & 1;
      bool needq = true, needh = has1;
#pragma nounroll
      for (unsigned spin = 0; (needq || needh) && spin < SPIN_CAP; ++spin) {
        const v2u GQ = ldc2(rws, needq ? lane * 8 : OOB, WS_QP + c * 512), GH = ldc2(rws1, needh ? lane * 8 : OOB, WS_HP + c1 * 512);
        if (needq && !__any((((GQ.x ^ ta) | (GQ.y ^ tb)) & 0xffu) != 0u)) {
          const unsigned gr = pk(x32_sum(x16_sum(untg(GQ.x))), x32_sum(x16_sum(untg(GQ.y)))), gn = dppu<XOR1>(gr), ty = (tag << 12) | (t4 & 0xfffu);
          stc4(rws, fvo + fb * 16384, WS_QF + c * 64, v4u{gr, ty, gn, ty});
          needq = false;
          STAMP(13);
        }
        if (needh && !__any((((GH.x ^ ta) | (GH.y ^ tb)) & 0xffu) != 0u)) {
          float h0 = rb(rb(x32_sum(x16_sum(untg(GH.x)))) + blo(b1p)), h1 = rb(rb(x32_sum(x16_sum(untg(GH.y)))) + bhi(b1p));
          constexpr float GC = -2.0f * 0.7978845608028654f * 1.4426950408889634f;
          h0 = h0 * rcp(1.0f + ex2(GC * (h0 + 0.044715f * h0 * h0 * h0)));
          h1 = h1 * rcp(1.0f + ex2(GC * (h1 + 0.044715f * h1 * h1 * h1)));
          const unsigned gr = pk(h0, h1), gn = dppu<XOR1>(gr);
          stc4(rws, fvo + fb * 14336, WS_HF + c1 * 64, v4u{gr, tag, gn, tag});
          needh = false;
          STAMP(14);
        }
        __builtin_amdgcn_s_sleep(PSLP);
      }
    }
    lds_wait<LSLP>(L, F_FULL, QGATE);
    {
      v4u Q[4];
      const unsigned t20 = tag & 0xfffffu;
#pragma nounroll
      for (unsigned spin = 0; spin < SPIN_CAP; ++spin) {
        unsigned bad = 0;
#pragma unroll
        for (int i = 0; i < 4; ++i) Q[i] = ldc4(rws, lane * 16, WS_QF + grp * 4096 + i * 1024);
#pragma unroll
        for (int i = 0; i < 4; ++i) bad |= ((Q[i].y >> 12) ^ t20) | ((Q[i].w >> 12) ^ t20);
        if (!__any(bad != 0u)) break;
        __builtin_amdgcn_s_sleep(PSLP);
      }
#pragma unroll
      for (int i = 0; i < 4; ++i) *(LAS v2u*)(L + L_QS + (2 * i + (lane >> 5)) * 272 + (lane & 31) * 8) = v2u{Q[i].x, Q[i].z};
      lds_signal<F_Q>(vsig);
      STAMP(15);
      if (CTL) {
        int d = 0;
#pragma unroll
        for (int i = 0; i < 4; ++i) d += (int)((Q[i].y - t4) << 20) >> 20;
        fl[F_REF] = (t4 + (int)(wave_sum((float)d) * (1.0f / 256))) | 1u;
      }
    }
    lds_wait<1>(L, F_P, 8);
    {
      f2 sv[8];
#pragma unroll
      for (int j = 0; j < 8; ++j) sv[j] = *(const LAS f2*)(L + L_ST + (j * 8 + (lane & 7)) * 8);
      float M = sv[0].x, Ls = 0.f;
#pragma unroll
      for (int j = 1; j < 8; ++j) M = fmaxf(M, sv[j].x);
      M = __builtin_ceilf(M);
#pragma unroll
      for (int j = 0; j < 8; ++j) Ls = fmaf(ex2(sv[j].x - M), sv[j].y, Ls);
      stc4(rws, lane < 8 ? (lane >> 2) * 2048 + (lane & 3) * 16 : OOB, WS_ST + grp * 4096 + sp * 64, v4u{asu(M), tag, asu(Ls), tag});
      const unsigned tag2 = has2 ? tag : 0u;
      v4u H;
#pragma nounroll
      for (unsigned spin = 0; spin < SPIN_CAP; ++spin) {
        H = ldc4(rws2, (lane >> 5) * 14336 + (lane & 31) * 16, WS_HF + ks2 * 512);
        if (!__any(((H.y ^ tag2) | (H.w ^ tag2)) != 0u)) break;
        __builtin_amdgcn_s_sleep(PSLP);
      }
      *(LAS v2u*)(L + L_HS + (lane >> 5) * 272 + (lane & 31) * 8) = v2u{H.x, H.z};
      lds_signal<F_H>(vsig);
      STAMP(16);
    }
    lds_wait<LSLP>(L, F_FULL, SGATE);
    {
      v4u SG;
#pragma nounroll
      for (unsigned spin = 0; spin < SPIN_CAP; ++spin) {
        SG = ldc4(rws, (lane >> 5) * 16384 + (lane & 31) * 64, WS_ST + (ho >> 2) * 2048 + (ho & 3) * 16);
        if (!__any(((SG.y ^ tag) | (SG.w ^ tag)) != 0u)) break;
        __builtin_amdgcn_s_sleep(PSLP);
      }
      const float m = asf(SG.x), M = x16_max(row_max(m));
      const float e = rb(ex2(m - M)), Ls = x16_sum(row_sum(e * asf(SG.z)));
      ((LAS unsigned short*)(L + L_WT))[lane] = (unsigned short)(asu(e) >> 16);
      ((LAS float*)(L + L_WT + 128))[lane >> 5] = rcp(Ls) * (1.0f + 1.0f / 131072);
      lds_signal<F_W>(vsig);
      STAMP(17);
    }
    {
      const rsrc_t rwf = mk(ws, c < 128 ? WS_END : 0u);
      const unsigned mlive = lane < 48 ? 0xffffffffu : 0u;
      __builtin_amdgcn_s_barrier();
      FENCE();
      PAD(D, 0, 1);
      if (c < 128) {
        int so = WS_MP + c * 2048, ov = OOB, ev = OOB, pass = 0;
        unsigned live = mlive;
        float gt = 0.f;
        const float xv = blo(xfin), bv = blo(b2fin);
        asm volatile("" : "+s"(pass));
#pragma nounroll
        for (;;) {
          v4u A0 = ldc4(rwf, lane * 16, so), A1 = ldc4(rwf, lane * 16, so + 1024);
#pragma nounroll
          for (unsigned spin = 0; spin < SPIN_CAP; ++spin) {
            const bool m0 = __any(bad4(A0, ta, tb) != 0u), m1 = __any((bad4(A1, ta, tb) & live) != 0u);
            if (!(m0 || m1)) break;
            FENCE();
            if (m0) A0 = ldc4(rwf, lane * 16, so);
            if (m1) A1 = ldc4(rwf, lane * 16, so + 1024);
          }
          if (pass) { STAMP(25); }
          PAD(D, 1, e);
          const float r = rb(red4(A0, A1 & live));
          const float o = xv + rb(r * gt);
          __builtin_amdgcn_raw_buffer_store_b16((unsigned short)pk(o, o), rout, ov, c * 16, OAUX);
          __builtin_amdgcn_raw_buffer_store_b32(tag, rws, ev, WS_E, EAUX);
          SB();
          if (pass) break;
          const float m = rb(r + bv);
          gt = rb(m * rcp(1.0f + ex2(-1.4426950408889634f * m)));
          pass = 1; so = WS_FP + c * 2048; live = 0xffffffffu;
          ov = (lane & 12) == 0 ? fo : OOB; ev = (c | lane) == 0 ? 0 : OOB;
          asm volatile("" : "+s"(pass), "+s"(so), "+v"(live), "+v"(ov), "+v"(ev), "+v"(gt));
        }
        STAMP(26);
      }
    }
  } else {
    STAMP(0);
    const rsrc_t rq = mk(wq, WQB);
    const int r8 = lane >> 3, c8 = lane & 7, f0 = r8 & 2;
    volatile LAS unsigned* fl = (volatile LAS unsigned*)(L + L_FL);
    const unsigned lds0 = (unsigned)(size_t)smem;
    Ld st = {WIN, 16, 0, lds0 + L_FL + F_FULL * 4};
    unsigned w4 = WIN - 4, to = 0x7fffffffu;
#pragma nounroll
    for (int i = WIN / 4; i < 8; ++i) { pace(st, w4, to); dma4<8 * 8192>(rq, vq0, vq1, vq0, vq1, so, m0v); st.cnt += 4; }
    to = TOMID;
    {
      const int v0 = r8 * 7168 + (c8 ^ f0) * 16;
      stream<8 * 7168, 2, 2>(L, st, mk(w1, has1 ? W1B : 0u), v0, v0 ^ 64, v0, v0 ^ 64, ks1 * 256 * 7168 + cg1 * 128, 0, lds0, w4, to);
    }
    PAD(C, 0, 1);
    const int kvb = (grp * 16384 + sp * 512) * 256;
    {
      const int v0 = g * 256 + (n16 ^ g) * 16;
      stream<1024, 4, 8>(L, st, mk(kc, KVB), v0, v0 ^ 64, v0 ^ 128, v0 ^ 192, kvb, sp, lds0, w4, to);
    }
    PAD(C, 1, 2);
    {
      const int v0 = g * 256 + (n16 ^ (2 * g)) * 16;
      stream<1024, 12, 8>(L, st, mk(vc, KVB), v0, v0 ^ 128, v0, v0 ^ 128, kvb, sp, lds0, w4, to);
    }
    PAD(C, 2, 3);
    {
      const int v0 = g * 2048 + (n16 ^ (2 * g)) * 16;
      stream<8192, 20, 2>(L, st, mk(w2, has2 ? W1B : 0u), v0, v0, v0 ^ 128, v0 ^ 128, ks2 * 128 * 2048 + cgo * 256, 0, lds0, w4, to);
      PAD(C, 3, 4);
      stream<8192, 22, 2>(L, st, mk(wo, WQB), v0, v0, v0 ^ 128, v0 ^ 128, ho * 128 * 2048 + cgo * 256, 0, lds0, w4, to);
    }
    PAD(C, 4, e);
    pace(st, 0u, 0x7fffffffu);
    asm volatile("s_waitcnt vmcnt(0)" ::: "memory");
    fl[F_FULL] = 24;
  }
}

}

extern "C" void kernel_launch(void* const* d_in, const int* in_sizes, int n_in, void* d_out, int out_size, void* d_ws, size_t ws_size,
                              hipStream_t stream) {
  (void)in_sizes; (void)n_in; (void)out_size; (void)ws_size;
  fused_decode<<<256, NT, 0, stream>>>((const unsigned short*)d_in[0], d_in[1], d_in[2], d_in[3], d_in[4], d_in[6], d_in[7], d_in[8], d_in[9],
                                       d_out, d_ws);
}
```

```cpp
#include <hip/hip_runtime.h>
#include <hip/hip_bf16.h>

namespace {

typedef unsigned v4u __attribute__((ext_vector_type(4)));
typedef unsigned v2u __attribute__((ext_vector_type(2)));
typedef float f4 __attribute__((ext_vector_type(4)));
typedef float f2 __attribute__((ext_vector_type(2)));
typedef __bf16 bf8 __attribute__((ext_vector_type(8)));
typedef __bf16 bf2 __attribute__((ext_vector_type(2)));
typedef short s4 __attribute__((ext_vector_type(4)));
typedef __amdgpu_buffer_rsrc_t rsrc_t;
#define LAS __attribute__((address_space(3)))

constexpr int NWAVES = 10;
constexpr int NT = 64 * NWAVES;
constexpr unsigned KVB = 2u * 4 * 16384 * 128 * 2, WQB = 1024u * 4096 * 2, W1B = 1024u * 3584 * 2;
constexpr unsigned WS_E = 0;
constexpr unsigned WS_QP = 4096;
constexpr unsigned WS_HP = WS_QP + 131072;
constexpr unsigned WS_QF = WS_HP + 114688;
constexpr unsigned WS_HF = WS_QF + 32768;
constexpr unsigned WS_ST = WS_HF + 28672;
constexpr unsigned WS_OP = WS_ST + 32768;
constexpr unsigned WS_FP = WS_OP + 1048576;
constexpr unsigned WS_MP = WS_FP + 262144;
constexpr unsigned WS_END = WS_MP + 262144;
constexpr int L_P = 131072, L_XS = L_P, L_YS = L_P + 4096, L_QS = L_P + 16384, L_HS = L_QS + 8 * 272, L_OS = L_HS + 2 * 272,
              L_ST = L_OS + 4 * 272, L_WT = L_ST + 512, L_FL = L_WT + 256, L_SINK = L_FL + 128, L_STAMP = L_SINK + 512, L_END = L_STAMP + NWAVES * 256;
constexpr int LDS_BYTES = L_END;
static_assert(L_END <= 163840 && (L_HS % 16) == 0 && (L_OS % 16) == 0 && (L_ST % 16) == 0 && (L_WT % 16) == 0 && (L_FL % 16) == 0, "lds map");
enum { F_FULL = 0, F_LN = 1, F_Q = 2, F_P = 3, F_H = 4, F_REF = 5, F_TAG = 6, F_W = 7, F_D = 8, F_DQ = F_D, F_DH = F_D + 2, F_DK = F_D + 4, F_DV = F_D + 12, F_WORDS = 32 };
constexpr int OOB = 0x7ff00000;
constexpr int AUX_SC1 = 16;
constexpr unsigned SPIN_CAP = 1u << 15, LSPIN_CAP = 1u << 20;
constexpr float LOG2E = 1.4426950408889634f;
#define WIN 24
#define WLO 17
#define WMID 25
#define WHI 33
#define NMID 4
#define NLO 8
#define TAU 56
#define TAU2 46
#define TOFF 15
#define TTH 20
#define S1GATE 6
#define QGATE 9
#define SGATE 19
#define OGATE 23
#define SHIFT 0
constexpr int PFBYTES = 14336;
static_assert(WIN % 4 == 0 && WIN <= 32 && WHI <= 60, "first burst in whole groups inside the wq tile; vmcnt has 6 bits");

__device__ __forceinline__ rsrc_t mk(const void* p, unsigned bytes) { return __builtin_amdgcn_make_buffer_rsrc((void*)p, 0, bytes, 0x00020000); }
__device__ __forceinline__ v4u ldc4(rsrc_t r, int vo, int so) { return __builtin_amdgcn_raw_buffer_load_b128(r, vo, so, AUX_SC1); }
__device__ __forceinline__ v2u ldc2(rsrc_t r, int vo, int so) { return __builtin_amdgcn_raw_buffer_load_b64(r, vo, so, AUX_SC1); }
__device__ __forceinline__ void stc4(rsrc_t r, int vo, int so, v4u v) { __builtin_amdgcn_raw_buffer_store_b128(v, r, vo, so, AUX_SC1); }
__device__ __forceinline__ float asf(unsigned u) { return __builtin_bit_cast(float, u); }
__device__ __forceinline__ unsigned asu(float f) { return __builtin_bit_cast(unsigned, f); }
__device__ __forceinline__ unsigned pk(float a, float b) { f2 v = {a, b}; return __builtin_bit_cast(unsigned, __builtin_convertvector(v, bf2)); }
__device__ __forceinline__ float blo(unsigned u) { return asf(u << 16); }
__device__ __forceinline__ float bhi(unsigned u) { return asf(u & 0xffff0000u); }
__device__ __forceinline__ float rb(float a) { return blo(pk(a, a)); }
__device__ __forceinline__ unsigned plo(unsigned a, unsigned b) { return __builtin_amdgcn_perm(b, a, 0x05040100u); }
__device__ __forceinline__ unsigned phi(unsigned a, unsigned b) { return __builtin_amdgcn_perm(b, a, 0x07060302u); }
__device__ __forceinline__ f4 mfma32(v4u a, v4u b, f4 c) { return __builtin_amdgcn_mfma_f32_16x16x32_bf16(__builtin_bit_cast(bf8, a), __builtin_bit_cast(bf8, b), c, 0, 0, 0); }
__device__ __forceinline__ f4 mfma16(v2u a, v2u b, f4 c) { return __builtin_amdgcn_mfma_f32_16x16x16bf16_1k(__builtin_bit_cast(s4, a), __builtin_bit_cast(s4, b), c, 0, 0, 0); }
template <int C> __device__ __forceinline__ float dpp(float x) { return asf(__builtin_amdgcn_mov_dpp(asu(x), C, 0xf, 0xf, true)); }
template <int C> __device__ __forceinline__ unsigned dppu(unsigned x) { return __builtin_amdgcn_mov_dpp(x, C, 0xf, 0xf, true); }
constexpr int XOR1 = 0xB1, XOR2 = 0x4E, ROR4 = 0x124, ROR8 = 0x128;
__device__ __forceinline__ float x16_sum(float x) { auto s = __builtin_amdgcn_permlane16_swap(asu(x), asu(x), false, false); return asf(s[0]) + asf(s[1]); }
__device__ __forceinline__ float x32_sum(float x) { auto s = __builtin_amdgcn_permlane32_swap(asu(x), asu(x), false, false); return asf(s[0]) + asf(s[1]); }
__device__ __forceinline__ float x16_max(float x) { auto s = __builtin_amdgcn_permlane16_swap(asu(x), asu(x), false, false); return fmaxf(asf(s[0]), asf(s[1])); }
__device__ __forceinline__ float x32_max(float x) { auto s = __builtin_amdgcn_permlane32_swap(asu(x), asu(x), false, false); return fmaxf(asf(s[0]), asf(s[1])); }
__device__ __forceinline__ float row_sum(float x) { x += dpp<XOR1>(x); x += dpp<XOR2>(x); x += dpp<ROR4>(x); x += dpp<ROR8>(x); return x; }
__device__ __forceinline__ float row_max(float x) { x = fmaxf(x, dpp<XOR1>(x)); x = fmaxf(x, dpp<XOR2>(x)); x = fmaxf(x, dpp<ROR4>(x)); x = fmaxf(x, dpp<ROR8>(x)); return x; }
__device__ __forceinline__ float wave_sum(float x) { return x32_sum(x16_sum(row_sum(x))); }
__device__ __forceinline__ float ex2(float x) { return __builtin_amdgcn_exp2f(x); }
__device__ __forceinline__ float rcp(float x) { return __builtin_amdgcn_rcpf(x); }
__device__ __forceinline__ unsigned tg(float v, unsigned t8) { return ((asu(v) + 0x80u) & 0xffffff00u) | t8; }
__device__ __forceinline__ v4u tg4(f4 v, unsigned ta, unsigned tb) { return v4u{tg(v.x, ta), tg(v.y, tb), tg(v.z, ta), tg(v.w, tb)}; }
__device__ __forceinline__ float untg(unsigned u) { return asf(u & 0xffffff00u); }
__device__ __forceinline__ unsigned xoror(unsigned a, unsigned b, unsigned c) { return __builtin_amdgcn_bitop3_b32(a, b, c, 0xBE); }
__device__ __forceinline__ unsigned bad4(v4u u, unsigned ta, unsigned tb) { return xoror(u.x, ta, xoror(u.y, tb, xoror(u.z, ta, u.w ^ tb))) & 0xffu; }
__device__ __forceinline__ float red4(v4u a, v4u b) {
  const auto s01 = __builtin_amdgcn_permlane32_swap(asu(untg(a.x) + untg(b.x)), asu(untg(a.y) + untg(b.y)), false, false);
  const auto s23 = __builtin_amdgcn_permlane32_swap(asu(untg(a.z) + untg(b.z)), asu(untg(a.w) + untg(b.w)), false, false);
  const auto t = __builtin_amdgcn_permlane16_swap(asu(asf(s01[0]) + asf(s01[1])), asu(asf(s23[0]) + asf(s23[1])), false, false);
  float v = asf(t[0]) + asf(t[1]);
  v += dpp<ROR4>(v); v += dpp<ROR8>(v);
  return v;
}
__device__ __forceinline__ v4u trfrag(const LAS unsigned char* a, int off0, int off1) {
  const v2u lo = __builtin_bit_cast(v2u, __builtin_amdgcn_ds_read_tr16_b64_v4i16((LAS s4*)(a + off0)));
  const v2u hi = __builtin_bit_cast(v2u, __builtin_amdgcn_ds_read_tr16_b64_v4i16((LAS s4*)(a + off1)));
  return v4u{lo.x, lo.y, hi.x, hi.y};
}

#define SB() __builtin_amdgcn_sched_barrier(0)
#define FENCE() do { SB(); asm volatile("" ::: "memory"); SB(); } while (0)
#define LGKM0() do { SB(); asm volatile("s_waitcnt lgkmcnt(0)" ::: "memory"); SB(); } while (0)
#define STAMP(i)

template <int SLEEP> __device__ __forceinline__ void lds_wait(LAS unsigned char* L, int word, unsigned want) {
  FENCE();
  const volatile LAS unsigned* p = (const volatile LAS unsigned*)(L + L_FL) + word;
#pragma nounroll
  for (unsigned i = 0; i < LSPIN_CAP; ++i) {
    if ((unsigned)__builtin_amdgcn_readfirstlane(*p) >= want) break;
    if (SLEEP) __builtin_amdgcn_s_sleep(SLEEP);
  }
  FENCE();
}
template <int WORD> __device__ __forceinline__ void lds_signal(unsigned vsig) {
  SB();
  asm volatile("s_waitcnt lgkmcnt(0)\n\tds_add_u32 %0, %1 offset:%2" :: "v"(vsig), "v"(1u), "i"(WORD * 4) : "memory");
  SB();
}
struct Ld { int gap; unsigned pub, vfull; };
template <int GS>
__device__ __forceinline__ void groups(Ld& s, rsrc_t r, int v0, int v1, int v2, int v3, int& so, unsigned& m0v, unsigned m0end, unsigned w4, unsigned to) {
  unsigned a, b, v, n;
  asm volatile("s_mov_b32 %3, 0\n"
               "0:\n\t"
               "s_getreg_b32 %0, hwreg(HW_REG_IB_STS)\n\t"
               "s_add_u32 %3, %3, 1\n\t"
               "s_and_b32 %1, %0, 15\n\t"
               "s_bfe_u32 %0, %0, 0x20016\n\t"
               "s_lshl4_add_u32 %0, %0, %1\n\t"
               "s_cmp_le_i32 %0, %4\n\t"
               "s_cbranch_scc1 2f\n"
               "1:\n\t"
               "s_cmp_ge_u32 %3, %16\n\t"
               "s_cselect_b32 %1, %17, %15\n\t"
               "s_cmp_le_u32 %0, %1\n\t"
               "s_cbranch_scc0 0b\n\t"
               "s_mov_b32 m0, %7\n\t"
               "s_add_u32 %7, %7, 0x1000\n\t"
               "buffer_load_dwordx4 %9, %13, %6 offen nt lds\n\ts_add_u32 %6, %6, %18\n\ts_mov_b32 %3, 0\n\t"
               "buffer_load_dwordx4 %10, %13, %6 offen offset:1024 nt lds\n\ts_add_u32 %6, %6, %18\n\ts_add_u32 %4, %4, 4\n\t"
               "buffer_load_dwordx4 %11, %13, %6 offen offset:2048 nt lds\n\ts_add_u32 %6, %6, %18\n\t"
               "buffer_load_dwordx4 %12, %13, %6 offen offset:3072 nt lds\n\ts_add_u32 %6, %6, %19\n\t"
               "s_cmp_lt_u32 %7, %14\n\t"
               "s_cbranch_scc1 0b\n\t"
               "s_branch 3f\n"
               "2:\n\t"
               "s_add_u32 %5, %5, 1\n\t"
               "v_mov_b32 %2, %5\n\t"
               "ds_write_b32 %8, %2\n\t"
               "s_sub_u32 %4, %4, 16\n\t"
               "s_branch 1b\n"
               "3:"
               : "=&s"(a), "=&s"(b), "=&v"(v), "=&s"(n), "+s"(s.gap), "+s"(s.pub), "+s"(so), "+s"(m0v)
               : "v"(s.vfull), "v"(v0), "v"(v1), "v"(v2), "v"(v3), "s"(r), "s"(m0end), "s"(w4), "s"(to), "i"(WHI - 4), "i"(GS - 1024), "i"(GS + 3072)
               : "memory", "scc");
}
template <int GS, int F0, int NF>
__device__ __forceinline__ void stream(LAS unsigned char* L, Ld& s, rsrc_t r, int v0, int v1, int v2, int v3, int so, int rot, unsigned lds0, unsigned& w4, unsigned& to) {
  const int base = so;
#pragma nounroll
  for (int f = F0; f < F0 + NF; ++f) {
    unsigned m0v = lds0 + (f & 7) * 16384;
    if (NF == 8) so = base + ((f - F0 + rot) & 7) * 16384;
    if (F0 + NF <= 7) {
      groups<GS>(s, r, v0, v1, v2, v3, so, m0v, m0v + 16384, w4, to);
    } else {
      const int nf = f + 1, word = F_D + (nf < 12 ? (nf & ~1) : nf) - 8;
      const unsigned want = nf < 8 || nf > 23 ? 0u : nf < 12 ? 4u : nf < 20 ? 1u : 8u;
      unsigned vg, vr;
      unsigned long long now;
      asm volatile("ds_read_b32 %0, %2\n\tds_read_b32 %1, %3" : "=&v"(vg), "=&v"(vr) : "v"(lds0 + L_FL + word * 4), "v"(lds0 + L_FL + F_REF * 4) : "memory");
      asm volatile("s_memrealtime %0" : "=s"(now) :: "memory");
      groups<GS>(s, r, v0, v1, v2, v3, so, m0v, m0v + 16384, w4, to);
      asm volatile("s_waitcnt lgkmcnt(0)" : "+v"(vg), "+v"(vr) :: "memory");
      asm volatile("" : "+s"(now) :: "memory");
      const unsigned ref = __builtin_amdgcn_readfirstlane(vr);
      const int lag = ref ? (int)((unsigned)now - ref) - (F0 < 20 ? TOFF + (f - 5) * TAU : TOFF + 15 * TAU + (f - 20) * TAU2) : 0;
      w4 = lag > TTH ? WHI - 4 : lag < -TTH ? WLO - 4 : WMID - 4;
      to = lag < -TTH ? NLO : NMID;
      if (__builtin_expect((unsigned)__builtin_amdgcn_readfirstlane(vg) < want, 0)) lds_wait<2>(L, word, want);
    }
  }
}

__global__ void __launch_bounds__(NT, 3)
fused_decode(const unsigned short* __restrict__ x, const void* __restrict__ kc, const void* __restrict__ vc, const void* __restrict__ wq,
             const void* __restrict__ wo, const void* __restrict__ w1, const void* __restrict__ b1, const void* __restrict__ w2,
             const void* __restrict__ b2, void* __restrict__ out, void* ws) {
  __shared__ __attribute__((aligned(16))) unsigned char smem[LDS_BYTES];
  LAS unsigned char* const L = (LAS unsigned char*)smem;
  const int tid = threadIdx.x, lane = tid & 63, wv = __builtin_amdgcn_readfirstlane(tid >> 6), c = blockIdx.x;
  int vq0, vq1, so;
  unsigned m0v;
  v4u rqw;
  if (wv == 0) {
    unsigned t0;
    asm volatile("s_load_dwordx2 s[36:37], %7, 0x18\n\t"
                 "s_getpc_b64 s[40:41]\n\t"
                 "s_setprio 3\n\t"
                 "s_andn2_b32 s40, s40, 63\n\t"
                 "s_lshl_b32 %4, %8, 7\n\t"
                 "s_movk_i32 s42, %9\n\t"
                 "s_mov_b32 s43, 0x20000\n\t"
                 "s_mov_b32 m0, %10\n\t"
                 "s_and_b32 s41, s41, 0xffff\n\t"
                 "v_lshlrev_b32 %0, 4, %6\n\t"
                 "s_and_b32 %2, %4, 0xfffffc00\n\t"
                 "buffer_load_dwordx4 %0, s[40:43], %2 offen lds\n\t"
                 "v_lshrrev_b32 %1, 3, %6\n\t"
                 "v_and_b32 %0, 0x70, %0\n\t"
                 "v_lshl_or_b32 %0, %1, 13, %0\n\t"
                 "v_and_b32 %1, 16, %6\n\t"
                 "v_lshlrev_b32 %1, 1, %1\n\t"
                 "v_xor_b32 %0, %0, %1\n\t"
                 "v_xor_b32 %1, 64, %0\n\t"
                 "s_lshl_b32 %2, %8, 20\n\t"
                 "s_and_b32 %2, %2, 0x600000\n\t"
                 "s_and_b32 %4, %4, 0x80\n\t"
                 "s_or_b32 %2, %2, %4\n\t"
                 "s_lshl_b32 %4, %8, 5\n\t"
                 "s_and_b32 %4, %4, 0x1f00\n\t"
                 "s_or_b32 %2, %2, %4\n\t"
                 "s_mov_b32 %3, %11\n\t"
                 "s_mov_b32 s38, %12\n\t"
                 "s_mov_b32 s39, s43\n\t"
                 "s_waitcnt lgkmcnt(0)\n\t"
                 "s_and_b32 s37, s37, 0xffff\n"
                 "1:\n\t"
                 "s_mov_b32 m0, %3\n\t"
                 "s_add_u32 %3, %3, 0x800\n\t"
                 "buffer_load_dwordx4 %0, s[36:39], %2 offen nt lds\n\t"
                 "s_add_u32 %2, %2, 0xfc00\n\t"
                 "buffer_load_dwordx4 %1, s[36:39], %2 offen offset:1024 nt lds\n\t"
                 "s_add_u32 %2, %2, 0x10400\n\t"
                 "s_cmp_lt_u32 %3, %13\n\t"
                 "s_cbranch_scc1 1b"
                 : "=&v"(vq0), "=&v"(vq1), "=&s"(so), "=&s"(m0v), "=&s"(t0), "={s[36:39]}"(rqw)
                 : "v"(tid), "s"(__builtin_amdgcn_kernarg_segment_ptr()), "s"(c), "i"(PFBYTES), "i"(L_P + 8192), "i"(0), "i"(WQB), "i"(WIN * 1024)
                 : "memory", "scc", "s40", "s41", "s42", "s43");
  }
  SB();
  const int g = lane >> 4, n16 = lane & 15;
  const int grp = c & 7, sp = c >> 3, ab = grp >> 2, ahkv = grp & 3;
  const int ksq = grp >> 1, cgq = sp * 2 + (grp & 1);
  const bool has1 = (sp & 7) != 7;
  const int ks1 = grp >> 1, cg1 = (sp - (sp >> 3)) * 2 + (grp & 1);
  const int c1 = ks1 * 56 + cg1;
  const int cgo = sp & 7;
  const int ho = grp * 4 + (sp >> 3);
  const int ks2 = (grp >> 1) * 7 + (grp & 1) * 4 + (sp >> 3);
  const bool has2 = !((grp & 1) && (sp >> 3) == 3);

  const unsigned vsig = (unsigned)(size_t)smem + (lane == 0 ? L_FL : L_SINK + lane * 4);
  if (wv == NWAVES - 1 && lane < F_WORDS) ((volatile LAS unsigned*)(L + L_FL))[lane] = 0u;
  LGKM0();
  __builtin_amdgcn_s_barrier();
  FENCE();
  if (wv >= 1 && wv < NWAVES - 1) {
    const int w = wv - 1;
    STAMP(0);
    const rsrc_t rws = mk(ws, WS_END);
    const rsrc_t rws2 = mk(ws, has2 ? WS_END : 0u);
    const rsrc_t rxl = mk(x, w < 2 ? 4096u : 0u);
    const v4u xa = __builtin_amdgcn_raw_buffer_load_b128(rxl, lane * 16, w * 2048, 0);
    const v4u xb = __builtin_amdgcn_raw_buffer_load_b128(rxl, lane * 16, w * 2048 + 1024, 0);
    FENCE();
    if (w < 2) {
      float xf[16];
#pragma unroll
      for (int i = 0; i < 4; ++i) { xf[2 * i] = blo(xa[i]); xf[2 * i + 1] = bhi(xa[i]); xf[8 + 2 * i] = blo(xb[i]); xf[9 + 2 * i] = bhi(xb[i]); }
      const float piv = asf(__builtin_amdgcn_readfirstlane(asu(xf[0])));
      float s1 = 0.f, s2 = 0.f;
#pragma unroll
      for (int i = 0; i < 16; ++i) { const float d = xf[i] - piv; s1 += d; s2 = fmaf(d, d, s2); }
      float a1 = wave_sum(s1) * (1.0f / 1024), a2 = wave_sum(s2) * (1.0f / 1024);
      float mean = rb(piv + a1), inv = rcp(rb(__builtin_amdgcn_sqrtf(rb(rb(fmaxf(a2 - a1 * a1, 0.f)) + 1e-5f))));
      v4u pa, pb;
      s1 = 0.f; s2 = 0.f;
#pragma unroll
      for (int i = 0; i < 8; ++i) {
        const unsigned t = pk(xf[2 * i] - mean, xf[2 * i + 1] - mean), u = pk(blo(t) * inv, bhi(t) * inv);
        if (i < 4) pa[i] = u; else pb[i - 4] = u;
        xf[2 * i] = blo(u); xf[2 * i + 1] = bhi(u);
        s1 += xf[2 * i] + xf[2 * i + 1]; s2 = fmaf(xf[2 * i], xf[2 * i], fmaf(xf[2 * i + 1], xf[2 * i + 1], s2));
      }
      *(LAS v4u*)(L + L_XS + w * 2048 + lane * 16) = pa;
      *(LAS v4u*)(L + L_XS + w * 2048 + 1024 + lane * 16) = pb;
      a1 = wave_sum(s1) * (1.0f / 1024); a2 = wave_sum(s2) * (1.0f / 1024);
      mean = rb(a1); inv = rcp(rb(__builtin_amdgcn_sqrtf(rb(rb(fmaxf(a2 - a1 * a1, 0.f)) + 1e-5f))));
#pragma unroll
      for (int i = 0; i < 8; ++i) {
        const unsigned t = pk(xf[2 * i] - mean, xf[2 * i + 1] - mean), u = pk(blo(t) * inv, bhi(t) * inv);
        if (i < 4) pa[i] = u; else pb[i - 4] = u;
      }
      *(LAS v4u*)(L + L_YS + w * 2048 + lane * 16) = pa;
      *(LAS v4u*)(L + L_YS + w * 2048 + 1024 + lane * 16) = pb;
      lds_signal<F_LN>(vsig);
    }
    const int q4 = n16 >> 2, p4 = lane & 3;
    lds_wait<2>(L, F_LN, 3);
    const unsigned tag = __builtin_amdgcn_readfirstlane(((volatile LAS unsigned*)(L + L_FL))[F_TAG]);
    const unsigned ta = tag & 0xffu, tb = (tag >> 8) & 0xffu;
    STAMP(1);
    {
      const bool isq = w < 4;
      const LAS unsigned char* ap = L + (isq ? L_XS + ksq * 512 : L_YS + (ks1 & 3) * 512) + (lane & 1) * 2048 + g * 16;
      v4u A[8];
#pragma unroll
      for (int s = 0; s < 8; ++s) A[s] = *(const LAS v4u*)(ap + s * 64);
      lds_wait<2>(L, F_FULL, isq ? 2 : 4);
      const LAS unsigned char* bp = L + (isq ? 0 : 32768) + (8 * g + q4) * 128 + (((w & 3) ^ ((q4 >> 1) | ((g & 1) << 1))) * 32) + p4 * 8;
      v4u Bf[8];
#pragma unroll
      for (int s = 0; s < 8; ++s) Bf[s] = trfrag(bp, s * 4096, s * 4096 + 512);
      lds_signal<F_DQ>(vsig + (isq ? 0 : (F_DH - F_DQ) * 4));
      f4 d0 = mfma32(Bf[0], A[0], f4{0.f, 0.f, 0.f, 0.f}), d1 = mfma32(Bf[1], A[1], f4{0.f, 0.f, 0.f, 0.f});
#pragma unroll
      for (int s = 2; s < 8; s += 2) { d0 = mfma32(Bf[s], A[s], d0); d1 = mfma32(Bf[s + 1], A[s + 1], d1); }
      const rsrc_t rp = mk(ws, (isq || has1) ? WS_END : 0u);
      const int pso = (isq ? WS_QP + cgq * 2048 + ksq * 128 : WS_HP + cg1 * 2048 + ks1 * 128) + (w & 3) * 512;
      stc4(rp, n16 < 2 ? g * 32 + n16 * 16 : OOB, pso, tg4(d0 + d1, ta, tb));
    }
    STAMP(2);
    v4u K[16];
    {
      const LAS unsigned char* kp = L + ((4 + w) & 7) * 16384 + n16 * 256;
      lds_wait<1>(L, F_FULL, 5 + w);
#pragma unroll
      for (int s = 0; s < 4; ++s) {
        const LAS unsigned char* kq = kp + (((4 * s + g) ^ n16) * 16);
#pragma unroll
        for (int t = 0; t < 4; ++t) K[4 * t + s] = *(const LAS v4u*)(kq + t * 4096);
      }
      lds_signal<F_DK>(vsig + w * 4);
    }
    STAMP(3);
    lds_wait<1>(L, F_Q, 1);
    {
      const LAS unsigned char* qb = L + L_QS + (n16 >> 1) * 272 + g * 16;
      v4u qf[4];
#pragma unroll
      for (int s = 0; s < 4; ++s) qf[s] = *(const LAS v4u*)(qb + s * 64);
      f4 sc[4];
#pragma unroll
      for (int t = 0; t < 4; ++t) sc[t] = mfma32(K[4 * t], qf[0], f4{0.f, 0.f, 0.f, 0.f});
#pragma unroll
      for (int s = 1; s < 4; ++s)
#pragma unroll
        for (int t = 0; t < 4; ++t) sc[t] = mfma32(K[4 * t + s], qf[s], sc[t]);
#pragma unroll
      for (int t = 0; t < 4; ++t)
#pragma unroll
        for (int r = 0; r < 4; r += 2) {
          const unsigned u = pk(sc[t][r], sc[t][r + 1]), v = pk(blo(u) * 0.088388347648318440f, bhi(u) * 0.088388347648318440f);
          sc[t][r] = blo(v); sc[t][r + 1] = bhi(v);
        }
      float mw = fmaxf(fmaxf(sc[0].x, sc[0].y), fmaxf(sc[0].z, sc[0].w));
#pragma unroll
      for (int t = 1; t < 4; ++t) mw = fmaxf(mw, fmaxf(fmaxf(sc[t].x, sc[t].y), fmaxf(sc[t].z, sc[t].w)));
      mw = x32_max(x16_max(mw)) * LOG2E;
      float lw = 0.f;
      const unsigned pm = (lane & 1) ? 0xffff0000u : 0u;
      unsigned P[8];
#pragma unroll
      for (int t = 0; t < 4; ++t)
#pragma unroll
        for (int r = 0; r < 4; r += 2) {
          const float v0 = ex2(fmaf(sc[t][r], LOG2E, -mw)), v1 = ex2(fmaf(sc[t][r + 1], LOG2E, -mw));
          lw += v0 + v1;
          const unsigned h = pk(v0, v1);
          P[2 * t + (r >> 1)] = pk(v0 - asf((h << 16) & pm), v1 - asf(h & pm));
        }
      *(LAS v4u*)(L + L_P + w * 2048 + lane * 16) = v4u{P[0], P[1], P[2], P[3]};
      *(LAS v4u*)(L + L_P + w * 2048 + 1024 + lane * 16) = v4u{P[4], P[5], P[6], P[7]};
      lw = x32_sum(x16_sum(lw));
      if (lane < 16 && !(lane & 1)) *(LAS f2*)(L + L_ST + (w * 8 + (lane >> 1)) * 8) = f2{mw, lw};
      lds_signal<F_P>(vsig);
    }
    STAMP(4);
    lds_wait<1>(L, F_P, 8);
    float o0 = 0.f, o1 = 0.f;
    {
      f4 st[8];
#pragma unroll
      for (int j = 0; j < 8; ++j) st[j] = *(const LAS f4*)(L + L_ST + (j * 8 + 2 * g) * 8);
      float M0 = st[0].x, M1 = st[0].z;
#pragma unroll
      for (int j = 1; j < 8; ++j) { M0 = fmaxf(M0, st[j].x); M1 = fmaxf(M1, st[j].z); }
      M0 = __builtin_ceilf(M0); M1 = __builtin_ceilf(M1);
#pragma unroll
      for (int j = 0; j < 8; ++j) { st[j].x = ex2(st[j].x - M0); st[j].z = ex2(st[j].z - M1); }
      STAMP(5);
      int ovs = (lane & 2) ? OOB : (2 * g + (lane & 1)) * 32768 + (n16 >> 2) * 16;
      asm volatile("" : "+v"(ovs));
      const LAS unsigned char* vp = L + (4 * g + q4) * 256 + ((w ^ (4 * (g & 1) + q4)) * 32) + p4 * 8;
#pragma unroll
      for (int j = 0; j < 8; ++j) {
        const v4u pa = *(const LAS v4u*)(L + L_P + j * 2048 + lane * 16), pb = *(const LAS v4u*)(L + L_P + j * 2048 + 1024 + lane * 16);
        if (j < 7) lds_wait<2>(L, F_FULL, 13 + j); else lds_wait<0>(L, F_FULL, 20);
        const LAS unsigned char* vq = vp + ((4 + j) & 7) * 16384;
        const v4u va = trfrag(vq, 0, 4096), vb = trfrag(vq, 8192, 12288);
        if (j == 0) lds_signal<F_DV>(vsig); else if (j == 1) lds_signal<F_DV + 1>(vsig); else if (j == 2) lds_signal<F_DV + 2>(vsig); else if (j == 3) lds_signal<F_DV + 3>(vsig);
        f4 d = mfma32(pa, va, f4{0.f, 0.f, 0.f, 0.f});
        d = mfma32(pb, vb, d);
        o0 = fmaf(st[j].x, d.x + d.y, o0);
        o1 = fmaf(st[j].z, d.z + d.w, o1);
      }
      const bool odd = (lane & 1) != 0;
      const float rcv = dpp<XOR1>(odd ? o0 : o1);
      const unsigned gr = odd ? pk(rcv, o1) : pk(o0, rcv);
      const unsigned gn = dppu<XOR2>(gr);
      stc4(rws, ovs, WS_OP + ((ahkv * 64 + w) * 32 + sp) * 128 + ab * 64, v4u{gr, tag, gn, tag});
    }
    STAMP(6);
    const LAS unsigned char* wp = L + (8 * g + q4) * 256 + ((w ^ (q4 | ((g & 1) << 2))) * 32) + p4 * 8;
    const int fvo = (g >> 1) * 2048 + (g & 1) * 32;
    {
      v4u A[4];
      lds_wait<1>(L, F_H, 1);
#pragma unroll
      for (int s = 0; s < 4; ++s) A[s] = *(const LAS v4u*)(L + L_HS + (lane & 1) * 272 + s * 64 + g * 16);
      lds_wait<2>(L, F_FULL, 22);
      f4 d = {0.f, 0.f, 0.f, 0.f};
#pragma unroll
      for (int s = 0; s < 4; ++s) d = mfma32(trfrag(wp + 4 * 16384, s * 8192, s * 8192 + 1024), A[s], d);
      stc4(rws2, n16 < 2 ? fvo + n16 * 16 : OOB, WS_MP + ((cgo * 16 + 2 * w) * 32 + ks2) * 64, tg4(d, ta, tb));
    }
    STAMP(7);
    {
      const int ovo = ((n16 >> 2) & 1) * 2048 + g * 512 + (n16 >> 3) * 64 + (lane & 3) * 16, oso = WS_OP + (ho * 8 + w) * 4096;
      v4u G[4];
      lds_wait<0>(L, F_FULL, OGATE);
#pragma unroll
      for (int j = 0; j < 4; ++j) G[j] = ldc4(rws, ovo + j * 128, oso);
      SB();
      lds_wait<0>(L, F_W, 1);
      v2u WF = *(const LAS v2u*)(L + L_WT + (lane & 3) * 32 + g * 8);
      float invl = ((const LAS float*)(L + L_WT + 128))[(lane >> 1) & 1];
      LAS unsigned char* osw = L + (n16 == g ? L_OS + g * 272 + w * 32 : L_QS + lane * 32);
      const LAS unsigned char* osr = L + L_OS + (lane & 3) * 272 + g * 16;
      const LAS unsigned char* wop = wp + 6 * 16384;
      int fv2 = (n16 & 13) == 0 ? fvo + (n16 >> 1) * 16 : OOB, fso = WS_FP + ((cgo * 16 + 2 * w) * 32 + ho) * 64;
      unsigned vta = ta, vtb = tb, vtag = tag;
      asm volatile("" : "+v"(WF), "+v"(invl), "+v"(osw), "+v"(osr), "+v"(wop), "+v"(fv2), "+s"(fso), "+v"(vta), "+v"(vtb), "+v"(vtag));
      lds_wait<0>(L, F_FULL, 24);
      v4u Bo[4];
#pragma unroll
      for (int s = 0; s < 4; ++s) Bo[s] = trfrag(wop, s * 8192, s * 8192 + 1024);
      SB();
      STAMP(8);
      const f4 z = {0.f, 0.f, 0.f, 0.f};
      f4 e0, e1, e2, e3;
#define COMBINE() do { \
        e0 = mfma16(v2u{plo(G[0].x, G[1].x), plo(G[2].x, G[3].x)}, WF, z); e1 = mfma16(v2u{phi(G[0].x, G[1].x), phi(G[2].x, G[3].x)}, WF, z); \
        e2 = mfma16(v2u{plo(G[0].z, G[1].z), plo(G[2].z, G[3].z)}, WF, z); e3 = mfma16(v2u{phi(G[0].z, G[1].z), phi(G[2].z, G[3].z)}, WF, z); } while (0)
#define OBAD(j, acc) xoror(G[j].y, vtag, xoror(G[j].w, vtag, acc))
      COMBINE();
      if (__builtin_expect(__any((OBAD(0, OBAD(1, 0u)) | OBAD(2, OBAD(3, 0u))) != 0u), 0)) {
#pragma nounroll
        for (unsigned spin = 0; spin < SPIN_CAP; ++spin) {
          const bool m0 = __any(OBAD(0, 0u) != 0u), m1 = __any(OBAD(1, 0u) != 0u), m2 = __any(OBAD(2, 0u) != 0u), m3 = __any(OBAD(3, 0u) != 0u);
          if (!(m0 || m1 || m2 || m3)) break;
          FENCE();
          if (m0) G[0] = ldc4(rws, ovo, oso);
          if (m1) G[1] = ldc4(rws, ovo + 128, oso);
          if (m2) G[2] = ldc4(rws, ovo + 256, oso);
          if (m3) G[3] = ldc4(rws, ovo + 384, oso);
        }
        COMBINE();
      }
      STAMP(9);
      *(LAS v4u*)osw = v4u{pk(e0.x, e1.x), pk(e2.x, e3.x), pk(e0.y, e1.y), pk(e2.y, e3.y)};
      *(LAS v4u*)(osw + 16) = v4u{pk(e0.z, e1.z), pk(e2.z, e3.z), pk(e0.w, e1.w), pk(e2.w, e3.w)};
      LGKM0();
      __builtin_amdgcn_s_barrier();
      FENCE();
      v4u a0 = *(const LAS v4u*)osr, a1 = *(const LAS v4u*)(osr + 64), a2 = *(const LAS v4u*)(osr + 128), a3 = *(const LAS v4u*)(osr + 192);
      SB();
      f4 d = mfma32(Bo[0], a0, z), d2 = mfma32(Bo[1], a1, z);
      d = mfma32(Bo[2], a2, d); d2 = mfma32(Bo[3], a3, d2);
      d += d2;
      d.x += dpp<XOR1>(d.x); d.y += dpp<XOR1>(d.y); d.z += dpp<XOR1>(d.z); d.w += dpp<XOR1>(d.w);
      d *= invl;
      stc4(rws, fv2, fso, v4u{(asu(d.x) & 0xffffff00u) | vta, (asu(d.y) & 0xffffff00u) | vtb, (asu(d.z) & 0xffffff00u) | vta, (asu(d.w) & 0xffffff00u) | vtb});
    }
    STAMP(10);
  } else if (wv == NWAVES - 1) {
    STAMP(0);
    volatile LAS unsigned* fl = (volatile LAS unsigned*)(L + L_FL);
    const rsrc_t rws = mk(ws, WS_END), rws1 = mk(ws, has1 ? WS_END : 0u), rws2 = mk(ws, has2 ? WS_END : 0u), rout = mk(out, 4096);
    const unsigned eraw = __builtin_amdgcn_raw_buffer_load_b32(rws, 0, WS_E, AUX_SC1);
    const unsigned b1p = __builtin_amdgcn_raw_buffer_load_b32(mk(b1, has1 ? 7168u : 0u), (n16 >> 2) * 8 + (lane & 1) * 4, c1 * 32, 0);
    const int col2 = ((lane >> 1) & 1) * 8 + ((g >> 1) | ((g & 1) << 1)) * 2;
    const int fo = (lane & 1) * 2048 + col2;
    const unsigned short xfin = __builtin_amdgcn_raw_buffer_load_b16(mk(x, c < 128 ? 4096u : 0u), fo, c * 16, 0);
    const unsigned short b2fin = __builtin_amdgcn_raw_buffer_load_b16(mk(b2, c < 128 ? 2048u : 0u), col2, c * 16, 0);
    FENCE();
    const unsigned tag = __builtin_amdgcn_readfirstlane(eraw) * 0x9E3779B1u + 0x7F4A7C15u;
    const unsigned ta = tag & 0xffu, tb = (tag >> 8) & 0xffu;
    fl[F_TAG] = tag;
    lds_signal<F_LN>(vsig);
    lds_wait<1>(L, F_FULL, 4);
    const unsigned t4 = (unsigned)__builtin_amdgcn_s_memrealtime();
    lds_wait<2>(L, F_FULL, S1GATE);
    {
      const int fvo = (lane < 16 && !(lane & 1)) ? (lane >> 2) * 16 : OOB, fb = (lane >> 1) & 1;
      bool needq = true, needh = has1;
#pragma nounroll
      for (unsigned spin = 0; (needq || needh) && spin < SPIN_CAP; ++spin) {
        const v2u GQ = ldc2(rws, needq ? lane * 8 : OOB, WS_QP + c * 512), GH = ldc2(rws1, needh ? lane * 8 : OOB, WS_HP + c1 * 512);
        if (needq && !__any((((GQ.x ^ ta) | (GQ.y ^ tb)) & 0xffu) != 0u)) {
          const unsigned gr = pk(x32_sum(x16_sum(untg(GQ.x))), x32_sum(x16_sum(untg(GQ.y)))), gn = dppu<XOR1>(gr), ty = (tag << 12) | (t4 & 0xfffu);
          stc4(rws, fvo + fb * 16384, WS_QF + c * 64, v4u{gr, ty, gn, ty});
          needq = false;
          STAMP(13);
        }
        if (needh && !__any((((GH.x ^ ta) | (GH.y ^ tb)) & 0xffu) != 0u)) {
          float h0 = rb(rb(x32_sum(x16_sum(untg(GH.x)))) + blo(b1p)), h1 = rb(rb(x32_sum(x16_sum(untg(GH.y)))) + bhi(b1p));
          constexpr float GC = -2.0f * 0.7978845608028654f * 1.4426950408889634f;
          h0 = h0 * rcp(1.0f + ex2(GC * (h0 + 0.044715f * h0 * h0 * h0)));
          h1 = h1 * rcp(1.0f + ex2(GC * (h1 + 0.044715f * h1 * h1 * h1)));
          const unsigned gr = pk(h0, h1), gn = dppu<XOR1>(gr);
          stc4(rws, fvo + fb * 14336, WS_HF + c1 * 64, v4u{gr, tag, gn, tag});
          needh = false;
          STAMP(14);
        }
        __builtin_amdgcn_s_sleep(2);
      }
    }
    lds_wait<2>(L, F_FULL, QGATE);
    lds_wait<2>(L, F_DQ, 4);
    lds_wait<2>(L, F_DH, 4);
    {
      v4u Q[4];
      const unsigned t20 = tag & 0xfffffu;
#pragma nounroll
      for (unsigned spin = 0; spin < SPIN_CAP; ++spin) {
        unsigned bad = 0;
#pragma unroll
        for (int i = 0; i < 4; ++i) Q[i] = ldc4(rws, lane * 16, WS_QF + grp * 4096 + i * 1024);
#pragma unroll
        for (int i = 0; i < 4; ++i) bad |= ((Q[i].y >> 12) ^ t20) | ((Q[i].w >> 12) ^ t20);
        if (!__any(bad != 0u)) break;
        __builtin_amdgcn_s_sleep(2);
      }
#pragma unroll
      for (int i = 0; i < 4; ++i) *(LAS v2u*)(L + L_QS + (2 * i + (lane >> 5)) * 272 + (lane & 31) * 8) = v2u{Q[i].x, Q[i].z};
      lds_signal<F_Q>(vsig);
      STAMP(15);
      int d = 0;
#pragma unroll
      for (int i = 0; i < 4; ++i) d += (int)((Q[i].y - t4) << 20) >> 20;
      fl[F_REF] = (t4 + (int)(wave_sum((float)d) * (1.0f / 256))) | 1u;
    }
    lds_wait<1>(L, F_P, 8);
    {
      f2 sv[8];
#pragma unroll
      for (int j = 0; j < 8; ++j) sv[j] = *(const LAS f2*)(L + L_ST + (j * 8 + (lane & 7)) * 8);
      float M = sv[0].x, Ls = 0.f;
#pragma unroll
      for (int j = 1; j < 8; ++j) M = fmaxf(M, sv[j].x);
      M = __builtin_ceilf(M);
#pragma unroll
      for (int j = 0; j < 8; ++j) Ls = fmaf(ex2(sv[j].x - M), sv[j].y, Ls);
      stc4(rws, lane < 8 ? (lane >> 2) * 2048 + (lane & 3) * 16 : OOB, WS_ST + grp * 4096 + sp * 64, v4u{asu(M), tag, asu(Ls), tag});
      const unsigned tag2 = has2 ? tag : 0u;
      v4u H;
#pragma nounroll
      for (unsigned spin = 0; spin < SPIN_CAP; ++spin) {
        H = ldc4(rws2, (lane >> 5) * 14336 + (lane & 31) * 16, WS_HF + ks2 * 512);
        if (!__any(((H.y ^ tag2) | (H.w ^ tag2)) != 0u)) break;
        __builtin_amdgcn_s_sleep(2);
      }
      *(LAS v2u*)(L + L_HS + (lane >> 5) * 272 + (lane & 31) * 8) = v2u{H.x, H.z};
      lds_signal<F_H>(vsig);
      STAMP(16);
    }
    lds_wait<2>(L, F_FULL, SGATE);
    {
      v4u SG;
#pragma nounroll
      for (unsigned spin = 0; spin < SPIN_CAP; ++spin) {
        SG = ldc4(rws, (lane >> 5) * 16384 + (lane & 31) * 64, WS_ST + (ho >> 2) * 2048 + (ho & 3) * 16);
        if (!__any(((SG.y ^ tag) | (SG.w ^ tag)) != 0u)) break;
        __builtin_amdgcn_s_sleep(2);
      }
      const float m = asf(SG.x), M = x16_max(row_max(m));
      const float e = rb(ex2(m - M)), Ls = x16_sum(row_sum(e * asf(SG.z)));
      ((LAS unsigned short*)(L + L_WT))[lane] = (unsigned short)(asu(e) >> 16);
      ((LAS float*)(L + L_WT + 128))[lane >> 5] = rcp(Ls) * (1.0f + 1.0f / 131072);
      lds_signal<F_W>(vsig);
      STAMP(17);
    }
    {
      const rsrc_t rwf = mk(ws, c < 128 ? WS_END : 0u);
      const unsigned mlive = lane < 48 ? 0xffffffffu : 0u;
      __builtin_amdgcn_s_barrier();
      FENCE();
      if (c < 128) {
        int so = WS_MP + c * 2048, ov = OOB, ev = OOB, pass = 0;
        unsigned live = mlive;
        float gt = 0.f;
        const float xv = blo(xfin), bv = blo(b2fin);
        asm volatile("" : "+s"(pass));
#pragma nounroll
        for (;;) {
          v4u A0 = ldc4(rwf, lane * 16, so), A1 = ldc4(rwf, lane * 16, so + 1024);
#pragma nounroll
          for (unsigned spin = 0; spin < SPIN_CAP; ++spin) {
            const bool m0 = __any(bad4(A0, ta, tb) != 0u), m1 = __any((bad4(A1, ta, tb) & live) != 0u);
            if (!(m0 || m1)) break;
            FENCE();
            if (m0) A0 = ldc4(rwf, lane * 16, so);
            if (m1) A1 = ldc4(rwf, lane * 16, so + 1024);
          }
          if (pass) { STAMP(25); }
          const float r = rb(red4(A0, A1 & live));
          const float o = xv + rb(r * gt);
          __builtin_amdgcn_raw_buffer_store_b16((unsigned short)pk(o, o), rout, ov, c * 16, AUX_SC1);
          stc4(rws, ev, WS_E, v4u{tag, tag, tag, tag});
          SB();
          if (pass) break;
          const float m = rb(r + bv);
          gt = rb(m * rcp(1.0f + ex2(-1.4426950408889634f * m)));
          pass = 1; so = WS_FP + c * 2048; live = 0xffffffffu;
          ov = (lane & 12) == 0 ? fo : OOB; ev = (c == 0 && lane < 4) ? lane * 16 : OOB;
          asm volatile("" : "+s"(pass), "+s"(so), "+v"(live), "+v"(ov), "+v"(ev), "+v"(gt));
        }
        STAMP(26);
      }
    }
  } else {
    STAMP(0);
    asm volatile(".fill %0, 4, 0xbf800000" :: "i"(SHIFT));
    const rsrc_t rq = mk(wq, WQB);
    const int r8 = lane >> 3, c8 = lane & 7, f0 = r8 & 2;
    volatile LAS unsigned* fl = (volatile LAS unsigned*)(L + L_FL);
    const unsigned lds0 = (unsigned)(size_t)smem;
    Ld st = {WIN - 16, 0, lds0 + L_FL + F_FULL * 4};
    unsigned w4 = WMID - 4, to = 0x7fffffffu;
    groups<8 * 8192>(st, rq, vq0, vq1, vq0, vq1, so, m0v, lds0 + 32768, w4, to);
    to = NMID;
    {
      const int v0 = r8 * 7168 + (c8 ^ f0) * 16;
      stream<8 * 7168, 2, 2>(L, st, mk(w1, has1 ? W1B : 0u), v0, v0 ^ 64, v0, v0 ^ 64, ks1 * 256 * 7168 + cg1 * 128, 0, lds0, w4, to);
    }
    const int kvb = (grp * 16384 + sp * 512) * 256;
    {
      const int v0 = g * 256 + (n16 ^ g) * 16;
      stream<1024, 4, 8>(L, st, mk(kc, KVB), v0, v0 ^ 64, v0 ^ 128, v0 ^ 192, kvb, sp, lds0, w4, to);
    }
    {
      const int v0 = g * 256 + (n16 ^ (2 * g)) * 16;
      stream<1024, 12, 8>(L, st, mk(vc, KVB), v0, v0 ^ 128, v0, v0 ^ 128, kvb, sp, lds0, w4, to);
    }
    {
      const int v0 = g * 2048 + (n16 ^ (2 * g)) * 16;
      stream<8192, 20, 2>(L, st, mk(w2, has2 ? W1B : 0u), v0, v0, v0 ^ 128, v0 ^ 128, ks2 * 128 * 2048 + cgo * 256, 0, lds0, w4, to);
      stream<8192, 22, 2>(L, st, mk(wo, WQB), v0, v0, v0 ^ 128, v0 ^ 128, ho * 128 * 2048 + cgo * 256, 0, lds0, w4, to);
    }
    asm volatile("s_waitcnt vmcnt(32)" ::: "memory");
    if (st.pub < 22) fl[F_FULL] = 22;
    asm volatile("s_waitcnt vmcnt(16)" ::: "memory");
    fl[F_FULL] = 23;
    asm volatile("s_waitcnt vmcnt(0)" ::: "memory");
    fl[F_FULL] = 24;
  }
}

}

extern "C" void kernel_launch(void* const* d_in, const int* in_sizes, int n_in, void* d_out, int out_size, void* d_ws, size_t ws_size,
                              hipStream_t stream) {
  (void)in_sizes; (void)n_in; (void)out_size; (void)ws_size;
  fused_decode<<<256, NT, 0, stream>>>((const unsigned short*)d_in[0], d_in[1], d_in[2], d_in[3], d_in[4], d_in[6], d_in[7], d_in[8], d_in[9],
                                       d_out, d_ws);
}
```

```cpp
#include <hip/hip_runtime.h>
#include <hip/hip_bf16.h>


namespace {

typedef unsigned v4u __attribute__((ext_vector_type(4)));
typedef unsigned v2u __attribute__((ext_vector_type(2)));
typedef float f4 __attribute__((ext_vector_type(4)));
typedef float f2 __attribute__((ext_vector_type(2)));
typedef __bf16 bf8 __attribute__((ext_vector_type(8)));
typedef __bf16 bf2 __attribute__((ext_vector_type(2)));
typedef short s4 __attribute__((ext_vector_type(4)));
typedef __amdgpu_buffer_rsrc_t rsrc_t;
#define LAS __attribute__((address_space(3)))

constexpr int NWAVES = 10;
constexpr int NT = 64 * NWAVES;
constexpr unsigned KVB = 2u * 4 * 16384 * 128 * 2, WQB = 1024u * 4096 * 2, W1B = 1024u * 3584 * 2;
constexpr unsigned BLK = 65536, GAP = 32768;
constexpr unsigned WS_OP = 0;
constexpr unsigned WS_FP = GAP;
constexpr unsigned WS_MP = 8 * BLK + GAP;
constexpr unsigned WS_QP = 16 * BLK + GAP;
constexpr unsigned WS_HP = 20 * BLK + GAP;
constexpr unsigned WS_QF = 24 * BLK + GAP;
constexpr unsigned WS_HF = 25 * BLK + GAP;
constexpr unsigned WS_ST = 26 * BLK + GAP;
constexpr unsigned WS_E = 27 * BLK + GAP;
constexpr unsigned WS_END = 32 * BLK;
constexpr int L_P = 131072, L_XS = L_P, L_YS = L_P + 4096, L_QS = L_P + 16384, L_HS = L_QS + 8 * 272, L_OS = L_HS + 2 * 272,
              L_ST = L_OS + 4 * 272, L_WT = L_ST + 512, L_FL = L_WT + 256, L_SINK = L_FL + 128, L_END = L_SINK + 512;
constexpr int LDS_BYTES = L_END;
static_assert(L_END <= 163840 && (L_HS % 16) == 0 && (L_OS % 16) == 0 && (L_ST % 16) == 0 && (L_WT % 16) == 0 && (L_FL % 16) == 0, "lds map");
enum { F_FULL = 0, F_LN = 1, F_Q = 2, F_P = 3, F_H = 4, F_REF = 5, F_TAG = 6, F_W = 7, F_D = 8, F_DQ = F_D, F_DH = F_D + 2, F_DK = F_D + 4, F_DV = F_D + 12, F_OG = 25, F_WORDS = 32 };
constexpr int OOB = 0x7ff00000;
constexpr int AUX_SC1 = 16;
constexpr unsigned SPIN_CAP = 1u << 15, LSPIN_CAP = 1u << 20;
constexpr float LOG2E = 1.4426950408889634f;
constexpr int WIN = 24, WLO = 17, WMID = 25, WHI = 33, NMID = 4, NLO = 8, TAU = 56, TAU2 = 46, TOFF = 15, TTH = 20;
constexpr unsigned S1GATE = 5, QGATE = 9, SGATE = 19;
constexpr int PFBYTES = 14336;
static_assert(WIN == 24 && WHI <= 60, "first burst in whole groups inside the wq tile; vmcnt has 6 bits");

__device__ __forceinline__ rsrc_t mk(const void* p, unsigned bytes) { return __builtin_amdgcn_make_buffer_rsrc((void*)p, 0, bytes, 0x00020000); }
__device__ __forceinline__ v4u ldc4(rsrc_t r, int vo, int so) { return __builtin_amdgcn_raw_buffer_load_b128(r, vo, so, AUX_SC1); }
__device__ __forceinline__ v2u ldc2(rsrc_t r, int vo, int so) { return __builtin_amdgcn_raw_buffer_load_b64(r, vo, so, AUX_SC1); }
__device__ __forceinline__ void stc4(rsrc_t r, int vo, int so, v4u v) { __builtin_amdgcn_raw_buffer_store_b128(v, r, vo, so, AUX_SC1); }
__device__ __forceinline__ float asf(unsigned u) { return __builtin_bit_cast(float, u); }
__device__ __forceinline__ unsigned asu(float f) { return __builtin_bit_cast(unsigned, f); }
__device__ __forceinline__ unsigned pk(float a, float b) { f2 v = {a, b}; return __builtin_bit_cast(unsigned, __builtin_convertvector(v, bf2)); }
__device__ __forceinline__ float blo(unsigned u) { return asf(u << 16); }
__device__ __forceinline__ float bhi(unsigned u) { return asf(u & 0xffff0000u); }
__device__ __forceinline__ float rb(float a) { return blo(pk(a, a)); }
__device__ __forceinline__ unsigned plo(unsigned a, unsigned b) { return __builtin_amdgcn_perm(b, a, 0x05040100u); }
__device__ __forceinline__ unsigned phi(unsigned a, unsigned b) { return __builtin_amdgcn_perm(b, a, 0x07060302u); }
__device__ __forceinline__ f4 mfma32(v4u a, v4u b, f4 c) { return __builtin_amdgcn_mfma_f32_16x16x32_bf16(__builtin_bit_cast(bf8, a), __builtin_bit_cast(bf8, b), c, 0, 0, 0); }
__device__ __forceinline__ f4 mfma16(v2u a, v2u b, f4 c) { return __builtin_amdgcn_mfma_f32_16x16x16bf16_1k(__builtin_bit_cast(s4, a), __builtin_bit_cast(s4, b), c, 0, 0, 0); }
template <int C> __device__ __forceinline__ float dpp(float x) { return asf(__builtin_amdgcn_mov_dpp(asu(x), C, 0xf, 0xf, true)); }
template <int C> __device__ __forceinline__ unsigned dppu(unsigned x) { return __builtin_amdgcn_mov_dpp(x, C, 0xf, 0xf, true); }
constexpr int XOR1 = 0xB1, XOR2 = 0x4E, ROR4 = 0x124, ROR8 = 0x128;
__device__ __forceinline__ float x16_sum(float x) { auto s = __builtin_amdgcn_permlane16_swap(asu(x), asu(x), false, false); return asf(s[0]) + asf(s[1]); }
__device__ __forceinline__ float x32_sum(float x) { auto s = __builtin_amdgcn_permlane32_swap(asu(x), asu(x), false, false); return asf(s[0]) + asf(s[1]); }
__device__ __forceinline__ float x16_max(float x) { auto s = __builtin_amdgcn_permlane16_swap(asu(x), asu(x), false, false); return fmaxf(asf(s[0]), asf(s[1])); }
__device__ __forceinline__ float x32_max(float x) { auto s = __builtin_amdgcn_permlane32_swap(asu(x), asu(x), false, false); return fmaxf(asf(s[0]), asf(s[1])); }
__device__ __forceinline__ float row_sum(float x) { x += dpp<XOR1>(x); x += dpp<XOR2>(x); x += dpp<ROR4>(x); x += dpp<ROR8>(x); return x; }
__device__ __forceinline__ float row_max(float x) { x = fmaxf(x, dpp<XOR1>(x)); x = fmaxf(x, dpp<XOR2>(x)); x = fmaxf(x, dpp<ROR4>(x)); x = fmaxf(x, dpp<ROR8>(x)); return x; }
__device__ __forceinline__ float wave_sum(float x) { return x32_sum(x16_sum(row_sum(x))); }
__device__ __forceinline__ float ex2(float x) { return __builtin_amdgcn_exp2f(x); }
__device__ __forceinline__ float rcp(float x) { return __builtin_amdgcn_rcpf(x); }
__device__ __forceinline__ unsigned tg(float v, unsigned t8) { return ((asu(v) + 0x80u) & 0xffffff00u) | t8; }
__device__ __forceinline__ v4u tg4(f4 v, unsigned ta, unsigned tb) { return v4u{tg(v.x, ta), tg(v.y, tb), tg(v.z, ta), tg(v.w, tb)}; }
__device__ __forceinline__ float untg(unsigned u) { return asf(u & 0xffffff00u); }
__device__ __forceinline__ unsigned xoror(unsigned a, unsigned b, unsigned c) { return __builtin_amdgcn_bitop3_b32(a, b, c, 0xBE); }
__device__ __forceinline__ unsigned bad4(v4u u, unsigned ta, unsigned tb) { return xoror(u.x, ta, xoror(u.y, tb, xoror(u.z, ta, u.w ^ tb))) & 0xffu; }
__device__ __forceinline__ float red4(v4u a, v4u b) {
  const auto s01 = __builtin_amdgcn_permlane32_swap(asu(untg(a.x) + untg(b.x)), asu(untg(a.y) + untg(b.y)), false, false);
  const auto s23 = __builtin_amdgcn_permlane32_swap(asu(untg(a.z) + untg(b.z)), asu(untg(a.w) + untg(b.w)), false, false);
  const auto t = __builtin_amdgcn_permlane16_swap(asu(asf(s01[0]) + asf(s01[1])), asu(asf(s23[0]) + asf(s23[1])), false, false);
  float v = asf(t[0]) + asf(t[1]);
  v += dpp<ROR4>(v); v += dpp<ROR8>(v);
  return v;
}
__device__ __forceinline__ v4u trfrag(const LAS unsigned char* a, int off0, int off1) {
  const v2u lo = __builtin_bit_cast(v2u, __builtin_amdgcn_ds_read_tr16_b64_v4i16((LAS s4*)(a + off0)));
  const v2u hi = __builtin_bit_cast(v2u, __builtin_amdgcn_ds_read_tr16_b64_v4i16((LAS s4*)(a + off1)));
  return v4u{lo.x, lo.y, hi.x, hi.y};
}

#define SB() __builtin_amdgcn_sched_barrier(0)
#define FENCE() do { SB(); asm volatile("" ::: "memory"); SB(); } while (0)
#define LGKM0() do { SB(); asm volatile("s_waitcnt lgkmcnt(0)" ::: "memory"); SB(); } while (0)

template <int SLEEP> __device__ __forceinline__ void lds_wait(LAS unsigned char* L, int word, unsigned want) {
  FENCE();
  const volatile LAS unsigned* p = (const volatile LAS unsigned*)(L + L_FL) + word;
#pragma nounroll
  for (unsigned i = 0; i < LSPIN_CAP; ++i) {
    if ((unsigned)__builtin_amdgcn_readfirstlane(*p) >= want) break;
    if (SLEEP) __builtin_amdgcn_s_sleep(SLEEP);
  }
  FENCE();
}
template <int WORD> __device__ __forceinline__ void lds_signal(unsigned vsig) {
  SB();
  asm volatile("s_waitcnt lgkmcnt(0)\n\tds_add_u32 %0, %1 offset:%2" :: "v"(vsig), "v"(1u), "i"(WORD * 4) : "memory");
  SB();
}
struct Ld { int gap; unsigned pub, vfull; };
template <int GS>
__device__ __forceinline__ void groups(Ld& s, rsrc_t r, int v0, int v1, int v2, int v3, int& so, unsigned& m0v, unsigned m0end, unsigned w4, unsigned to) {
  unsigned a, b, v, n;
  asm volatile("s_mov_b32 %3, 0\n"
               "0:\n\t"
               "s_getreg_b32 %0, hwreg(HW_REG_IB_STS)\n\t"
               "s_add_u32 %3, %3, 1\n\t"
               "s_and_b32 %1, %0, 15\n\t"
               "s_bfe_u32 %0, %0, 0x20016\n\t"
               "s_lshl4_add_u32 %0, %0, %1\n\t"
               "s_cmp_le_i32 %0, %4\n\t"
               "s_cbranch_scc1 2f\n"
               "1:\n\t"
               "s_cmp_ge_u32 %3, %16\n\t"
               "s_cselect_b32 %1, %17, %15\n\t"
               "s_cmp_le_u32 %0, %1\n\t"
               "s_cbranch_scc0 0b\n\t"
               "s_mov_b32 m0, %7\n\t"
               "s_add_u32 %7, %7, 0x1000\n\t"
               "buffer_load_dwordx4 %9, %13, %6 offen nt lds\n\ts_add_u32 %6, %6, %18\n\ts_mov_b32 %3, 0\n\t"
               "buffer_load_dwordx4 %10, %13, %6 offen offset:1024 nt lds\n\ts_add_u32 %6, %6, %18\n\ts_add_u32 %4, %4, 4\n\t"
               "buffer_load_dwordx4 %11, %13, %6 offen offset:2048 nt lds\n\ts_add_u32 %6, %6, %18\n\t"
               "buffer_load_dwordx4 %12, %13, %6 offen offset:3072 nt lds\n\ts_add_u32 %6, %6, %19\n\t"
               "s_cmp_lt_u32 %7, %14\n\t"
               "s_cbranch_scc1 0b\n\t"
               "s_branch 3f\n"
               "2:\n\t"
               "s_add_u32 %5, %5, 1\n\t"
               "v_mov_b32 %2, %5\n\t"
               "ds_write_b32 %8, %2\n\t"
               "s_sub_u32 %4, %4, 16\n\t"
               "s_branch 1b\n"
               "3:"
               : "=&s"(a), "=&s"(b), "=&v"(v), "=&s"(n), "+s"(s.gap), "+s"(s.pub), "+s"(so), "+s"(m0v)
               : "v"(s.vfull), "v"(v0), "v"(v1), "v"(v2), "v"(v3), "s"(r), "s"(m0end), "s"(w4), "s"(to), "i"(WHI - 4), "i"(GS - 1024), "i"(GS + 3072)
               : "memory", "scc");
}
template <int GS, int F0, int NF>
__device__ __forceinline__ void stream(LAS unsigned char* L, Ld& s, rsrc_t r, int v0, int v1, int v2, int v3, int so, int rot, unsigned lds0, unsigned& w4, unsigned& to) {
  const int base = so;
#pragma nounroll
  for (int f = F0; f < F0 + NF; ++f) {
    unsigned m0v = lds0 + (f & 7) * 16384;
    if (NF == 8) so = base + ((f - F0 + rot) & 7) * 524288;
    if (F0 + NF <= 7) {
      groups<GS>(s, r, v0, v1, v2, v3, so, m0v, m0v + 16384, w4, to);
    } else {
      const int nf = f + 1, word = F_D + (nf < 12 ? (nf & ~1) : nf) - 8;
      const unsigned want = nf < 8 || nf > 23 ? 0u : nf < 12 ? 4u : nf < 20 ? 1u : 8u;
      unsigned vg, vr;
      unsigned long long now;
      asm volatile("ds_read_b32 %0, %2\n\tds_read_b32 %1, %3" : "=&v"(vg), "=&v"(vr) : "v"(lds0 + L_FL + word * 4), "v"(lds0 + L_FL + F_REF * 4) : "memory");
      asm volatile("s_memrealtime %0" : "=s"(now) :: "memory");
      groups<GS>(s, r, v0, v1, v2, v3, so, m0v, m0v + 16384, w4, to);
      asm volatile("s_waitcnt lgkmcnt(0)" : "+v"(vg), "+v"(vr) :: "memory");
      asm volatile("" : "+s"(now) :: "memory");
      const unsigned ref = __builtin_amdgcn_readfirstlane(vr);
      const int lag = ref ? (int)((unsigned)now - ref) - (F0 < 20 ? TOFF + (f - 5) * TAU : TOFF + 15 * TAU + (f - 20) * TAU2) : 0;
      w4 = lag > TTH ? WHI - 4 : lag < -TTH ? WLO - 4 : WMID - 4;
      to = lag < -TTH ? NLO : NMID;
      if (__builtin_expect((unsigned)__builtin_amdgcn_readfirstlane(vg) < want, 0)) lds_wait<2>(L, word, want);
    }
  }
}

__global__ void __launch_bounds__(NT, 3)
fused_decode(const unsigned short* __restrict__ x, const void* __restrict__ wq, void* ws, const void* __restrict__ w1, const void* __restrict__ kc,
             const void* __restrict__ vc, const void* __restrict__ w2, const void* __restrict__ wo, const void* __restrict__ b1,
             const void* __restrict__ b2, void* __restrict__ out) {
  __shared__ __attribute__((aligned(16))) unsigned char smem[LDS_BYTES];
  LAS unsigned char* const L = (LAS unsigned char*)smem;
  const int tid = threadIdx.x, lane = tid & 63, wv = __builtin_amdgcn_readfirstlane(tid >> 6), c = blockIdx.x;
  int vq0, vq1, so;
  unsigned m0v;
  v4u rqw;
  if (wv == 0) {
    unsigned t0;
    asm volatile("s_load_dwordx2 s[36:37], %7, 0x8\n\t"
                 "s_getpc_b64 s[40:41]\n\t"
                 "s_setprio 3\n\t"
                 "s_andn2_b32 s40, s40, 63\n\t"
                 "s_lshl_b32 %4, %8, 7\n\t"
                 "s_movk_i32 s42, %9\n\t"
                 "s_mov_b32 s43, 0x20000\n\t"
                 "s_mov_b32 m0, %10\n\t"
                 "s_and_b32 s41, s41, 0xffff\n\t"
                 "v_lshlrev_b32 %0, 4, %6\n\t"
                 "s_and_b32 %2, %4, 0xfffffc00\n\t"
                 "buffer_load_dwordx4 %0, s[40:43], %2 offen lds\n\t"
                 "v_lshrrev_b32 %1, 3, %6\n\t"
                 "v_and_b32 %0, 0x70, %0\n\t"
                 "v_lshl_or_b32 %0, %1, 13, %0\n\t"
                 "v_and_b32 %1, 16, %6\n\t"
                 "v_lshlrev_b32 %1, 1, %1\n\t"
                 "v_xor_b32 %0, %0, %1\n\t"
                 "v_xor_b32 %1, 64, %0\n\t"
                 "s_lshl_b32 %2, %8, 20\n\t"
                 "s_and_b32 %2, %2, 0x600000\n\t"
                 "s_and_b32 %4, %4, 0x80\n\t"
                 "s_or_b32 %2, %2, %4\n\t"
                 "s_lshl_b32 %4, %8, 5\n\t"
                 "s_and_b32 %4, %4, 0x1f00\n\t"
                 "s_or_b32 %2, %2, %4\n\t"
                 "s_bfe_u32 %3, %8, 0x20003\n\t"
                 "s_lshl_b32 %4, %3, 19\n\t"
                 "s_lshl_b32 %3, %3, 13\n\t"
                 "s_and_b32 vcc_lo, %2, 0x600000\n\t"
                 "s_or_b32 %2, %2, %4\n\t"
                 "s_mov_b32 %4, %13\n\t"
                 "s_mov_b32 s38, %12\n\t"
                 "s_mov_b32 s39, s43\n\t"
                 "s_waitcnt lgkmcnt(0)\n\t"
                 "s_and_b32 s37, s37, 0xffff\n"
                 "1:\n\t"
                 "s_mov_b32 m0, %3\n\t"
                 "s_add_u32 %3, %3, 0x800\n\t"
                 "buffer_load_dwordx4 %0, s[36:39], %2 offen nt lds\n\t"
                 "s_add_u32 %2, %2, 0xfc00\n\t"
                 "s_bitset0_b32 %3, 15\n\t"
                 "buffer_load_dwordx4 %1, s[36:39], %2 offen offset:1024 nt lds\n\t"
                 "s_add_u32 %2, %2, 0x10400\n\t"
                 "s_and_b32 %2, %2, 0x1fffff\n\t"
                 "s_or_b32 %2, %2, vcc_lo\n\t"
                 "s_sub_u32 %4, %4, 0x800\n\t"
                 "s_cmp_lg_u32 %4, 0\n\t"
                 "s_cbranch_scc1 1b"
                 : "=&v"(vq0), "=&v"(vq1), "=&s"(so), "=&s"(m0v), "=&s"(t0), "={s[36:39]}"(rqw)
                 : "v"(tid), "s"(__builtin_amdgcn_kernarg_segment_ptr()), "s"(c), "i"(PFBYTES), "i"(L_P + 8192), "i"(0), "i"(WQB), "i"(WIN * 1024)
                 : "memory", "scc", "vcc", "s40", "s41", "s42", "s43");
  }
  SB();
  v4u xa = {0u, 0u, 0u, 0u}, xb = xa;
  unsigned eraw = 0u;
  if (wv != 0) {
    unsigned long long xl = (unsigned long long)x;
    asm volatile("" : "+s"(xl));
    const rsrc_t rxl = mk((const void*)xl, wv < 3 ? 4096u : 0u);
    xa = __builtin_amdgcn_raw_buffer_load_b128(rxl, lane * 16, (wv - 1) * 2048, 0);
    xb = __builtin_amdgcn_raw_buffer_load_b128(rxl, lane * 16, (wv - 1) * 2048 + 1024, 0);
    eraw = __builtin_amdgcn_raw_buffer_load_b32(mk(ws, wv == NWAVES - 1 ? WS_END : 0u), 0, WS_E, AUX_SC1);
  }
  FENCE();
  const int g = lane >> 4, n16 = lane & 15;
  const int grp = c & 7, sp = c >> 3, ab = grp >> 2, ahkv = grp & 3;
  const int ksq = grp >> 1, cgq = sp * 2 + (grp & 1);
  const bool has1 = (sp & 7) != 7;
  const int ks1 = grp >> 1, cg1 = (sp - (sp >> 3)) * 2 + (grp & 1);
  const int c1 = ks1 * 56 + cg1;
  const int cgo = sp & 7;
  const int ho = grp * 4 + (sp >> 3);
  const int ks2 = (grp >> 1) * 7 + (grp & 1) * 4 + (sp >> 3);
  const bool has2 = !((grp & 1) && (sp >> 3) == 3);

  const unsigned vsig = (unsigned)(size_t)smem + (lane == 0 ? L_FL : L_SINK + lane * 4);
  if (wv == NWAVES - 1 && lane < F_WORDS) ((volatile LAS unsigned*)(L + L_FL))[lane] = 0u;
  LGKM0();
  __builtin_amdgcn_s_barrier();
  FENCE();
  if (wv >= 1 && wv < NWAVES - 1) {
    const int w = wv - 1;
    const rsrc_t rws = mk(ws, WS_END);
    const rsrc_t rws2 = mk(ws, has2 ? WS_END : 0u);
    if (w < 2) {
      float xf[16];
#pragma unroll
      for (int i = 0; i < 4; ++i) { xf[2 * i] = blo(xa[i]); xf[2 * i + 1] = bhi(xa[i]); xf[8 + 2 * i] = blo(xb[i]); xf[9 + 2 * i] = bhi(xb[i]); }
      const float piv = asf(__builtin_amdgcn_readfirstlane(asu(xf[0])));
      float s1 = 0.f, s2 = 0.f;
#pragma unroll
      for (int i = 0; i < 16; ++i) { const float d = xf[i] - piv; s1 += d; s2 = fmaf(d, d, s2); }
      float a1 = wave_sum(s1) * (1.0f / 1024), a2 = wave_sum(s2) * (1.0f / 1024);
      float mean = rb(piv + a1), inv = rcp(rb(__builtin_amdgcn_sqrtf(rb(rb(fmaxf(a2 - a1 * a1, 0.f)) + 1e-5f))));
      v4u pa, pb;
      s1 = 0.f; s2 = 0.f;
#pragma unroll
      for (int i = 0; i < 8; ++i) {
        const unsigned t = pk(xf[2 * i] - mean, xf[2 * i + 1] - mean), u = pk(blo(t) * inv, bhi(t) * inv);
        if (i < 4) pa[i] = u; else pb[i - 4] = u;
        xf[2 * i] = blo(u); xf[2 * i + 1] = bhi(u);
        s1 += xf[2 * i] + xf[2 * i + 1]; s2 = fmaf(xf[2 * i], xf[2 * i], fmaf(xf[2 * i + 1], xf[2 * i + 1], s2));
      }
      *(LAS v4u*)(L + L_XS + w * 2048 + lane * 16) = pa;
      *(LAS v4u*)(L + L_XS + w * 2048 + 1024 + lane * 16) = pb;
      a1 = wave_sum(s1) * (1.0f / 1024); a2 = wave_sum(s2) * (1.0f / 1024);
      mean = rb(a1); inv = rcp(rb(__builtin_amdgcn_sqrtf(rb(rb(fmaxf(a2 - a1 * a1, 0.f)) + 1e-5f))));
#pragma unroll
      for (int i = 0; i < 8; ++i) {
        const unsigned t = pk(xf[2 * i] - mean, xf[2 * i + 1] - mean), u = pk(blo(t) * inv, bhi(t) * inv);
        if (i < 4) pa[i] = u; else pb[i - 4] = u;
      }
      *(LAS v4u*)(L + L_YS + w * 2048 + lane * 16) = pa;
      *(LAS v4u*)(L + L_YS + w * 2048 + 1024 + lane * 16) = pb;
      lds_signal<F_LN>(vsig);
    }
    const int q4 = n16 >> 2, p4 = lane & 3;
    lds_wait<2>(L, F_LN, 3);
    const unsigned tag = __builtin_amdgcn_readfirstlane(((volatile LAS unsigned*)(L + L_FL))[F_TAG]);
    const unsigned ta = tag & 0xffu, tb = (tag >> 8) & 0xffu;
    {
      const bool isq = w < 4;
      const LAS unsigned char* ap = L + (isq ? L_XS + ksq * 512 : L_YS + (ks1 & 3) * 512) + (lane & 1) * 2048 + g * 16;
      v4u A[8];
#pragma unroll
      for (int s = 0; s < 8; ++s) A[s] = *(const LAS v4u*)(ap + s * 64);
      lds_wait<2>(L, F_FULL, isq ? 2 : 4);
      const LAS unsigned char* bp = L + (isq ? 0 : 32768) + (8 * g + q4) * 128 + (((w & 3) ^ ((q4 >> 1) | ((g & 1) << 1))) * 32) + p4 * 8;
      v4u Bf[8];
#pragma unroll
      for (int s = 0; s < 8; ++s) Bf[s] = trfrag(bp, s * 4096, s * 4096 + 512);
      lds_signal<F_DQ>(vsig + (isq ? 0 : (F_DH - F_DQ) * 4));
      f4 d0 = mfma32(Bf[0], A[0], f4{0.f, 0.f, 0.f, 0.f}), d1 = mfma32(Bf[1], A[1], f4{0.f, 0.f, 0.f, 0.f});
#pragma unroll
      for (int s = 2; s < 8; s += 2) { d0 = mfma32(Bf[s], A[s], d0); d1 = mfma32(Bf[s + 1], A[s + 1], d1); }
      const rsrc_t rp = mk(ws, (isq || has1) ? WS_END : 0u);
      const int cg = isq ? cgq : cg1;
      const int pso = (isq ? WS_QP + ksq * 128 : WS_HP + ks1 * 128) + (cg >> 4) * BLK + (cg & 15) * 2048 + (w & 3) * 512;
      stc4(rp, n16 < 2 ? g * 32 + n16 * 16 : OOB, pso, tg4(d0 + d1, ta, tb));
    }
    v4u K[16];
    {
      const LAS unsigned char* kp = L + ((4 + w) & 7) * 16384 + n16 * 256;
      lds_wait<1>(L, F_FULL, 5 + w);
#pragma unroll
      for (int s = 0; s < 4; ++s) {
        const LAS unsigned char* kq = kp + (((4 * s + g) ^ n16) * 16);
#pragma unroll
        for (int t = 0; t < 4; ++t) K[4 * t + s] = *(const LAS v4u*)(kq + t * 4096);
      }
      lds_signal<F_DK>(vsig + w * 4);
    }
    lds_wait<1>(L, F_Q, 1);
    {
      const LAS unsigned char* qb = L + L_QS + (n16 >> 1) * 272 + g * 16;
      v4u qf[4];
#pragma unroll
      for (int s = 0; s < 4; ++s) qf[s] = *(const LAS v4u*)(qb + s * 64);
      f4 sc[4];
#pragma unroll
      for (int t = 0; t < 4; ++t) sc[t] = mfma32(K[4 * t], qf[0], f4{0.f, 0.f, 0.f, 0.f});
#pragma unroll
      for (int s = 1; s < 4; ++s)
#pragma unroll
        for (int t = 0; t < 4; ++t) sc[t] = mfma32(K[4 * t + s], qf[s], sc[t]);
#pragma unroll
      for (int t = 0; t < 4; ++t)
#pragma unroll
        for (int r = 0; r < 4; r += 2) {
          const unsigned u = pk(sc[t][r], sc[t][r + 1]), v = pk(blo(u) * 0.088388347648318440f, bhi(u) * 0.088388347648318440f);
          sc[t][r] = blo(v); sc[t][r + 1] = bhi(v);
        }
      float mw = fmaxf(fmaxf(sc[0].x, sc[0].y), fmaxf(sc[0].z, sc[0].w));
#pragma unroll
      for (int t = 1; t < 4; ++t) mw = fmaxf(mw, fmaxf(fmaxf(sc[t].x, sc[t].y), fmaxf(sc[t].z, sc[t].w)));
      mw = x32_max(x16_max(mw)) * LOG2E;
      float lw = 0.f;
      const unsigned pm = (lane & 1) ? 0xffff0000u : 0u;
      unsigned P[8];
#pragma unroll
      for (int t = 0; t < 4; ++t)
#pragma unroll
        for (int r = 0; r < 4; r += 2) {
          const float v0 = ex2(fmaf(sc[t][r], LOG2E, -mw)), v1 = ex2(fmaf(sc[t][r + 1], LOG2E, -mw));
          lw += v0 + v1;
          const unsigned h = pk(v0, v1);
          P[2 * t + (r >> 1)] = pk(v0 - asf((h << 16) & pm), v1 - asf(h & pm));
        }
      *(LAS v4u*)(L + L_P + w * 2048 + lane * 16) = v4u{P[0], P[1], P[2], P[3]};
      *(LAS v4u*)(L + L_P + w * 2048 + 1024 + lane * 16) = v4u{P[4], P[5], P[6], P[7]};
      lw = x32_sum(x16_sum(lw));
      if (lane < 16 && !(lane & 1)) *(LAS f2*)(L + L_ST + (w * 8 + (lane >> 1)) * 8) = f2{mw, lw};
      lds_signal<F_P>(vsig);
    }
    lds_wait<1>(L, F_P, 8);
    float o0 = 0.f, o1 = 0.f;
    {
      f4 st[8];
#pragma unroll
      for (int j = 0; j < 8; ++j) st[j] = *(const LAS f4*)(L + L_ST + (j * 8 + 2 * g) * 8);
      float M0 = st[0].x, M1 = st[0].z;
#pragma unroll
      for (int j = 1; j < 8; ++j) { M0 = fmaxf(M0, st[j].x); M1 = fmaxf(M1, st[j].z); }
      M0 = __builtin_ceilf(M0); M1 = __builtin_ceilf(M1);
#pragma unroll
      for (int j = 0; j < 8; ++j) { st[j].x = ex2(st[j].x - M0); st[j].z = ex2(st[j].z - M1); }
      int ovs = (lane & 2) ? OOB : (2 * g + (lane & 1)) * BLK + (n16 >> 2) * 16;
      asm volatile("" : "+v"(ovs));
      const LAS unsigned char* vp = L + (4 * g + q4) * 256 + ((w ^ (4 * (g & 1) + q4)) * 32) + p4 * 8;
#pragma unroll
      for (int j = 0; j < 8; ++j) {
        const v4u pa = *(const LAS v4u*)(L + L_P + j * 2048 + lane * 16), pb = *(const LAS v4u*)(L + L_P + j * 2048 + 1024 + lane * 16);
        if (j < 7) lds_wait<2>(L, F_FULL, 13 + j); else lds_wait<0>(L, F_FULL, 20);
        const LAS unsigned char* vq = vp + ((4 + j) & 7) * 16384;
        const v4u va = trfrag(vq, 0, 4096), vb = trfrag(vq, 8192, 12288);
        if (j == 0) lds_signal<F_DV>(vsig); else if (j == 1) lds_signal<F_DV + 1>(vsig); else if (j == 2) lds_signal<F_DV + 2>(vsig); else if (j == 3) lds_signal<F_DV + 3>(vsig);
        f4 d = mfma32(pa, va, f4{0.f, 0.f, 0.f, 0.f});
        d = mfma32(pb, vb, d);
        o0 = fmaf(st[j].x, d.x + d.y, o0);
        o1 = fmaf(st[j].z, d.z + d.w, o1);
      }
      const bool odd = (lane & 1) != 0;
      const float rcv = dpp<XOR1>(odd ? o0 : o1);
      const unsigned gr = odd ? pk(rcv, o1) : pk(o0, rcv);
      const unsigned gn = dppu<XOR2>(gr);
      stc4(rws, ovs, WS_OP + ahkv * 8 * BLK + (w * 32 + sp) * 128 + ab * 64, v4u{gr, tag, gn, tag});
    }
    const LAS unsigned char* wp = L + (8 * g + q4) * 256 + ((w ^ (q4 | ((g & 1) << 2))) * 32) + p4 * 8;
    const int fvo = (g >> 1) * 2048 + (g & 1) * 32;
    {
      v4u A[4];
      lds_wait<1>(L, F_H, 1);
#pragma unroll
      for (int s = 0; s < 4; ++s) A[s] = *(const LAS v4u*)(L + L_HS + (lane & 1) * 272 + s * 64 + g * 16);
      lds_wait<2>(L, F_FULL, 22);
      f4 d = {0.f, 0.f, 0.f, 0.f};
#pragma unroll
      for (int s = 0; s < 4; ++s) d = mfma32(trfrag(wp + 4 * 16384, s * 8192, s * 8192 + 1024), A[s], d);
      stc4(rws2, n16 < 2 ? fvo + n16 * 16 : OOB, WS_MP + cgo * BLK + w * 4096 + ks2 * 64, tg4(d, ta, tb));
    }
    {
      const int ovo = ((n16 >> 2) & 1) * 2048 + g * 512 + (n16 >> 3) * 64 + (lane & 3) * 16, oso = WS_OP + ho * BLK + w * 4096;
      v4u G[4];
      lds_wait<0>(L, F_OG, 1);
#pragma unroll
      for (int j = 0; j < 4; ++j) G[j] = ldc4(rws, ovo + j * 128, oso);
      SB();
      lds_wait<0>(L, F_W, 1);
      v2u WF = *(const LAS v2u*)(L + L_WT + (lane & 3) * 32 + g * 8);
      float invl = ((const LAS float*)(L + L_WT + 128))[(lane >> 1) & 1];
      LAS unsigned char* osw = L + (n16 == g ? L_OS + g * 272 + w * 32 : L_QS + lane * 32);
      const LAS unsigned char* osr = L + L_OS + (lane & 3) * 272 + g * 16;
      const LAS unsigned char* wop = wp + 6 * 16384;
      int fv2 = (n16 & 13) == 0 ? fvo + (n16 >> 1) * 16 : OOB, fso = WS_FP + cgo * BLK + w * 4096 + ho * 64;
      unsigned vta = ta, vtb = tb, vtag = tag;
      asm volatile("" : "+v"(WF), "+v"(invl), "+v"(osw), "+v"(osr), "+v"(wop), "+v"(fv2), "+s"(fso), "+v"(vta), "+v"(vtb), "+v"(vtag));
      lds_wait<0>(L, F_FULL, 24);
      v4u Bo[4];
#pragma unroll
      for (int s = 0; s < 4; ++s) Bo[s] = trfrag(wop, s * 8192, s * 8192 + 1024);
      SB();
      const f4 z = {0.f, 0.f, 0.f, 0.f};
      f4 e0, e1, e2, e3;
#define COMBINE() do { \
        e0 = mfma16(v2u{plo(G[0].x, G[1].x), plo(G[2].x, G[3].x)}, WF, z); e1 = mfma16(v2u{phi(G[0].x, G[1].x), phi(G[2].x, G[3].x)}, WF, z); \
        e2 = mfma16(v2u{plo(G[0].z, G[1].z), plo(G[2].z, G[3].z)}, WF, z); e3 = mfma16(v2u{phi(G[0].z, G[1].z), phi(G[2].z, G[3].z)}, WF, z); } while (0)
#define OBAD(j, acc) xoror(G[j].y, vtag, xoror(G[j].w, vtag, acc))
      COMBINE();
      if (__builtin_expect(__any((OBAD(0, OBAD(1, 0u)) | OBAD(2, OBAD(3, 0u))) != 0u), 0)) {
#pragma nounroll
        for (unsigned spin = 0; spin < SPIN_CAP; ++spin) {
          const bool m0 = __any(OBAD(0, 0u) != 0u), m1 = __any(OBAD(1, 0u) != 0u), m2 = __any(OBAD(2, 0u) != 0u), m3 = __any(OBAD(3, 0u) != 0u);
          if (!(m0 || m1 || m2 || m3)) break;
          FENCE();
          if (m0) G[0] = ldc4(rws, ovo, oso);
          if (m1) G[1] = ldc4(rws, ovo + 128, oso);
          if (m2) G[2] = ldc4(rws, ovo + 256, oso);
          if (m3) G[3] = ldc4(rws, ovo + 384, oso);
        }
        COMBINE();
      }
      *(LAS v4u*)osw = v4u{pk(e0.x, e1.x), pk(e2.x, e3.x), pk(e0.y, e1.y), pk(e2.y, e3.y)};
      *(LAS v4u*)(osw + 16) = v4u{pk(e0.z, e1.z), pk(e2.z, e3.z), pk(e0.w, e1.w), pk(e2.w, e3.w)};
      LGKM0();
      __builtin_amdgcn_s_barrier();
      FENCE();
      v4u a0 = *(const LAS v4u*)osr, a1 = *(const LAS v4u*)(osr + 64), a2 = *(const LAS v4u*)(osr + 128), a3 = *(const LAS v4u*)(osr + 192);
      SB();
      f4 d = mfma32(Bo[0], a0, z), d2 = mfma32(Bo[1], a1, z);
      d = mfma32(Bo[2], a2, d); d2 = mfma32(Bo[3], a3, d2);
      d += d2;
      d.x += dpp<XOR1>(d.x); d.y += dpp<XOR1>(d.y); d.z += dpp<XOR1>(d.z); d.w += dpp<XOR1>(d.w);
      d *= invl;
      stc4(rws, fv2, fso, v4u{(asu(d.x) & 0xffffff00u) | vta, (asu(d.y) & 0xffffff00u) | vtb, (asu(d.z) & 0xffffff00u) | vta, (asu(d.w) & 0xffffff00u) | vtb});
    }
  } else if (wv == NWAVES - 1) {
    volatile LAS unsigned* fl = (volatile LAS unsigned*)(L + L_FL);
    const rsrc_t rws = mk(ws, WS_END), rws1 = mk(ws, has1 ? WS_END : 0u), rws2 = mk(ws, has2 ? WS_END : 0u), rout = mk(out, 4096);
    const unsigned tag = __builtin_amdgcn_readfirstlane(eraw) * 0x9E3779B1u + 0x7F4A7C15u;
    const unsigned ta = tag & 0xffu, tb = (tag >> 8) & 0xffu;
    fl[F_TAG] = tag;
    lds_signal<F_LN>(vsig);
    const unsigned b1p = __builtin_amdgcn_raw_buffer_load_b32(mk(b1, has1 ? 7168u : 0u), (n16 >> 2) * 8 + (lane & 1) * 4, c1 * 32, 0);
    const int col2 = ((lane >> 1) & 1) * 8 + ((g >> 1) | ((g & 1) << 1)) * 2;
    const int fo = (lane & 1) * 2048 + col2;
    const unsigned short xfin = __builtin_amdgcn_raw_buffer_load_b16(mk(x, c < 128 ? 4096u : 0u), fo, c * 16, 0);
    const unsigned short b2fin = __builtin_amdgcn_raw_buffer_load_b16(mk(b2, c < 128 ? 2048u : 0u), col2, c * 16, 0);
    FENCE();
    lds_wait<1>(L, F_FULL, 4);
    const unsigned t4 = (unsigned)__builtin_amdgcn_s_memrealtime();
    lds_wait<2>(L, F_FULL, S1GATE);
    lds_wait<2>(L, F_DQ, 4);
    lds_wait<2>(L, F_DH, 4);
    {
      const int fvo = (lane < 16 && !(lane & 1)) ? (lane >> 2) * 16 : OOB, fb = (lane >> 1) & 1;
      const unsigned t20 = tag & 0xfffffu;
      bool needq = true, needh = has1, needg = true;
#pragma nounroll
      for (unsigned spin = 0; (needq || needh || needg) && spin < SPIN_CAP; ++spin) {
        const bool tryg = needg && !needq && (unsigned)__builtin_amdgcn_readfirstlane(fl[F_FULL]) >= QGATE;
        if (!(needq || needh || tryg)) { __builtin_amdgcn_s_sleep(1); continue; }
        const v2u GQ = ldc2(rws, needq ? lane * 8 : OOB, WS_QP + (c >> 6) * BLK + (c & 63) * 512), GH = ldc2(rws1, needh ? lane * 8 : OOB, WS_HP + (c1 >> 6) * BLK + (c1 & 63) * 512);
        v4u Q[4];
#pragma unroll
        for (int i = 0; i < 4; ++i) Q[i] = ldc4(rws, tryg ? lane * 16 : OOB, WS_QF + grp * 4096 + i * 1024);
        if (needq && !__any((((GQ.x ^ ta) | (GQ.y ^ tb)) & 0xffu) != 0u)) {
          const unsigned gr = pk(x32_sum(x16_sum(untg(GQ.x))), x32_sum(x16_sum(untg(GQ.y)))), gn = dppu<XOR1>(gr), ty = (tag << 12) | (t4 & 0xfffu);
          stc4(rws, fvo + fb * 16384, WS_QF + c * 64, v4u{gr, ty, gn, ty});
          needq = false;
        }
        if (tryg) {
          unsigned bad = 0;
#pragma unroll
          for (int i = 0; i < 4; ++i) bad |= ((Q[i].y >> 12) ^ t20) | ((Q[i].w >> 12) ^ t20);
          if (!__any(bad != 0u)) {
#pragma unroll
            for (int i = 0; i < 4; ++i) *(LAS v2u*)(L + L_QS + (2 * i + (lane >> 5)) * 272 + (lane & 31) * 8) = v2u{Q[i].x, Q[i].z};
            lds_signal<F_Q>(vsig);
            int d = 0;
#pragma unroll
            for (int i = 0; i < 4; ++i) d += (int)((Q[i].y - t4) << 20) >> 20;
            fl[F_REF] = (t4 + (int)(wave_sum((float)d) * (1.0f / 256))) | 1u;
            needg = false;
          }
        }
        if (needh && !__any((((GH.x ^ ta) | (GH.y ^ tb)) & 0xffu) != 0u)) {
          float h0 = rb(rb(x32_sum(x16_sum(untg(GH.x)))) + blo(b1p)), h1 = rb(rb(x32_sum(x16_sum(untg(GH.y)))) + bhi(b1p));
          constexpr float GC = -2.0f * 0.7978845608028654f * 1.4426950408889634f;
          h0 = h0 * rcp(1.0f + ex2(GC * (h0 + 0.044715f * h0 * h0 * h0)));
          h1 = h1 * rcp(1.0f + ex2(GC * (h1 + 0.044715f * h1 * h1 * h1)));
          const unsigned gr = pk(h0, h1), gn = dppu<XOR1>(gr);
          stc4(rws, fvo + fb * 14336, WS_HF + c1 * 64, v4u{gr, tag, gn, tag});
          needh = false;
        }
      }
      if (needg) lds_signal<F_Q>(vsig);
    }
    lds_wait<1>(L, F_P, 8);
    {
      f2 sv[8];
#pragma unroll
      for (int j = 0; j < 8; ++j) sv[j] = *(const LAS f2*)(L + L_ST + (j * 8 + (lane & 7)) * 8);
      float M = sv[0].x, Ls = 0.f;
#pragma unroll
      for (int j = 1; j < 8; ++j) M = fmaxf(M, sv[j].x);
      M = __builtin_ceilf(M);
#pragma unroll
      for (int j = 0; j < 8; ++j) Ls = fmaf(ex2(sv[j].x - M), sv[j].y, Ls);
      stc4(rws, lane < 8 ? (lane >> 2) * 2048 + (lane & 3) * 16 : OOB, WS_ST + grp * 4096 + sp * 64, v4u{asu(M), tag, asu(Ls), tag});
      const unsigned tag2 = has2 ? tag : 0u;
      v4u H;
#pragma nounroll
      for (unsigned spin = 0; spin < SPIN_CAP; ++spin) {
        H = ldc4(rws2, (lane >> 5) * 14336 + (lane & 31) * 16, WS_HF + ks2 * 512);
        if (!__any(((H.y ^ tag2) | (H.w ^ tag2)) != 0u)) break;
        __builtin_amdgcn_s_sleep(2);
      }
      *(LAS v2u*)(L + L_HS + (lane >> 5) * 272 + (lane & 31) * 8) = v2u{H.x, H.z};
      lds_signal<F_H>(vsig);
    }
    lds_wait<2>(L, F_FULL, SGATE);
    {
      v4u SG;
#pragma nounroll
      for (unsigned spin = 0; spin < SPIN_CAP; ++spin) {
        SG = ldc4(rws, (lane >> 5) * 16384 + (lane & 31) * 64, WS_ST + (ho >> 2) * 2048 + (ho & 3) * 16);
        if (!__any(((SG.y ^ tag) | (SG.w ^ tag)) != 0u)) break;
        __builtin_amdgcn_s_sleep(2);
      }
      const float m = asf(SG.x), M = x16_max(row_max(m));
      const float e = rb(ex2(m - M)), Ls = x16_sum(row_sum(e * asf(SG.z)));
      ((LAS unsigned short*)(L + L_WT))[lane] = (unsigned short)(asu(e) >> 16);
      ((LAS float*)(L + L_WT + 128))[lane >> 5] = rcp(Ls) * (1.0f + 1.0f / 131072);
      lds_signal<F_W>(vsig);
    }
    {
      const rsrc_t rwf = mk(ws, c < 128 ? WS_END : 0u);
      const unsigned mlive = lane < 48 ? 0xffffffffu : 0u;
      __builtin_amdgcn_s_barrier();
      FENCE();
      if (c < 128) {
        int so = WS_MP + (c >> 4) * BLK + (c & 15) * 2048, ov = OOB, ev = OOB, pass = 0;
        unsigned live = mlive;
        float gt = 0.f;
        const float xv = blo(xfin), bv = blo(b2fin);
        asm volatile("" : "+s"(pass));
#pragma nounroll
        for (;;) {
          v4u A0 = ldc4(rwf, lane * 16, so), A1 = ldc4(rwf, lane * 16, so + 1024);
#pragma nounroll
          for (unsigned spin = 0; spin < SPIN_CAP; ++spin) {
            const bool m0 = __any(bad4(A0, ta, tb) != 0u), m1 = __any((bad4(A1, ta, tb) & live) != 0u);
            if (!(m0 || m1)) break;
            FENCE();
            if (m0) A0 = ldc4(rwf, lane * 16, so);
            if (m1) A1 = ldc4(rwf, lane * 16, so + 1024);
          }
          const float r = rb(red4(A0, A1 & live));
          const float o = xv + rb(r * gt);
          __builtin_amdgcn_raw_buffer_store_b16((unsigned short)pk(o, o), rout, ov, c * 16, AUX_SC1);
          stc4(rws, ev, WS_E, v4u{tag, tag, tag, tag});
          SB();
          if (pass) break;
          const float m = rb(r + bv);
          gt = rb(m * rcp(1.0f + ex2(-1.4426950408889634f * m)));
          pass = 1; so -= WS_MP - WS_FP; live = 0xffffffffu;
          ov = (lane & 12) == 0 ? fo : OOB; ev = (c == 0 && lane < 4) ? lane * 16 : OOB;
          asm volatile("" : "+s"(pass), "+s"(so), "+v"(live), "+v"(ov), "+v"(ev), "+v"(gt));
        }
      }
    }
  } else {
    const rsrc_t rq = mk(wq, WQB);
    const int r8 = lane >> 3, c8 = lane & 7, f0 = r8 & 2;
    volatile LAS unsigned* fl = (volatile LAS unsigned*)(L + L_FL);
    const unsigned lds0 = (unsigned)(size_t)smem;
    Ld st = {WIN - 16, 0, lds0 + L_FL + F_FULL * 4};
    unsigned w4 = WMID - 4, to = 0x7fffffffu;
    groups<8 * 8192>(st, rq, vq0, vq1, vq0, vq1, so, m0v, m0v + (32 - WIN) * 1024, w4, to);
    to = NMID;
    {
      const int v0 = r8 * 7168 + (c8 ^ f0) * 16;
      const rsrc_t r1 = mk(w1, has1 ? W1B : 0u);
#pragma nounroll
      for (int j = 0; j < 4; ++j) {
        const int qq = (j + sp) & 3;
        int so1 = (ks1 * 256 + qq * 64) * 7168 + cg1 * 128;
        unsigned m1 = lds0 + 32768 + qq * 8192;
        groups<8 * 7168>(st, r1, v0, v0 ^ 64, v0, v0 ^ 64, so1, m1, m1 + 8192, w4, to);
      }
    }
    const int kvb = grp * 4194304 + sp * 16384, rot = grp * 3 + (sp >> 2);
    {
      const int v0 = g * 256 + (n16 ^ g) * 16;
      stream<1024, 4, 8>(L, st, mk(kc, KVB), v0, v0 ^ 64, v0 ^ 128, v0 ^ 192, kvb, rot, lds0, w4, to);
    }
    {
      const int v0 = g * 256 + (n16 ^ (2 * g)) * 16;
      stream<1024, 12, 8>(L, st, mk(vc, KVB), v0, v0 ^ 128, v0, v0 ^ 128, kvb, rot, lds0, w4, to);
    }
    {
      const int v0 = g * 2048 + (n16 ^ (2 * g)) * 16;
      stream<8192, 20, 2>(L, st, mk(w2, has2 ? W1B : 0u), v0, v0, v0 ^ 128, v0 ^ 128, ks2 * 128 * 2048 + cgo * 256, 0, lds0, w4, to);
      stream<8192, 22, 2>(L, st, mk(wo, WQB), v0, v0, v0 ^ 128, v0 ^ 128, ho * 128 * 2048 + cgo * 256, 0, lds0, w4, to);
    }
    fl[F_OG] = 1;
    asm volatile("s_waitcnt vmcnt(32)" ::: "memory");
    if (st.pub < 22) fl[F_FULL] = 22;
    asm volatile("s_waitcnt vmcnt(16)" ::: "memory");
    fl[F_FULL] = 23;
    asm volatile("s_waitcnt vmcnt(0)" ::: "memory");
    fl[F_FULL] = 24;
  }
}

}

extern "C" void kernel_launch(void* const* d_in, const int* in_sizes, int n_in, void* d_out, int out_size, void* d_ws, size_t ws_size,
                              hipStream_t stream) {
  (void)in_sizes; (void)n_in; (void)out_size; (void)ws_size;
  void* ws = (void*)(((size_t)d_ws + WS_END - 1) & ~(size_t)(WS_END - 1));
  fused_decode<<<256, NT, 0, stream>>>((const unsigned short*)d_in[0], d_in[3], ws, d_in[6], d_in[1], d_in[2], d_in[8], d_in[4], d_in[7], d_in[9], d_out);
}
```

```cpp
#include <hip/hip_runtime.h>
#include <hip/hip_bf16.h>


namespace {

typedef unsigned v4u __attribute__((ext_vector_type(4)));
typedef unsigned v2u __attribute__((ext_vector_type(2)));
typedef float f4 __attribute__((ext_vector_type(4)));
typedef float f2 __attribute__((ext_vector_type(2)));
typedef __bf16 bf8 __attribute__((ext_vector_type(8)));
typedef __bf16 bf2 __attribute__((ext_vector_type(2)));
typedef short s4 __attribute__((ext_vector_type(4)));
typedef __amdgpu_buffer_rsrc_t rsrc_t;
#define LAS __attribute__((address_space(3)))

constexpr int NWAVES = 10;
constexpr int NT = 64 * NWAVES;
constexpr unsigned KVB = 2u * 4 * 16384 * 128 * 2, WQB = 1024u * 4096 * 2, W1B = 1024u * 3584 * 2;
constexpr unsigned BLK = 65536, GAP = 32768;
constexpr unsigned WS_OP = 0;
constexpr unsigned WS_FP = GAP;
constexpr unsigned WS_MP = 8 * BLK + GAP;
constexpr unsigned WS_QP = 16 * BLK + GAP;
constexpr unsigned WS_HP = 20 * BLK + GAP;
constexpr unsigned WS_QF = 24 * BLK + GAP;
constexpr unsigned WS_HF = 25 * BLK + GAP;
constexpr unsigned WS_ST = 26 * BLK + GAP;
constexpr unsigned WS_E = 27 * BLK + GAP;
constexpr unsigned WS_END = 32 * BLK;
constexpr int L_P = 131072, L_XS = L_P, L_YS = L_P + 4096, L_QS = L_P + 16384, L_HS = L_QS + 8 * 272, L_OS = L_HS + 2 * 272,
              L_ST = L_OS + 4 * 272, L_WT = L_ST + 512, L_FL = L_WT + 256, L_SINK = L_FL + 128, L_END = L_SINK + 512;
constexpr int LDS_BYTES = L_END;
static_assert(L_END <= 163840 && (L_HS % 16) == 0 && (L_OS % 16) == 0 && (L_ST % 16) == 0 && (L_WT % 16) == 0 && (L_FL % 16) == 0, "lds map");
enum { F_FULL = 0, F_LN = 1, F_Q = 2, F_P = 3, F_H = 4, F_REF = 5, F_TAG = 6, F_W = 7, F_D = 8, F_DQ = F_D, F_DH = F_D + 2, F_DK = F_D + 4, F_DV = F_D + 12, F_OG = 25, F_WORDS = 32 };
constexpr int OOB = 0x7ff00000;
constexpr int AUX_SC1 = 16;
constexpr unsigned SPIN_CAP = 1u << 15, LSPIN_CAP = 1u << 20;
constexpr float LOG2E = 1.4426950408889634f;
constexpr int WIN = 24, WLO = 17, WMID = 25, WHI = 33, NMID = 4, NLO = 8, TAU = 56, TAU2 = 46, TOFF = 15, TTH = 20;
constexpr unsigned S1GATE = 5, QGATE = 9, SGATE = 19;
constexpr int PFBYTES = 14336;
static_assert(WIN == 24 && WHI <= 60, "first burst in whole groups inside the wq tile; vmcnt has 6 bits");

__device__ __forceinline__ rsrc_t mk(const void* p, unsigned bytes) { return __builtin_amdgcn_make_buffer_rsrc((void*)p, 0, bytes, 0x00020000); }
__device__ __forceinline__ v4u ldc4(rsrc_t r, int vo, int so) { return __builtin_amdgcn_raw_buffer_load_b128(r, vo, so, AUX_SC1); }
__device__ __forceinline__ v2u ldc2(rsrc_t r, int vo, int so) { return __builtin_amdgcn_raw_buffer_load_b64(r, vo, so, AUX_SC1); }
__device__ __forceinline__ void stc4(rsrc_t r, int vo, int so, v4u v) { __builtin_amdgcn_raw_buffer_store_b128(v, r, vo, so, AUX_SC1); }
__device__ __forceinline__ float asf(unsigned u) { return __builtin_bit_cast(float, u); }
__device__ __forceinline__ unsigned asu(float f) { return __builtin_bit_cast(unsigned, f); }
__device__ __forceinline__ unsigned pk(float a, float b) { f2 v = {a, b}; return __builtin_bit_cast(unsigned, __builtin_convertvector(v, bf2)); }
__device__ __forceinline__ float blo(unsigned u) { return asf(u << 16); }
__device__ __forceinline__ float bhi(unsigned u) { return asf(u & 0xffff0000u); }
__device__ __forceinline__ float rb(float a) { return blo(pk(a, a)); }
__device__ __forceinline__ unsigned plo(unsigned a, unsigned b) { return __builtin_amdgcn_perm(b, a, 0x05040100u); }
__device__ __forceinline__ unsigned phi(unsigned a, unsigned b) { return __builtin_amdgcn_perm(b, a, 0x07060302u); }
__device__ __forceinline__ f4 mfma32(v4u a, v4u b, f4 c) { return __builtin_amdgcn_mfma_f32_16x16x32_bf16(__builtin_bit_cast(bf8, a), __builtin_bit_cast(bf8, b), c, 0, 0, 0); }
__device__ __forceinline__ f4 mfma16(v2u a, v2u b, f4 c) { return __builtin_amdgcn_mfma_f32_16x16x16bf16_1k(__builtin_bit_cast(s4, a), __builtin_bit_cast(s4, b), c, 0, 0, 0); }
template <int C> __device__ __forceinline__ float dpp(float x) { return asf(__builtin_amdgcn_mov_dpp(asu(x), C, 0xf, 0xf, true)); }
template <int C> __device__ __forceinline__ unsigned dppu(unsigned x) { return __builtin_amdgcn_mov_dpp(x, C, 0xf, 0xf, true); }
constexpr int XOR1 = 0xB1, XOR2 = 0x4E, ROR4 = 0x124, ROR8 = 0x128;
__device__ __forceinline__ float x16_sum(float x) { auto s = __builtin_amdgcn_permlane16_swap(asu(x), asu(x), false, false); return asf(s[0]) + asf(s[1]); }
__device__ __forceinline__ float x32_sum(float x) { auto s = __builtin_amdgcn_permlane32_swap(asu(x), asu(x), false, false); return asf(s[0]) + asf(s[1]); }
__device__ __forceinline__ float x16_max(float x) { auto s = __builtin_amdgcn_permlane16_swap(asu(x), asu(x), false, false); return fmaxf(asf(s[0]), asf(s[1])); }
__device__ __forceinline__ float x32_max(float x) { auto s = __builtin_amdgcn_permlane32_swap(asu(x), asu(x), false, false); return fmaxf(asf(s[0]), asf(s[1])); }
__device__ __forceinline__ float row_sum(float x) { x += dpp<XOR1>(x); x += dpp<XOR2>(x); x += dpp<ROR4>(x); x += dpp<ROR8>(x); return x; }
__device__ __forceinline__ float row_max(float x) { x = fmaxf(x, dpp<XOR1>(x)); x = fmaxf(x, dpp<XOR2>(x)); x = fmaxf(x, dpp<ROR4>(x)); x = fmaxf(x, dpp<ROR8>(x)); return x; }
__device__ __forceinline__ float wave_sum(float x) { return x32_sum(x16_sum(row_sum(x))); }
__device__ __forceinline__ float ex2(float x) { return __builtin_amdgcn_exp2f(x); }
__device__ __forceinline__ float rcp(float x) { return __builtin_amdgcn_rcpf(x); }
__device__ __forceinline__ unsigned tg(float v, unsigned t8) { return ((asu(v) + 0x80u) & 0xffffff00u) | t8; }
__device__ __forceinline__ v4u tg4(f4 v, unsigned ta, unsigned tb) { return v4u{tg(v.x, ta), tg(v.y, tb), tg(v.z, ta), tg(v.w, tb)}; }
__device__ __forceinline__ float untg(unsigned u) { return asf(u & 0xffffff00u); }
__device__ __forceinline__ unsigned xoror(unsigned a, unsigned b, unsigned c) { return __builtin_amdgcn_bitop3_b32(a, b, c, 0xBE); }
__device__ __forceinline__ unsigned bad4(v4u u, unsigned ta, unsigned tb) { return xoror(u.x, ta, xoror(u.y, tb, xoror(u.z, ta, u.w ^ tb))) & 0xffu; }
__device__ __forceinline__ float red4(v4u a, v4u b) {
  const auto s01 = __builtin_amdgcn_permlane32_swap(asu(untg(a.x) + untg(b.x)), asu(untg(a.y) + untg(b.y)), false, false);
  const auto s23 = __builtin_amdgcn_permlane32_swap(asu(untg(a.z) + untg(b.z)), asu(untg(a.w) + untg(b.w)), false, false);
  const auto t = __builtin_amdgcn_permlane16_swap(asu(asf(s01[0]) + asf(s01[1])), asu(asf(s23[0]) + asf(s23[1])), false, false);
  float v = asf(t[0]) + asf(t[1]);
  v += dpp<ROR4>(v); v += dpp<ROR8>(v);
  return v;
}
__device__ __forceinline__ v4u trfrag(const LAS unsigned char* a, int off0, int off1) {
  const v2u lo = __builtin_bit_cast(v2u, __builtin_amdgcn_ds_read_tr16_b64_v4i16((LAS s4*)(a + off0)));
  const v2u hi = __builtin_bit_cast(v2u, __builtin_amdgcn_ds_read_tr16_b64_v4i16((LAS s4*)(a + off1)));
  return v4u{lo.x, lo.y, hi.x, hi.y};
}

#define SB() __builtin_amdgcn_sched_barrier(0)
#define FENCE() do { SB(); asm volatile("" ::: "memory"); SB(); } while (0)
#define LGKM0() do { SB(); asm volatile("s_waitcnt lgkmcnt(0)" ::: "memory"); SB(); } while (0)

template <int SLEEP> __device__ __forceinline__ void lds_wait(LAS unsigned char* L, int word, unsigned want) {
  FENCE();
  const volatile LAS unsigned* p = (const volatile LAS unsigned*)(L + L_FL) + word;
#pragma nounroll
  for (unsigned i = 0; i < LSPIN_CAP; ++i) {
    if ((unsigned)__builtin_amdgcn_readfirstlane(*p) >= want) break;
    if (SLEEP) __builtin_amdgcn_s_sleep(SLEEP);
  }
  FENCE();
}
template <int WORD> __device__ __forceinline__ void lds_signal(unsigned vsig) {
  SB();
  asm volatile("s_waitcnt lgkmcnt(0)\n\tds_add_u32 %0, %1 offset:%2" :: "v"(vsig), "v"(1u), "i"(WORD * 4) : "memory");
  SB();
}
struct Ld { int gap; unsigned pub, vfull; };
template <int GS>
__device__ __forceinline__ void groups(Ld& s, rsrc_t r, int v0, int v1, int v2, int v3, int& so, unsigned& m0v, unsigned m0end, unsigned w4, unsigned to) {
  unsigned a, b, v, n;
  asm volatile("s_mov_b32 %3, 0\n"
               "0:\n\t"
               "s_getreg_b32 %0, hwreg(HW_REG_IB_STS)\n\t"
               "s_add_u32 %3, %3, 1\n\t"
               "s_and_b32 %1, %0, 15\n\t"
               "s_bfe_u32 %0, %0, 0x20016\n\t"
               "s_lshl4_add_u32 %0, %0, %1\n\t"
               "s_cmp_le_i32 %0, %4\n\t"
               "s_cbranch_scc1 2f\n"
               "1:\n\t"
               "s_cmp_ge_u32 %3, %16\n\t"
               "s_cselect_b32 %1, %17, %15\n\t"
               "s_cmp_le_u32 %0, %1\n\t"
               "s_cbranch_scc0 0b\n\t"
               "s_mov_b32 m0, %7\n\t"
               "s_add_u32 %7, %7, 0x1000\n\t"
               "buffer_load_dwordx4 %9, %13, %6 offen nt lds\n\ts_add_u32 %6, %6, %18\n\ts_mov_b32 %3, 0\n\t"
               "buffer_load_dwordx4 %10, %13, %6 offen offset:1024 nt lds\n\ts_add_u32 %6, %6, %18\n\ts_add_u32 %4, %4, 4\n\t"
               "buffer_load_dwordx4 %11, %13, %6 offen offset:2048 nt lds\n\ts_add_u32 %6, %6, %18\n\t"
               "buffer_load_dwordx4 %12, %13, %6 offen offset:3072 nt lds\n\ts_add_u32 %6, %6, %19\n\t"
               "s_cmp_lt_u32 %7, %14\n\t"
               "s_cbranch_scc1 0b\n\t"
               "s_branch 3f\n"
               "2:\n\t"
               "s_add_u32 %5, %5, 1\n\t"
               "v_mov_b32 %2, %5\n\t"
               "ds_write_b32 %8, %2\n\t"
               "s_sub_u32 %4, %4, 16\n\t"
               "s_branch 1b\n"
               "3:"
               : "=&s"(a), "=&s"(b), "=&v"(v), "=&s"(n), "+s"(s.gap), "+s"(s.pub), "+s"(so), "+s"(m0v)
               : "v"(s.vfull), "v"(v0), "v"(v1), "v"(v2), "v"(v3), "s"(r), "s"(m0end), "s"(w4), "s"(to), "i"(WHI - 4), "i"(GS - 1024), "i"(GS + 3072)
               : "memory", "scc");
}
template <int GS, int F0, int NF>
__device__ __forceinline__ void stream(LAS unsigned char* L, Ld& s, rsrc_t r, int v0, int v1, int v2, int v3, int so, int rot, unsigned lds0, unsigned& w4, unsigned& to) {
  const int base = so;
#pragma nounroll
  for (int f = F0; f < F0 + NF; ++f) {
    unsigned m0v = lds0 + (f & 7) * 16384;
    if (NF == 8) so = base + ((f - F0 + rot) & 7) * 524288;
    if (F0 + NF <= 7) {
      groups<GS>(s, r, v0, v1, v2, v3, so, m0v, m0v + 16384, w4, to);
    } else {
      const int nf = f + 1, word = F_D + (nf < 12 ? (nf & ~1) : nf) - 8;
      const unsigned want = nf < 8 || nf > 23 ? 0u : nf < 12 ? 4u : nf < 20 ? 1u : 8u;
      unsigned vg, vr;
      unsigned long long now;
      asm volatile("ds_read_b32 %0, %2\n\tds_read_b32 %1, %3" : "=&v"(vg), "=&v"(vr) : "v"(lds0 + L_FL + word * 4), "v"(lds0 + L_FL + F_REF * 4) : "memory");
      asm volatile("s_memrealtime %0" : "=s"(now) :: "memory");
      groups<GS>(s, r, v0, v1, v2, v3, so, m0v, m0v + 16384, w4, to);
      asm volatile("s_waitcnt lgkmcnt(0)" : "+v"(vg), "+v"(vr) :: "memory");
      asm volatile("" : "+s"(now) :: "memory");
      const unsigned ref = __builtin_amdgcn_readfirstlane(vr);
      const int lag = ref ? (int)((unsigned)now - ref) - (F0 < 20 ? TOFF + (f - 5) * TAU : TOFF + 15 * TAU + (f - 20) * TAU2) : 0;
      w4 = lag > TTH ? WHI - 4 : lag < -TTH ? WLO - 4 : WMID - 4;
      to = lag < -TTH ? NLO : NMID;
      if (__builtin_expect((unsigned)__builtin_amdgcn_readfirstlane(vg) < want, 0)) lds_wait<2>(L, word, want);
    }
  }
}

__global__ void __launch_bounds__(NT, 3)
fused_decode(const unsigned short* __restrict__ x, const void* __restrict__ wq, void* ws, const void* __restrict__ w1, const void* __restrict__ kc,
             const void* __restrict__ vc, const void* __restrict__ w2, const void* __restrict__ wo, const void* __restrict__ b1,
             const void* __restrict__ b2, void* __restrict__ out) {
  unsigned long long pa, pb; unsigned pd;
  asm volatile("s_memrealtime %0\n\ts_load_dword %2, %3, 0x8\n\ts_waitcnt lgkmcnt(0)\n\ts_memrealtime %1\n\ts_waitcnt lgkmcnt(0)" : "=&s"(pa), "=&s"(pb), "=&s"(pd) : "s"(__builtin_amdgcn_kernarg_segment_ptr()));
  SB();
  __shared__ __attribute__((aligned(16))) unsigned char smem[LDS_BYTES];
  LAS unsigned char* const L = (LAS unsigned char*)smem;
  const int tid = threadIdx.x, lane = tid & 63, wv = __builtin_amdgcn_readfirstlane(tid >> 6), c = blockIdx.x;
  int vq0, vq1, so;
  unsigned m0v;
  v4u rqw;
  if (wv == 0) {
    unsigned t0;
    asm volatile("s_load_dwordx2 s[36:37], %7, 0x8\n\t"
                 "s_getpc_b64 s[40:41]\n\t"
                 "s_setprio 3\n\t"
                 "s_andn2_b32 s40, s40, 63\n\t"
                 "s_lshl_b32 %4, %8, 7\n\t"
                 "s_movk_i32 s42, %9\n\t"
                 "s_mov_b32 s43, 0x20000\n\t"
                 "s_mov_b32 m0, %10\n\t"
                 "s_and_b32 s41, s41, 0xffff\n\t"
                 "v_lshlrev_b32 %0, 4, %6\n\t"
                 "s_and_b32 %2, %4, 0xfffffc00\n\t"
                 "buffer_load_dwordx4 %0, s[40:43], %2 offen lds\n\t"
                 "v_lshrrev_b32 %1, 3, %6\n\t"
                 "v_and_b32 %0, 0x70, %0\n\t"
                 "v_lshl_or_b32 %0, %1, 13, %0\n\t"
                 "v_and_b32 %1, 16, %6\n\t"
                 "v_lshlrev_b32 %1, 1, %1\n\t"
                 "v_xor_b32 %0, %0, %1\n\t"
                 "v_xor_b32 %1, 64, %0\n\t"
                 "s_lshl_b32 %2, %8, 20\n\t"
                 "s_and_b32 %2, %2, 0x600000\n\t"
                 "s_and_b32 %4, %4, 0x80\n\t"
                 "s_or_b32 %2, %2, %4\n\t"
                 "s_lshl_b32 %4, %8, 5\n\t"
                 "s_and_b32 %4, %4, 0x1f00\n\t"
                 "s_or_b32 %2, %2, %4\n\t"
                 "s_bfe_u32 %3, %8, 0x20003\n\t"
                 "s_lshl_b32 %4, %3, 19\n\t"
                 "s_lshl_b32 %3, %3, 13\n\t"
                 "s_and_b32 vcc_lo, %2, 0x600000\n\t"
                 "s_or_b32 %2, %2, %4\n\t"
                 "s_mov_b32 %4, %13\n\t"
                 "s_mov_b32 s38, %12\n\t"
                 "s_mov_b32 s39, s43\n\t"
                 "s_waitcnt lgkmcnt(0)\n\t"
                 "s_and_b32 s37, s37, 0xffff\n"
                 "1:\n\t"
                 "s_mov_b32 m0, %3\n\t"
                 "s_add_u32 %3, %3, 0x800\n\t"
                 "buffer_load_dwordx4 %0, s[36:39], %2 offen nt lds\n\t"
                 "s_add_u32 %2, %2, 0xfc00\n\t"
                 "s_bitset0_b32 %3, 15\n\t"
                 "buffer_load_dwordx4 %1, s[36:39], %2 offen offset:1024 nt lds\n\t"
                 "s_add_u32 %2, %2, 0x10400\n\t"
                 "s_and_b32 %2, %2, 0x1fffff\n\t"
                 "s_or_b32 %2, %2, vcc_lo\n\t"
                 "s_sub_u32 %4, %4, 0x800\n\t"
                 "s_cmp_lg_u32 %4, 0\n\t"
                 "s_cbranch_scc1 1b"
                 : "=&v"(vq0), "=&v"(vq1), "=&s"(so), "=&s"(m0v), "=&s"(t0), "={s[36:39]}"(rqw)
                 : "v"(tid), "s"(__builtin_amdgcn_kernarg_segment_ptr()), "s"(c), "i"(PFBYTES), "i"(L_P + 8192), "i"(0), "i"(WQB), "i"(WIN * 1024)
                 : "memory", "scc", "vcc", "s40", "s41", "s42", "s43");
  }
  SB();
  v4u xa = {0u, 0u, 0u, 0u}, xb = xa;
  unsigned eraw = 0u;
  if (wv != 0) {
    unsigned long long xl = (unsigned long long)x;
    asm volatile("" : "+s"(xl));
    const rsrc_t rxl = mk((const void*)xl, wv < 3 ? 4096u : 0u);
    xa = __builtin_amdgcn_raw_buffer_load_b128(rxl, lane * 16, (wv - 1) * 2048, 0);
    xb = __builtin_amdgcn_raw_buffer_load_b128(rxl, lane * 16, (wv - 1) * 2048 + 1024, 0);
    eraw = __builtin_amdgcn_raw_buffer_load_b32(mk(ws, wv == NWAVES - 1 ? WS_END : 0u), 0, WS_E, AUX_SC1);
  }
  FENCE();
  const int g = lane >> 4, n16 = lane & 15;
  const int grp = c & 7, sp = c >> 3, ab = grp >> 2, ahkv = grp & 3;
  const int ksq = grp >> 1, cgq = sp * 2 + (grp & 1);
  const bool has1 = (sp & 7) != 7;
  const int ks1 = grp >> 1, cg1 = (sp - (sp >> 3)) * 2 + (grp & 1);
  const int c1 = ks1 * 56 + cg1;
  const int cgo = sp & 7;
  const int ho = grp * 4 + (sp >> 3);
  const int ks2 = (grp >> 1) * 7 + (grp & 1) * 4 + (sp >> 3);
  const bool has2 = !((grp & 1) && (sp >> 3) == 3);

  const unsigned vsig = (unsigned)(size_t)smem + (lane == 0 ? L_FL : L_SINK + lane * 4);
  if (wv == NWAVES - 1 && lane < F_WORDS) ((volatile LAS unsigned*)(L + L_FL))[lane] = 0u;
  LGKM0();
  const unsigned pc_ = (unsigned)__builtin_amdgcn_s_memrealtime();
  __builtin_amdgcn_s_barrier();
  FENCE();
  if (wv >= 1 && wv < NWAVES - 1) {
    const int w = wv - 1;
    const rsrc_t rws = mk(ws, WS_END);
    const rsrc_t rws2 = mk(ws, has2 ? WS_END : 0u);
    if (w < 2) {
      float xf[16];
#pragma unroll
      for (int i = 0; i < 4; ++i) { xf[2 * i] = blo(xa[i]); xf[2 * i + 1] = bhi(xa[i]); xf[8 + 2 * i] = blo(xb[i]); xf[9 + 2 * i] = bhi(xb[i]); }
      const float piv = asf(__builtin_amdgcn_readfirstlane(asu(xf[0])));
      float s1 = 0.f, s2 = 0.f;
#pragma unroll
      for (int i = 0; i < 16; ++i) { const float d = xf[i] - piv; s1 += d; s2 = fmaf(d, d, s2); }
      float a1 = wave_sum(s1) * (1.0f / 1024), a2 = wave_sum(s2) * (1.0f / 1024);
      float mean = rb(piv + a1), inv = rcp(rb(__builtin_amdgcn_sqrtf(rb(rb(fmaxf(a2 - a1 * a1, 0.f)) + 1e-5f))));
      v4u pa, pb;
      s1 = 0.f; s2 = 0.f;
#pragma unroll
      for (int i = 0; i < 8; ++i) {
        const unsigned t = pk(xf[2 * i] - mean, xf[2 * i + 1] - mean), u = pk(blo(t) * inv, bhi(t) * inv);
        if (i < 4) pa[i] = u; else pb[i - 4] = u;
        xf[2 * i] = blo(u); xf[2 * i + 1] = bhi(u);
        s1 += xf[2 * i] + xf[2 * i + 1]; s2 = fmaf(xf[2 * i], xf[2 * i], fmaf(xf[2 * i + 1], xf[2 * i + 1], s2));
      }
      *(LAS v4u*)(L + L_XS + w * 2048 + lane * 16) = pa;
      *(LAS v4u*)(L + L_XS + w * 2048 + 1024 + lane * 16) = pb;
      a1 = wave_sum(s1) * (1.0f / 1024); a2 = wave_sum(s2) * (1.0f / 1024);
      mean = rb(a1); inv = rcp(rb(__builtin_amdgcn_sqrtf(rb(rb(fmaxf(a2 - a1 * a1, 0.f)) + 1e-5f))));
#pragma unroll
      for (int i = 0; i < 8; ++i) {
        const unsigned t = pk(xf[2 * i] - mean, xf[2 * i + 1] - mean), u = pk(blo(t) * inv, bhi(t) * inv);
        if (i < 4) pa[i] = u; else pb[i - 4] = u;
      }
      *(LAS v4u*)(L + L_YS + w * 2048 + lane * 16) = pa;
      *(LAS v4u*)(L + L_YS + w * 2048 + 1024 + lane * 16) = pb;
      lds_signal<F_LN>(vsig);
    }
    const int q4 = n16 >> 2, p4 = lane & 3;
    lds_wait<2>(L, F_LN, 3);
    const unsigned tag = __builtin_amdgcn_readfirstlane(((volatile LAS unsigned*)(L + L_FL))[F_TAG]);
    const unsigned ta = tag & 0xffu, tb = (tag >> 8) & 0xffu;
    {
      const bool isq = w < 4;
      const LAS unsigned char* ap = L + (isq ? L_XS + ksq * 512 : L_YS + (ks1 & 3) * 512) + (lane & 1) * 2048 + g * 16;
      v4u A[8];
#pragma unroll
      for (int s = 0; s < 8; ++s) A[s] = *(const LAS v4u*)(ap + s * 64);
      lds_wait<2>(L, F_FULL, isq ? 2 : 4);
      const LAS unsigned char* bp = L + (isq ? 0 : 32768) + (8 * g + q4) * 128 + (((w & 3) ^ ((q4 >> 1) | ((g & 1) << 1))) * 32) + p4 * 8;
      v4u Bf[8];
#pragma unroll
      for (int s = 0; s < 8; ++s) Bf[s] = trfrag(bp, s * 4096, s * 4096 + 512);
      lds_signal<F_DQ>(vsig + (isq ? 0 : (F_DH - F_DQ) * 4));
      f4 d0 = mfma32(Bf[0], A[0], f4{0.f, 0.f, 0.f, 0.f}), d1 = mfma32(Bf[1], A[1], f4{0.f, 0.f, 0.f, 0.f});
#pragma unroll
      for (int s = 2; s < 8; s += 2) { d0 = mfma32(Bf[s], A[s], d0); d1 = mfma32(Bf[s + 1], A[s + 1], d1); }
      const rsrc_t rp = mk(ws, (isq || has1) ? WS_END : 0u);
      const int cg = isq ? cgq : cg1;
      const int pso = (isq ? WS_QP + ksq * 128 : WS_HP + ks1 * 128) + (cg >> 4) * BLK + (cg & 15) * 2048 + (w & 3) * 512;
      stc4(rp, n16 < 2 ? g * 32 + n16 * 16 : OOB, pso, tg4(d0 + d1, ta, tb));
    }
    v4u K[16];
    {
      const LAS unsigned char* kp = L + ((4 + w) & 7) * 16384 + n16 * 256;
      lds_wait<1>(L, F_FULL, 5 + w);
#pragma unroll
      for (int s = 0; s < 4; ++s) {
        const LAS unsigned char* kq = kp + (((4 * s + g) ^ n16) * 16);
#pragma unroll
        for (int t = 0; t < 4; ++t) K[4 * t + s] = *(const LAS v4u*)(kq + t * 4096);
      }
      lds_signal<F_DK>(vsig + w * 4);
    }
    lds_wait<1>(L, F_Q, 1);
    {
      const LAS unsigned char* qb = L + L_QS + (n16 >> 1) * 272 + g * 16;
      v4u qf[4];
#pragma unroll
      for (int s = 0; s < 4; ++s) qf[s] = *(const LAS v4u*)(qb + s * 64);
      f4 sc[4];
#pragma unroll
      for (int t = 0; t < 4; ++t) sc[t] = mfma32(K[4 * t], qf[0], f4{0.f, 0.f, 0.f, 0.f});
#pragma unroll
      for (int s = 1; s < 4; ++s)
#pragma unroll
        for (int t = 0; t < 4; ++t) sc[t] = mfma32(K[4 * t + s], qf[s], sc[t]);
#pragma unroll
      for (int t = 0; t < 4; ++t)
#pragma unroll
        for (int r = 0; r < 4; r += 2) {
          const unsigned u = pk(sc[t][r], sc[t][r + 1]), v = pk(blo(u) * 0.088388347648318440f, bhi(u) * 0.088388347648318440f);
          sc[t][r] = blo(v); sc[t][r + 1] = bhi(v);
        }
      float mw = fmaxf(fmaxf(sc[0].x, sc[0].y), fmaxf(sc[0].z, sc[0].w));
#pragma unroll
      for (int t = 1; t < 4; ++t) mw = fmaxf(mw, fmaxf(fmaxf(sc[t].x, sc[t].y), fmaxf(sc[t].z, sc[t].w)));
      mw = x32_max(x16_max(mw)) * LOG2E;
      float lw = 0.f;
      const unsigned pm = (lane & 1) ? 0xffff0000u : 0u;
      unsigned P[8];
#pragma unroll
      for (int t = 0; t < 4; ++t)
#pragma unroll
        for (int r = 0; r < 4; r += 2) {
          const float v0 = ex2(fmaf(sc[t][r], LOG2E, -mw)), v1 = ex2(fmaf(sc[t][r + 1], LOG2E, -mw));
          lw += v0 + v1;
          const unsigned h = pk(v0, v1);
          P[2 * t + (r >> 1)] = pk(v0 - asf((h << 16) & pm), v1 - asf(h & pm));
        }
      *(LAS v4u*)(L + L_P + w * 2048 + lane * 16) = v4u{P[0], P[1], P[2], P[3]};
      *(LAS v4u*)(L + L_P + w * 2048 + 1024 + lane * 16) = v4u{P[4], P[5], P[6], P[7]};
      lw = x32_sum(x16_sum(lw));
      if (lane < 16 && !(lane & 1)) *(LAS f2*)(L + L_ST + (w * 8 + (lane >> 1)) * 8) = f2{mw, lw};
      lds_signal<F_P>(vsig);
    }
    lds_wait<1>(L, F_P, 8);
    float o0 = 0.f, o1 = 0.f;
    {
      f4 st[8];
#pragma unroll
      for (int j = 0; j < 8; ++j) st[j] = *(const LAS f4*)(L + L_ST + (j * 8 + 2 * g) * 8);
      float M0 = st[0].x, M1 = st[0].z;
#pragma unroll
      for (int j = 1; j < 8; ++j) { M0 = fmaxf(M0, st[j].x); M1 = fmaxf(M1, st[j].z); }
      M0 = __builtin_ceilf(M0); M1 = __builtin_ceilf(M1);
#pragma unroll
      for (int j = 0; j < 8; ++j) { st[j].x = ex2(st[j].x - M0); st[j].z = ex2(st[j].z - M1); }
      int ovs = (lane & 2) ? OOB : (2 * g + (lane & 1)) * BLK + (n16 >> 2) * 16;
      asm volatile("" : "+v"(ovs));
      const LAS unsigned char* vp = L + (4 * g + q4) * 256 + ((w ^ (4 * (g & 1) + q4)) * 32) + p4 * 8;
#pragma unroll
      for (int j = 0; j < 8; ++j) {
        const v4u pa = *(const LAS v4u*)(L + L_P + j * 2048 + lane * 16), pb = *(const LAS v4u*)(L + L_P + j * 2048 + 1024 + lane * 16);
        if (j < 7) lds_wait<2>(L, F_FULL, 13 + j); else lds_wait<0>(L, F_FULL, 20);
        const LAS unsigned char* vq = vp + ((4 + j) & 7) * 16384;
        const v4u va = trfrag(vq, 0, 4096), vb = trfrag(vq, 8192, 12288);
        if (j == 0) lds_signal<F_DV>(vsig); else if (j == 1) lds_signal<F_DV + 1>(vsig); else if (j == 2) lds_signal<F_DV + 2>(vsig); else if (j == 3) lds_signal<F_DV + 3>(vsig);
        f4 d = mfma32(pa, va, f4{0.f, 0.f, 0.f, 0.f});
        d = mfma32(pb, vb, d);
        o0 = fmaf(st[j].x, d.x + d.y, o0);
        o1 = fmaf(st[j].z, d.z + d.w, o1);
      }
      const bool odd = (lane & 1) != 0;
      const float rcv = dpp<XOR1>(odd ? o0 : o1);
      const unsigned gr = odd ? pk(rcv, o1) : pk(o0, rcv);
      const unsigned gn = dppu<XOR2>(gr);
      stc4(rws, ovs, WS_OP + ahkv * 8 * BLK + (w * 32 + sp) * 128 + ab * 64, v4u{gr, tag, gn, tag});
    }
    const LAS unsigned char* wp = L + (8 * g + q4) * 256 + ((w ^ (q4 | ((g & 1) << 2))) * 32) + p4 * 8;
    const int fvo = (g >> 1) * 2048 + (g & 1) * 32;
    {
      v4u A[4];
      lds_wait<1>(L, F_H, 1);
#pragma unroll
      for (int s = 0; s < 4; ++s) A[s] = *(const LAS v4u*)(L + L_HS + (lane & 1) * 272 + s * 64 + g * 16);
      lds_wait<2>(L, F_FULL, 22);
      f4 d = {0.f, 0.f, 0.f, 0.f};
#pragma unroll
      for (int s = 0; s < 4; ++s) d = mfma32(trfrag(wp + 4 * 16384, s * 8192, s * 8192 + 1024), A[s], d);
      stc4(rws2, n16 < 2 ? fvo + n16 * 16 : OOB, WS_MP + cgo * BLK + w * 4096 + ks2 * 64, tg4(d, ta, tb));
    }
    {
      const int ovo = ((n16 >> 2) & 1) * 2048 + g * 512 + (n16 >> 3) * 64 + (lane & 3) * 16, oso = WS_OP + ho * BLK + w * 4096;
      v4u G[4];
      lds_wait<0>(L, F_OG, 1);
#pragma unroll
      for (int j = 0; j < 4; ++j) G[j] = ldc4(rws, ovo + j * 128, oso);
      SB();
      lds_wait<0>(L, F_W, 1);
      v2u WF = *(const LAS v2u*)(L + L_WT + (lane & 3) * 32 + g * 8);
      float invl = ((const LAS float*)(L + L_WT + 128))[(lane >> 1) & 1];
      LAS unsigned char* osw = L + (n16 == g ? L_OS + g * 272 + w * 32 : L_QS + lane * 32);
      const LAS unsigned char* osr = L + L_OS + (lane & 3) * 272 + g * 16;
      const LAS unsigned char* wop = wp + 6 * 16384;
      int fv2 = (n16 & 13) == 0 ? fvo + (n16 >> 1) * 16 : OOB, fso = WS_FP + cgo * BLK + w * 4096 + ho * 64;
      unsigned vta = ta, vtb = tb, vtag = tag;
      asm volatile("" : "+v"(WF), "+v"(invl), "+v"(osw), "+v"(osr), "+v"(wop), "+v"(fv2), "+s"(fso), "+v"(vta), "+v"(vtb), "+v"(vtag));
      lds_wait<0>(L, F_FULL, 24);
      v4u Bo[4];
#pragma unroll
      for (int s = 0; s < 4; ++s) Bo[s] = trfrag(wop, s * 8192, s * 8192 + 1024);
      SB();
      const f4 z = {0.f, 0.f, 0.f, 0.f};
      f4 e0, e1, e2, e3;
#define COMBINE() do { \
        e0 = mfma16(v2u{plo(G[0].x, G[1].x), plo(G[2].x, G[3].x)}, WF, z); e1 = mfma16(v2u{phi(G[0].x, G[1].x), phi(G[2].x, G[3].x)}, WF, z); \
        e2 = mfma16(v2u{plo(G[0].z, G[1].z), plo(G[2].z, G[3].z)}, WF, z); e3 = mfma16(v2u{phi(G[0].z, G[1].z), phi(G[2].z, G[3].z)}, WF, z); } while (0)
#define OBAD(j, acc) xoror(G[j].y, vtag, xoror(G[j].w, vtag, acc))
      COMBINE();
      if (__builtin_expect(__any((OBAD(0, OBAD(1, 0u)) | OBAD(2, OBAD(3, 0u))) != 0u), 0)) {
#pragma nounroll
        for (unsigned spin = 0; spin < SPIN_CAP; ++spin) {
          const bool m0 = __any(OBAD(0, 0u) != 0u), m1 = __any(OBAD(1, 0u) != 0u), m2 = __any(OBAD(2, 0u) != 0u), m3 = __any(OBAD(3, 0u) != 0u);
          if (!(m0 || m1 || m2 || m3)) break;
          FENCE();
          if (m0) G[0] = ldc4(rws, ovo, oso);
          if (m1) G[1] = ldc4(rws, ovo + 128, oso);
          if (m2) G[2] = ldc4(rws, ovo + 256, oso);
          if (m3) G[3] = ldc4(rws, ovo + 384, oso);
        }
        COMBINE();
      }
      *(LAS v4u*)osw = v4u{pk(e0.x, e1.x), pk(e2.x, e3.x), pk(e0.y, e1.y), pk(e2.y, e3.y)};
      *(LAS v4u*)(osw + 16) = v4u{pk(e0.z, e1.z), pk(e2.z, e3.z), pk(e0.w, e1.w), pk(e2.w, e3.w)};
      LGKM0();
      __builtin_amdgcn_s_barrier();
      FENCE();
      v4u a0 = *(const LAS v4u*)osr, a1 = *(const LAS v4u*)(osr + 64), a2 = *(const LAS v4u*)(osr + 128), a3 = *(const LAS v4u*)(osr + 192);
      SB();
      f4 d = mfma32(Bo[0], a0, z), d2 = mfma32(Bo[1], a1, z);
      d = mfma32(Bo[2], a2, d); d2 = mfma32(Bo[3], a3, d2);
      d += d2;
      d.x += dpp<XOR1>(d.x); d.y += dpp<XOR1>(d.y); d.z += dpp<XOR1>(d.z); d.w += dpp<XOR1>(d.w);
      d *= invl;
      stc4(rws, fv2, fso, v4u{(asu(d.x) & 0xffffff00u) | vta, (asu(d.y) & 0xffffff00u) | vtb, (asu(d.z) & 0xffffff00u) | vta, (asu(d.w) & 0xffffff00u) | vtb});
    }
  } else if (wv == NWAVES - 1) {
    volatile LAS unsigned* fl = (volatile LAS unsigned*)(L + L_FL);
    const rsrc_t rws = mk(ws, WS_END), rws1 = mk(ws, has1 ? WS_END : 0u), rws2 = mk(ws, has2 ? WS_END : 0u), rout = mk(out, 4096);
    const unsigned tag = __builtin_amdgcn_readfirstlane(eraw) * 0x9E3779B1u + 0x7F4A7C15u;
    const unsigned ta = tag & 0xffu, tb = (tag >> 8) & 0xffu;
    fl[F_TAG] = tag;
    lds_signal<F_LN>(vsig);
    const unsigned b1p = __builtin_amdgcn_raw_buffer_load_b32(mk(b1, has1 ? 7168u : 0u), (n16 >> 2) * 8 + (lane & 1) * 4, c1 * 32, 0);
    const int col2 = ((lane >> 1) & 1) * 8 + ((g >> 1) | ((g & 1) << 1)) * 2;
    const int fo = (lane & 1) * 2048 + col2;
    const unsigned short xfin = __builtin_amdgcn_raw_buffer_load_b16(mk(x, c < 128 ? 4096u : 0u), fo, c * 16, 0);
    const unsigned short b2fin = __builtin_amdgcn_raw_buffer_load_b16(mk(b2, c < 128 ? 2048u : 0u), col2, c * 16, 0);
    FENCE();
    lds_wait<1>(L, F_FULL, 4);
    const unsigned t4 = (unsigned)__builtin_amdgcn_s_memrealtime();
    lds_wait<2>(L, F_FULL, S1GATE);
    lds_wait<2>(L, F_DQ, 4);
    lds_wait<2>(L, F_DH, 4);
    {
      const int fvo = (lane < 16 && !(lane & 1)) ? (lane >> 2) * 16 : OOB, fb = (lane >> 1) & 1;
      const unsigned t20 = tag & 0xfffffu;
      bool needq = true, needh = has1, needg = true;
#pragma nounroll
      for (unsigned spin = 0; (needq || needh || needg) && spin < SPIN_CAP; ++spin) {
        const bool tryg = needg && !needq && (unsigned)__builtin_amdgcn_readfirstlane(fl[F_FULL]) >= QGATE;
        if (!(needq || needh || tryg)) { __builtin_amdgcn_s_sleep(1); continue; }
        const v2u GQ = ldc2(rws, needq ? lane * 8 : OOB, WS_QP + (c >> 6) * BLK + (c & 63) * 512), GH = ldc2(rws1, needh ? lane * 8 : OOB, WS_HP + (c1 >> 6) * BLK + (c1 & 63) * 512);
        v4u Q[4];
#pragma unroll
        for (int i = 0; i < 4; ++i) Q[i] = ldc4(rws, tryg ? lane * 16 : OOB, WS_QF + grp * 4096 + i * 1024);
        if (needq && !__any((((GQ.x ^ ta) | (GQ.y ^ tb)) & 0xffu) != 0u)) {
          const unsigned gr = pk(x32_sum(x16_sum(untg(GQ.x))), x32_sum(x16_sum(untg(GQ.y)))), gn = dppu<XOR1>(gr), ty = (tag << 12) | (t4 & 0xfffu);
          stc4(rws, fvo + fb * 16384, WS_QF + c * 64, v4u{gr, ty, gn, ty});
          needq = false;
        }
        if (tryg) {
          unsigned bad = 0;
#pragma unroll
          for (int i = 0; i < 4; ++i) bad |= ((Q[i].y >> 12) ^ t20) | ((Q[i].w >> 12) ^ t20);
          if (!__any(bad != 0u)) {
#pragma unroll
            for (int i = 0; i < 4; ++i) *(LAS v2u*)(L + L_QS + (2 * i + (lane >> 5)) * 272 + (lane & 31) * 8) = v2u{Q[i].x, Q[i].z};
            lds_signal<F_Q>(vsig);
            int d = 0;
#pragma unroll
            for (int i = 0; i < 4; ++i) d += (int)((Q[i].y - t4) << 20) >> 20;
            fl[F_REF] = (t4 + (int)(wave_sum((float)d) * (1.0f / 256))) | 1u;
            needg = false;
          }
        }
        if (needh && !__any((((GH.x ^ ta) | (GH.y ^ tb)) & 0xffu) != 0u)) {
          float h0 = rb(rb(x32_sum(x16_sum(untg(GH.x)))) + blo(b1p)), h1 = rb(rb(x32_sum(x16_sum(untg(GH.y)))) + bhi(b1p));
          constexpr float GC = -2.0f * 0.7978845608028654f * 1.4426950408889634f;
          h0 = h0 * rcp(1.0f + ex2(GC * (h0 + 0.044715f * h0 * h0 * h0)));
          h1 = h1 * rcp(1.0f + ex2(GC * (h1 + 0.044715f * h1 * h1 * h1)));
          const unsigned gr = pk(h0, h1), gn = dppu<XOR1>(gr);
          stc4(rws, fvo + fb * 14336, WS_HF + c1 * 64, v4u{gr, tag, gn, tag});
          needh = false;
        }
      }
      if (needg) lds_signal<F_Q>(vsig);
    }
    lds_wait<1>(L, F_P, 8);
    {
      f2 sv[8];
#pragma unroll
      for (int j = 0; j < 8; ++j) sv[j] = *(const LAS f2*)(L + L_ST + (j * 8 + (lane & 7)) * 8);
      float M = sv[0].x, Ls = 0.f;
#pragma unroll
      for (int j = 1; j < 8; ++j) M = fmaxf(M, sv[j].x);
      M = __builtin_ceilf(M);
#pragma unroll
      for (int j = 0; j < 8; ++j) Ls = fmaf(ex2(sv[j].x - M), sv[j].y, Ls);
      stc4(rws, lane < 8 ? (lane >> 2) * 2048 + (lane & 3) * 16 : OOB, WS_ST + grp * 4096 + sp * 64, v4u{asu(M), tag, asu(Ls), tag});
      const unsigned tag2 = has2 ? tag : 0u;
      v4u H;
#pragma nounroll
      for (unsigned spin = 0; spin < SPIN_CAP; ++spin) {
        H = ldc4(rws2, (lane >> 5) * 14336 + (lane & 31) * 16, WS_HF + ks2 * 512);
        if (!__any(((H.y ^ tag2) | (H.w ^ tag2)) != 0u)) break;
        __builtin_amdgcn_s_sleep(2);
      }
      *(LAS v2u*)(L + L_HS + (lane >> 5) * 272 + (lane & 31) * 8) = v2u{H.x, H.z};
      lds_signal<F_H>(vsig);
    }
    lds_wait<2>(L, F_FULL, SGATE);
    {
      v4u SG;
#pragma nounroll
      for (unsigned spin = 0; spin < SPIN_CAP; ++spin) {
        SG = ldc4(rws, (lane >> 5) * 16384 + (lane & 31) * 64, WS_ST + (ho >> 2) * 2048 + (ho & 3) * 16);
        if (!__any(((SG.y ^ tag) | (SG.w ^ tag)) != 0u)) break;
        __builtin_amdgcn_s_sleep(2);
      }
      const float m = asf(SG.x), M = x16_max(row_max(m));
      const float e = rb(ex2(m - M)), Ls = x16_sum(row_sum(e * asf(SG.z)));
      ((LAS unsigned short*)(L + L_WT))[lane] = (unsigned short)(asu(e) >> 16);
      ((LAS float*)(L + L_WT + 128))[lane >> 5] = rcp(Ls) * (1.0f + 1.0f / 131072);
      lds_signal<F_W>(vsig);
    }
    {
      const rsrc_t rwf = mk(ws, c < 128 ? WS_END : 0u);
      const unsigned mlive = lane < 48 ? 0xffffffffu : 0u;
      __builtin_amdgcn_s_barrier();
      FENCE();
      if (c < 128) {
        int so = WS_MP + (c >> 4) * BLK + (c & 15) * 2048, ov = OOB, ev = OOB, pass = 0;
        unsigned live = mlive;
        float gt = 0.f;
        const float xv = blo(xfin), bv = blo(b2fin);
        asm volatile("" : "+s"(pass));
#pragma nounroll
        for (;;) {
          v4u A0 = ldc4(rwf, lane * 16, so), A1 = ldc4(rwf, lane * 16, so + 1024);
#pragma nounroll
          for (unsigned spin = 0; spin < SPIN_CAP; ++spin) {
            const bool m0 = __any(bad4(A0, ta, tb) != 0u), m1 = __any((bad4(A1, ta, tb) & live) != 0u);
            if (!(m0 || m1)) break;
            FENCE();
            if (m0) A0 = ldc4(rwf, lane * 16, so);
            if (m1) A1 = ldc4(rwf, lane * 16, so + 1024);
          }
          const float r = rb(red4(A0, A1 & live));
          const float o = xv + rb(r * gt);
          __builtin_amdgcn_raw_buffer_store_b16((unsigned short)pk(o, o), rout, ov, c * 16, AUX_SC1);
          stc4(rws, ev, WS_E, v4u{tag, tag, tag, tag});
          SB();
          if (pass) break;
          const float m = rb(r + bv);
          gt = rb(m * rcp(1.0f + ex2(-1.4426950408889634f * m)));
          pass = 1; so -= WS_MP - WS_FP; live = 0xffffffffu;
          ov = (lane & 12) == 0 ? fo : OOB; ev = (c == 0 && lane < 4) ? lane * 16 : OOB;
          asm volatile("" : "+s"(pass), "+s"(so), "+v"(live), "+v"(ov), "+v"(ev), "+v"(gt));
        }
        { const unsigned pend = (unsigned)__builtin_amdgcn_s_memrealtime(); int d = 8 * (int)((unsigned)pb - (unsigned)pa); if (d > 6000) d = 6000; if (d < 0) d = 0; for (int it = 0; it < 100000; ++it) if ((unsigned)__builtin_amdgcn_s_memrealtime() - pend >= (unsigned)d) break; }
      }
    }
  } else {
    const rsrc_t rq = mk(wq, WQB);
    const int r8 = lane >> 3, c8 = lane & 7, f0 = r8 & 2;
    volatile LAS unsigned* fl = (volatile LAS unsigned*)(L + L_FL);
    const unsigned lds0 = (unsigned)(size_t)smem;
    Ld st = {WIN - 16, 0, lds0 + L_FL + F_FULL * 4};
    unsigned w4 = WMID - 4, to = 0x7fffffffu;
    groups<8 * 8192>(st, rq, vq0, vq1, vq0, vq1, so, m0v, m0v + (32 - WIN) * 1024, w4, to);
    to = NMID;
    {
      const int v0 = r8 * 7168 + (c8 ^ f0) * 16;
      const rsrc_t r1 = mk(w1, has1 ? W1B : 0u);
#pragma nounroll
      for (int j = 0; j < 4; ++j) {
        const int qq = (j + sp) & 3;
        int so1 = (ks1 * 256 + qq * 64) * 7168 + cg1 * 128;
        unsigned m1 = lds0 + 32768 + qq * 8192;
        groups<8 * 7168>(st, r1, v0, v0 ^ 64, v0, v0 ^ 64, so1, m1, m1 + 8192, w4, to);
      }
    }
    const int kvb = grp * 4194304 + sp * 16384, rot = grp * 3 + (sp >> 2);
    {
      const int v0 = g * 256 + (n16 ^ g) * 16;
      stream<1024, 4, 8>(L, st, mk(kc, KVB), v0, v0 ^ 64, v0 ^ 128, v0 ^ 192, kvb, rot, lds0, w4, to);
    }
    {
      const int v0 = g * 256 + (n16 ^ (2 * g)) * 16;
      stream<1024, 12, 8>(L, st, mk(vc, KVB), v0, v0 ^ 128, v0, v0 ^ 128, kvb, rot, lds0, w4, to);
    }
    {
      const int v0 = g * 2048 + (n16 ^ (2 * g)) * 16;
      stream<8192, 20, 2>(L, st, mk(w2, has2 ? W1B : 0u), v0, v0, v0 ^ 128, v0 ^ 128, ks2 * 128 * 2048 + cgo * 256, 0, lds0, w4, to);
      stream<8192, 22, 2>(L, st, mk(wo, WQB), v0, v0, v0 ^ 128, v0 ^ 128, ho * 128 * 2048 + cgo * 256, 0, lds0, w4, to);
    }
    fl[F_OG] = 1;
    asm volatile("s_waitcnt vmcnt(32)" ::: "memory");
    if (st.pub < 22) fl[F_FULL] = 22;
    asm volatile("s_waitcnt vmcnt(16)" ::: "memory");
    fl[F_FULL] = 23;
    asm volatile("s_waitcnt vmcnt(0)" ::: "memory");
    fl[F_FULL] = 24;
  }
}

}

extern "C" void kernel_launch(void* const* d_in, const int* in_sizes, int n_in, void* d_out, int out_size, void* d_ws, size_t ws_size,
                              hipStream_t stream) {
  (void)in_sizes; (void)n_in; (void)out_size; (void)ws_size;
  void* ws = (void*)(((size_t)d_ws + WS_END - 1) & ~(size_t)(WS_END - 1));
  fused_decode<<<256, NT, 0, stream>>>((const unsigned short*)d_in[0], d_in[3], ws, d_in[6], d_in[1], d_in[2], d_in[8], d_in[4], d_in[7], d_in[9], d_out);
}
```

```cpp
#include <hip/hip_runtime.h>
#include <hip/hip_bf16.h>


namespace {

typedef unsigned v4u __attribute__((ext_vector_type(4)));
typedef unsigned v2u __attribute__((ext_vector_type(2)));
typedef float f4 __attribute__((ext_vector_type(4)));
typedef float f2 __attribute__((ext_vector_type(2)));
typedef __bf16 bf8 __attribute__((ext_vector_type(8)));
typedef __bf16 bf2 __attribute__((ext_vector_type(2)));
typedef short s4 __attribute__((ext_vector_type(4)));
typedef __amdgpu_buffer_rsrc_t rsrc_t;
#define LAS __attribute__((address_space(3)))

constexpr int NWAVES = 10;
constexpr int NT = 64 * NWAVES;
constexpr unsigned KVB = 2u * 4 * 16384 * 128 * 2, WQB = 1024u * 4096 * 2, W1B = 1024u * 3584 * 2;
constexpr unsigned BLK = 65536, GAP = 32768;
constexpr unsigned WS_OP = 0;
constexpr unsigned WS_FP = GAP;
constexpr unsigned WS_MP = 8 * BLK + GAP;
constexpr unsigned WS_QP = 16 * BLK + GAP;
constexpr unsigned WS_HP = 20 * BLK + GAP;
constexpr unsigned WS_QF = 24 * BLK + GAP;
constexpr unsigned WS_HF = 25 * BLK + GAP;
constexpr unsigned WS_ST = 26 * BLK + GAP;
constexpr unsigned WS_E = 27 * BLK + GAP;
constexpr unsigned WS_END = 32 * BLK;
constexpr int L_P = 131072, L_XS = L_P, L_YS = L_P + 4096, L_QS = L_P + 16384, L_HS = L_QS + 8 * 272, L_OS = L_HS + 2 * 272,
              L_ST = L_OS + 4 * 272, L_WT = L_ST + 512, L_FL = L_WT + 256, L_SINK = L_FL + 128, L_END = L_SINK + 512;
constexpr int LDS_BYTES = L_END;
static_assert(L_END <= 163840 && (L_HS % 16) == 0 && (L_OS % 16) == 0 && (L_ST % 16) == 0 && (L_WT % 16) == 0 && (L_FL % 16) == 0, "lds map");
enum { F_FULL = 0, F_LN = 1, F_Q = 2, F_P = 3, F_H = 4, F_REF = 5, F_TAG = 6, F_W = 7, F_D = 8, F_DQ = F_D, F_DH = F_D + 2, F_DK = F_D + 4, F_DV = F_D + 12, F_OG = 25, F_WORDS = 32 };
constexpr int OOB = 0x7ff00000;
constexpr int AUX_SC1 = 16;
constexpr unsigned SPIN_CAP = 1u << 18, LSPIN_CAP = 1u << 22;
constexpr float LOG2E = 1.4426950408889634f;
constexpr int WIN = 24, WLO = 17, WMID = 25, WHI = 33, NMID = 4, NLO = 8, TAU = 56, TAU2 = 46, TOFF = 15, TTH = 20;
constexpr unsigned S1GATE = 5, QGATE = 9, SGATE = 19;
constexpr int PFBYTES = 14336;
static_assert(WIN == 24 && WHI <= 60, "first burst in whole groups inside the wq tile; vmcnt has 6 bits");

__device__ __forceinline__ rsrc_t mk(const void* p, unsigned bytes) { return __builtin_amdgcn_make_buffer_rsrc((void*)p, 0, bytes, 0x00020000); }
__device__ __forceinline__ v4u ldc4(rsrc_t r, int vo, int so) { return __builtin_amdgcn_raw_buffer_load_b128(r, vo, so, AUX_SC1); }
__device__ __forceinline__ v2u ldc2(rsrc_t r, int vo, int so) { return __builtin_amdgcn_raw_buffer_load_b64(r, vo, so, AUX_SC1); }
__device__ __forceinline__ void stc4(rsrc_t r, int vo, int so, v4u v) { __builtin_amdgcn_raw_buffer_store_b128(v, r, vo, so, AUX_SC1); }
__device__ __forceinline__ float asf(unsigned u) { return __builtin_bit_cast(float, u); }
__device__ __forceinline__ unsigned asu(float f) { return __builtin_bit_cast(unsigned, f); }
__device__ __forceinline__ unsigned pk(float a, float b) { f2 v = {a, b}; return __builtin_bit_cast(unsigned, __builtin_convertvector(v, bf2)); }
__device__ __forceinline__ float blo(unsigned u) { return asf(u << 16); }
__device__ __forceinline__ float bhi(unsigned u) { return asf(u & 0xffff0000u); }
__device__ __forceinline__ float rb(float a) { return blo(pk(a, a)); }
__device__ __forceinline__ unsigned plo(unsigned a, unsigned b) { return __builtin_amdgcn_perm(b, a, 0x05040100u); }
__device__ __forceinline__ unsigned phi(unsigned a, unsigned b) { return __builtin_amdgcn_perm(b, a, 0x07060302u); }
__device__ __forceinline__ f4 mfma32(v4u a, v4u b, f4 c) { return __builtin_amdgcn_mfma_f32_16x16x32_bf16(__builtin_bit_cast(bf8, a), __builtin_bit_cast(bf8, b), c, 0, 0, 0); }
__device__ __forceinline__ f4 mfma16(v2u a, v2u b, f4 c) { return __builtin_amdgcn_mfma_f32_16x16x16bf16_1k(__builtin_bit_cast(s4, a), __builtin_bit_cast(s4, b), c, 0, 0, 0); }
template <int C> __device__ __forceinline__ float dpp(float x) { return asf(__builtin_amdgcn_mov_dpp(asu(x), C, 0xf, 0xf, true)); }
template <int C> __device__ __forceinline__ unsigned dppu(unsigned x) { return __builtin_amdgcn_mov_dpp(x, C, 0xf, 0xf, true); }
constexpr int XOR1 = 0xB1, XOR2 = 0x4E, ROR4 = 0x124, ROR8 = 0x128;
__device__ __forceinline__ float x16_sum(float x) { auto s = __builtin_amdgcn_permlane16_swap(asu(x), asu(x), false, false); return asf(s[0]) + asf(s[1]); }
__device__ __forceinline__ float x32_sum(float x) { auto s = __builtin_amdgcn_permlane32_swap(asu(x), asu(x), false, false); return asf(s[0]) + asf(s[1]); }
__device__ __forceinline__ float x16_max(float x) { auto s = __builtin_amdgcn_permlane16_swap(asu(x), asu(x), false, false); return fmaxf(asf(s[0]), asf(s[1])); }
__device__ __forceinline__ float x32_max(float x) { auto s = __builtin_amdgcn_permlane32_swap(asu(x), asu(x), false, false); return fmaxf(asf(s[0]), asf(s[1])); }
__device__ __forceinline__ float row_sum(float x) { x += dpp<XOR1>(x); x += dpp<XOR2>(x); x += dpp<ROR4>(x); x += dpp<ROR8>(x); return x; }
__device__ __forceinline__ float row_max(float x) { x = fmaxf(x, dpp<XOR1>(x)); x = fmaxf(x, dpp<XOR2>(x)); x = fmaxf(x, dpp<ROR4>(x)); x = fmaxf(x, dpp<ROR8>(x)); return x; }
__device__ __forceinline__ float wave_sum(float x) { return x32_sum(x16_sum(row_sum(x))); }
__device__ __forceinline__ float ex2(float x) { return __builtin_amdgcn_exp2f(x); }
__device__ __forceinline__ float rcp(float x) { return __builtin_amdgcn_rcpf(x); }
__device__ __forceinline__ unsigned tg(float v, unsigned t8) { return ((asu(v) + 0x80u) & 0xffffff00u) | t8; }
__device__ __forceinline__ v4u tg4(f4 v, unsigned ta, unsigned tb) { return v4u{tg(v.x, ta), tg(v.y, tb), tg(v.z, ta), tg(v.w, tb)}; }
__device__ __forceinline__ float untg(unsigned u) { return asf(u & 0xffffff00u); }
__device__ __forceinline__ unsigned xoror(unsigned a, unsigned b, unsigned c) { return __builtin_amdgcn_bitop3_b32(a, b, c, 0xBE); }
__device__ __forceinline__ unsigned bad4(v4u u, unsigned ta, unsigned tb) { return xoror(u.x, ta, xoror(u.y, tb, xoror(u.z, ta, u.w ^ tb))) & 0xffu; }
__device__ __forceinline__ float red4(v4u a, v4u b) {
  const auto s01 = __builtin_amdgcn_permlane32_swap(asu(untg(a.x) + untg(b.x)), asu(untg(a.y) + untg(b.y)), false, false);
  const auto s23 = __builtin_amdgcn_permlane32_swap(asu(untg(a.z) + untg(b.z)), asu(untg(a.w) + untg(b.w)), false, false);
  const auto t = __builtin_amdgcn_permlane16_swap(asu(asf(s01[0]) + asf(s01[1])), asu(asf(s23[0]) + asf(s23[1])), false, false);
  float v = asf(t[0]) + asf(t[1]);
  v += dpp<ROR4>(v); v += dpp<ROR8>(v);
  return v;
}
__device__ __forceinline__ v4u trfrag(const LAS unsigned char* a, int off0, int off1) {
  const v2u lo = __builtin_bit_cast(v2u, __builtin_amdgcn_ds_read_tr16_b64_v4i16((LAS s4*)(a + off0)));
  const v2u hi = __builtin_bit_cast(v2u, __builtin_amdgcn_ds_read_tr16_b64_v4i16((LAS s4*)(a + off1)));
  return v4u{lo.x, lo.y, hi.x, hi.y};
}

#define SB() __builtin_amdgcn_sched_barrier(0)
#define FENCE() do { SB(); asm volatile("" ::: "memory"); SB(); } while (0)
#define LGKM0() do { SB(); asm volatile("s_waitcnt lgkmcnt(0)" ::: "memory"); SB(); } while (0)

template <int SLEEP> __device__ __forceinline__ void lds_wait(LAS unsigned char* L, int word, unsigned want) {
  FENCE();
  const volatile LAS unsigned* p = (const volatile LAS unsigned*)(L + L_FL) + word;
#pragma nounroll
  for (unsigned i = 0; i < LSPIN_CAP; ++i) {
    if ((unsigned)__builtin_amdgcn_readfirstlane(*p) >= want) break;
    if (SLEEP) __builtin_amdgcn_s_sleep(SLEEP);
  }
  FENCE();
}
template <int WORD> __device__ __forceinline__ void lds_signal(unsigned vsig) {
  SB();
  asm volatile("s_waitcnt lgkmcnt(0)\n\tds_add_u32 %0, %1 offset:%2" :: "v"(vsig), "v"(1u), "i"(WORD * 4) : "memory");
  SB();
}
struct Ld { int gap; unsigned pub, vfull; };
template <int GS>
__device__ __forceinline__ void groups(Ld& s, rsrc_t r, int v0, int v1, int v2, int v3, int& so, unsigned& m0v, unsigned m0end, unsigned w4, unsigned to) {
  unsigned a, b, v, n;
  asm volatile("s_mov_b32 %3, 0\n"
               "0:\n\t"
               "s_getreg_b32 %0, hwreg(HW_REG_IB_STS)\n\t"
               "s_add_u32 %3, %3, 1\n\t"
               "s_and_b32 %1, %0, 15\n\t"
               "s_bfe_u32 %0, %0, 0x20016\n\t"
               "s_lshl4_add_u32 %0, %0, %1\n\t"
               "s_cmp_le_i32 %0, %4\n\t"
               "s_cbranch_scc1 2f\n"
               "1:\n\t"
               "s_cmp_ge_u32 %3, %16\n\t"
               "s_cselect_b32 %1, %17, %15\n\t"
               "s_cmp_le_u32 %0, %1\n\t"
               "s_cbranch_scc0 0b\n\t"
               "s_mov_b32 m0, %7\n\t"
               "s_add_u32 %7, %7, 0x1000\n\t"
               "buffer_load_dwordx4 %9, %13, %6 offen nt lds\n\ts_add_u32 %6, %6, %18\n\ts_mov_b32 %3, 0\n\t"
               "buffer_load_dwordx4 %10, %13, %6 offen offset:1024 nt lds\n\ts_add_u32 %6, %6, %18\n\ts_add_u32 %4, %4, 4\n\t"
               "buffer_load_dwordx4 %11, %13, %6 offen offset:2048 nt lds\n\ts_add_u32 %6, %6, %18\n\t"
               "buffer_load_dwordx4 %12, %13, %6 offen offset:3072 nt lds\n\ts_add_u32 %6, %6, %19\n\t"
               "s_cmp_lt_u32 %7, %14\n\t"
               "s_cbranch_scc1 0b\n\t"
               "s_branch 3f\n"
               "2:\n\t"
               "s_add_u32 %5, %5, 1\n\t"
               "v_mov_b32 %2, %5\n\t"
               "ds_write_b32 %8, %2\n\t"
               "s_sub_u32 %4, %4, 16\n\t"
               "s_branch 1b\n"
               "3:"
               : "=&s"(a), "=&s"(b), "=&v"(v), "=&s"(n), "+s"(s.gap), "+s"(s.pub), "+s"(so), "+s"(m0v)
               : "v"(s.vfull), "v"(v0), "v"(v1), "v"(v2), "v"(v3), "s"(r), "s"(m0end), "s"(w4), "s"(to), "i"(WHI - 4), "i"(GS - 1024), "i"(GS + 3072)
               : "memory", "scc");
}
template <int GS, int F0, int NF>
__device__ __forceinline__ void stream(LAS unsigned char* L, Ld& s, rsrc_t r, int v0, int v1, int v2, int v3, int so, int rot, unsigned lds0, unsigned& w4, unsigned& to) {
  const int base = so;
#pragma nounroll
  for (int f = F0; f < F0 + NF; ++f) {
    unsigned m0v = lds0 + (f & 7) * 16384;
    if (NF == 8) so = base + ((f - F0 + rot) & 7) * 524288;
    if (F0 + NF <= 7) {
      groups<GS>(s, r, v0, v1, v2, v3, so, m0v, m0v + 16384, w4, to);
    } else {
      const int nf = f + 1, word = F_D + (nf < 12 ? (nf & ~1) : nf) - 8;
      const unsigned want = nf < 8 || nf > 23 ? 0u : nf < 12 ? 4u : nf < 20 ? 1u : 8u;
      unsigned vg, vr;
      unsigned long long now;
      asm volatile("ds_read_b32 %0, %2\n\tds_read_b32 %1, %3" : "=&v"(vg), "=&v"(vr) : "v"(lds0 + L_FL + word * 4), "v"(lds0 + L_FL + F_REF * 4) : "memory");
      asm volatile("s_memrealtime %0" : "=s"(now) :: "memory");
      groups<GS>(s, r, v0, v1, v2, v3, so, m0v, m0v + 16384, w4, to);
      asm volatile("s_waitcnt lgkmcnt(0)" : "+v"(vg), "+v"(vr) :: "memory");
      asm volatile("" : "+s"(now) :: "memory");
      const unsigned ref = __builtin_amdgcn_readfirstlane(vr);
      const int lag = ref ? (int)((unsigned)now - ref) - (F0 < 20 ? TOFF + (f - 5) * TAU : TOFF + 15 * TAU + (f - 20) * TAU2) : 0;
      w4 = lag > TTH ? WHI - 4 : lag < -TTH ? WLO - 4 : WMID - 4;
      to = lag < -TTH ? NLO : NMID;
      if (__builtin_expect((unsigned)__builtin_amdgcn_readfirstlane(vg) < want, 0)) lds_wait<2>(L, word, want);
    }
  }
}

__global__ void __launch_bounds__(NT, 3)
fused_decode(const unsigned short* __restrict__ x, const void* __restrict__ wq, void* ws, const void* __restrict__ w1, const void* __restrict__ kc,
             const void* __restrict__ vc, const void* __restrict__ w2, const void* __restrict__ wo, const void* __restrict__ b1,
             const void* __restrict__ b2, void* __restrict__ out) {
  __shared__ __attribute__((aligned(16))) unsigned char smem[LDS_BYTES];
  LAS unsigned char* const L = (LAS unsigned char*)smem;
  const int tid = threadIdx.x, lane = tid & 63, wv = __builtin_amdgcn_readfirstlane(tid >> 6), c = blockIdx.x;
  int vq0, vq1, so;
  unsigned m0v;
  v4u rqw;
  if (wv == 0) {
    unsigned t0;
    asm volatile("s_load_dwordx2 s[36:37], %7, 0x8\n\t"
                 "s_getpc_b64 s[40:41]\n\t"
                 "s_setprio 3\n\t"
                 "s_andn2_b32 s40, s40, 63\n\t"
                 "s_lshl_b32 %4, %8, 7\n\t"
                 "s_movk_i32 s42, %9\n\t"
                 "s_mov_b32 s43, 0x20000\n\t"
                 "s_mov_b32 m0, %10\n\t"
                 "s_and_b32 s41, s41, 0xffff\n\t"
                 "v_lshlrev_b32 %0, 4, %6\n\t"
                 "s_and_b32 %2, %4, 0xfffffc00\n\t"
                 "buffer_load_dwordx4 %0, s[40:43], %2 offen lds\n\t"
                 "v_lshrrev_b32 %1, 3, %6\n\t"
                 "v_and_b32 %0, 0x70, %0\n\t"
                 "v_lshl_or_b32 %0, %1, 13, %0\n\t"
                 "v_and_b32 %1, 16, %6\n\t"
                 "v_lshlrev_b32 %1, 1, %1\n\t"
                 "v_xor_b32 %0, %0, %1\n\t"
                 "v_xor_b32 %1, 64, %0\n\t"
                 "s_lshl_b32 %2, %8, 20\n\t"
                 "s_and_b32 %2, %2, 0x600000\n\t"
                 "s_and_b32 %4, %4, 0x80\n\t"
                 "s_or_b32 %2, %2, %4\n\t"
                 "s_lshl_b32 %4, %8, 5\n\t"
                 "s_and_b32 %4, %4, 0x1f00\n\t"
                 "s_or_b32 %2, %2, %4\n\t"
                 "s_bfe_u32 %3, %8, 0x20003\n\t"
                 "s_lshl_b32 %4, %3, 19\n\t"
                 "s_lshl_b32 %3, %3, 13\n\t"
                 "s_and_b32 vcc_lo, %2, 0x600000\n\t"
                 "s_or_b32 %2, %2, %4\n\t"
                 "s_mov_b32 %4, %13\n\t"
                 "s_mov_b32 s38, %12\n\t"
                 "s_mov_b32 s39, s43\n\t"
                 "s_waitcnt lgkmcnt(0)\n\t"
                 "s_and_b32 s37, s37, 0xffff\n"
                 "1:\n\t"
                 "s_mov_b32 m0, %3\n\t"
                 "s_add_u32 %3, %3, 0x800\n\t"
                 "buffer_load_dwordx4 %0, s[36:39], %2 offen nt lds\n\t"
                 "s_add_u32 %2, %2, 0xfc00\n\t"
                 "s_bitset0_b32 %3, 15\n\t"
                 "buffer_load_dwordx4 %1, s[36:39], %2 offen offset:1024 nt lds\n\t"
                 "s_add_u32 %2, %2, 0x10400\n\t"
                 "s_and_b32 %2, %2, 0x1fffff\n\t"
                 "s_or_b32 %2, %2, vcc_lo\n\t"
                 "s_sub_u32 %4, %4, 0x800\n\t"
                 "s_cmp_lg_u32 %4, 0\n\t"
                 "s_cbranch_scc1 1b"
                 : "=&v"(vq0), "=&v"(vq1), "=&s"(so), "=&s"(m0v), "=&s"(t0), "={s[36:39]}"(rqw)
                 : "v"(tid), "s"(__builtin_amdgcn_kernarg_segment_ptr()), "s"(c), "i"(PFBYTES), "i"(L_P + 8192), "i"(0), "i"(WQB), "i"(WIN * 1024)
                 : "memory", "scc", "vcc", "s40", "s41", "s42", "s43");
  }
  SB();
  v4u xa = {0u, 0u, 0u, 0u}, xb = xa;
  unsigned eraw = 0u;
  if (wv != 0) {
    unsigned long long xl = (unsigned long long)x;
    asm volatile("" : "+s"(xl));
    const rsrc_t rxl = mk((const void*)xl, wv < 3 ? 4096u : 0u);
    xa = __builtin_amdgcn_raw_buffer_load_b128(rxl, lane * 16, (wv - 1) * 2048, 0);
    xb = __builtin_amdgcn_raw_buffer_load_b128(rxl, lane * 16, (wv - 1) * 2048 + 1024, 0);
    eraw = __builtin_amdgcn_raw_buffer_load_b32(mk(ws, wv == NWAVES - 1 ? WS_END : 0u), 0, WS_E, AUX_SC1);
  }
  FENCE();
  const int g = lane >> 4, n16 = lane & 15;
  const int grp = c & 7, sp = c >> 3, ab = grp >> 2, ahkv = grp & 3;
  const int ksq = grp >> 1, cgq = sp * 2 + (grp & 1);
  const bool has1 = (sp & 7) != 7;
  const int ks1 = grp >> 1, cg1 = (sp - (sp >> 3)) * 2 + (grp & 1);
  const int c1 = ks1 * 56 + cg1;
  const int cgo = sp & 7;
  const int ho = grp * 4 + (sp >> 3);
  const int ks2 = (grp >> 1) * 7 + (grp & 1) * 4 + (sp >> 3);
  const bool has2 = !((grp & 1) && (sp >> 3) == 3);

  const unsigned vsig = (unsigned)(size_t)smem + (lane == 0 ? L_FL : L_SINK + lane * 4);
  if (wv == NWAVES - 1 && lane < F_WORDS) ((volatile LAS unsigned*)(L + L_FL))[lane] = 0u;
  LGKM0();
  __builtin_amdgcn_s_barrier();
  FENCE();
  if (wv >= 1 && wv < NWAVES - 1) {
    const int w = wv - 1;
    const rsrc_t rws = mk(ws, WS_END);
    const rsrc_t rws2 = mk(ws, has2 ? WS_END : 0u);
    if (w < 2) {
      float xf[16];
#pragma unroll
      for (int i = 0; i < 4; ++i) { xf[2 * i] = blo(xa[i]); xf[2 * i + 1] = bhi(xa[i]); xf[8 + 2 * i] = blo(xb[i]); xf[9 + 2 * i] = bhi(xb[i]); }
      const float piv = asf(__builtin_amdgcn_readfirstlane(asu(xf[0])));
      float s1 = 0.f, s2 = 0.f;
#pragma unroll
      for (int i = 0; i < 16; ++i) { const float d = xf[i] - piv; s1 += d; s2 = fmaf(d, d, s2); }
      float a1 = wave_sum(s1) * (1.0f / 1024), a2 = wave_sum(s2) * (1.0f / 1024);
      float mean = rb(piv + a1), inv = rcp(rb(__builtin_amdgcn_sqrtf(rb(rb(fmaxf(a2 - a1 * a1, 0.f)) + 1e-5f))));
      v4u pa, pb;
      s1 = 0.f; s2 = 0.f;
#pragma unroll
      for (int i = 0; i < 8; ++i) {
        const unsigned t = pk(xf[2 * i] - mean, xf[2 * i + 1] - mean), u = pk(blo(t) * inv, bhi(t) * inv);
        if (i < 4) pa[i] = u; else pb[i - 4] = u;
        xf[2 * i] = blo(u); xf[2 * i + 1] = bhi(u);
        s1 += xf[2 * i] + xf[2 * i + 1]; s2 = fmaf(xf[2 * i], xf[2 * i], fmaf(xf[2 * i + 1], xf[2 * i + 1], s2));
      }
      *(LAS v4u*)(L + L_XS + w * 2048 + lane * 16) = pa;
      *(LAS v4u*)(L + L_XS + w * 2048 + 1024 + lane * 16) = pb;
      a1 = wave_sum(s1) * (1.0f / 1024); a2 = wave_sum(s2) * (1.0f / 1024);
      mean = rb(a1); inv = rcp(rb(__builtin_amdgcn_sqrtf(rb(rb(fmaxf(a2 - a1 * a1, 0.f)) + 1e-5f))));
#pragma unroll
      for (int i = 0; i < 8; ++i) {
        const unsigned t = pk(xf[2 * i] - mean, xf[2 * i + 1] - mean), u = pk(blo(t) * inv, bhi(t) * inv);
        if (i < 4) pa[i] = u; else pb[i - 4] = u;
      }
      *(LAS v4u*)(L + L_YS + w * 2048 + lane * 16) = pa;
      *(LAS v4u*)(L + L_YS + w * 2048 + 1024 + lane * 16) = pb;
      lds_signal<F_LN>(vsig);
    }
    const int q4 = n16 >> 2, p4 = lane & 3;
    lds_wait<2>(L, F_LN, 3);
    const unsigned tag = __builtin_amdgcn_readfirstlane(((volatile LAS unsigned*)(L + L_FL))[F_TAG]);
    const unsigned ta = tag & 0xffu, tb = (tag >> 8) & 0xffu;
    {
      const bool isq = w < 4;
      const LAS unsigned char* ap = L + (isq ? L_XS + ksq * 512 : L_YS + (ks1 & 3) * 512) + (lane & 1) * 2048 + g * 16;
      v4u A[8];
#pragma unroll
      for (int s = 0; s < 8; ++s) A[s] = *(const LAS v4u*)(ap + s * 64);
      lds_wait<2>(L, F_FULL, isq ? 2 : 4);
      const LAS unsigned char* bp = L + (isq ? 0 : 32768) + (8 * g + q4) * 128 + (((w & 3) ^ ((q4 >> 1) | ((g & 1) << 1))) * 32) + p4 * 8;
      v4u Bf[8];
#pragma unroll
      for (int s = 0; s < 8; ++s) Bf[s] = trfrag(bp, s * 4096, s * 4096 + 512);
      lds_signal<F_DQ>(vsig + (isq ? 0 : (F_DH - F_DQ) * 4));
      f4 d0 = mfma32(Bf[0], A[0], f4{0.f, 0.f, 0.f, 0.f}), d1 = mfma32(Bf[1], A[1], f4{0.f, 0.f, 0.f, 0.f});
#pragma unroll
      for (int s = 2; s < 8; s += 2) { d0 = mfma32(Bf[s], A[s], d0); d1 = mfma32(Bf[s + 1], A[s + 1], d1); }
      const rsrc_t rp = mk(ws, (isq || has1) ? WS_END : 0u);
      const int cg = isq ? cgq : cg1;
      const int pso = (isq ? WS_QP + ksq * 128 : WS_HP + ks1 * 128) + (cg >> 4) * BLK + (cg & 15) * 2048 + (w & 3) * 512;
      stc4(rp, n16 < 2 ? g * 32 + n16 * 16 : OOB, pso, tg4(d0 + d1, ta, tb));
    }
    v4u K[16];
    {
      const LAS unsigned char* kp = L + ((4 + w) & 7) * 16384 + n16 * 256;
      lds_wait<1>(L, F_FULL, 5 + w);
#pragma unroll
      for (int s = 0; s < 4; ++s) {
        const LAS unsigned char* kq = kp + (((4 * s + g) ^ n16) * 16);
#pragma unroll
        for (int t = 0; t < 4; ++t) K[4 * t + s] = *(const LAS v4u*)(kq + t * 4096);
      }
      lds_signal<F_DK>(vsig + w * 4);
    }
    lds_wait<1>(L, F_Q, 1);
    {
      const LAS unsigned char* qb = L + L_QS + (n16 >> 1) * 272 + g * 16;
      v4u qf[4];
#pragma unroll
      for (int s = 0; s < 4; ++s) qf[s] = *(const LAS v4u*)(qb + s * 64);
      f4 sc[4];
#pragma unroll
      for (int t = 0; t < 4; ++t) sc[t] = mfma32(K[4 * t], qf[0], f4{0.f, 0.f, 0.f, 0.f});
#pragma unroll
      for (int s = 1; s < 4; ++s)
#pragma unroll
        for (int t = 0; t < 4; ++t) sc[t] = mfma32(K[4 * t + s], qf[s], sc[t]);
#pragma unroll
      for (int t = 0; t < 4; ++t)
#pragma unroll
        for (int r = 0; r < 4; r += 2) {
          const unsigned u = pk(sc[t][r], sc[t][r + 1]), v = pk(blo(u) * 0.088388347648318440f, bhi(u) * 0.088388347648318440f);
          sc[t][r] = blo(v); sc[t][r + 1] = bhi(v);
        }
      float mw = fmaxf(fmaxf(sc[0].x, sc[0].y), fmaxf(sc[0].z, sc[0].w));
#pragma unroll
      for (int t = 1; t < 4; ++t) mw = fmaxf(mw, fmaxf(fmaxf(sc[t].x, sc[t].y), fmaxf(sc[t].z, sc[t].w)));
      mw = x32_max(x16_max(mw)) * LOG2E;
      float lw = 0.f;
      const unsigned pm = (lane & 1) ? 0xffff0000u : 0u;
      unsigned P[8];
#pragma unroll
      for (int t = 0; t < 4; ++t)
#pragma unroll
        for (int r = 0; r < 4; r += 2) {
          const float v0 = ex2(fmaf(sc[t][r], LOG2E, -mw)), v1 = ex2(fmaf(sc[t][r + 1], LOG2E, -mw));
          lw += v0 + v1;
          const unsigned h = pk(v0, v1);
          P[2 * t + (r >> 1)] = pk(v0 - asf((h << 16) & pm), v1 - asf(h & pm));
        }
      *(LAS v4u*)(L + L_P + w * 2048 + lane * 16) = v4u{P[0], P[1], P[2], P[3]};
      *(LAS v4u*)(L + L_P + w * 2048 + 1024 + lane * 16) = v4u{P[4], P[5], P[6], P[7]};
      lw = x32_sum(x16_sum(lw));
      if (lane < 16 && !(lane & 1)) *(LAS f2*)(L + L_ST + (w * 8 + (lane >> 1)) * 8) = f2{mw, lw};
      lds_signal<F_P>(vsig);
    }
    lds_wait<1>(L, F_P, 8);
    float o0 = 0.f, o1 = 0.f;
    {
      f4 st[8];
#pragma unroll
      for (int j = 0; j < 8; ++j) st[j] = *(const LAS f4*)(L + L_ST + (j * 8 + 2 * g) * 8);
      float M0 = st[0].x, M1 = st[0].z;
#pragma unroll
      for (int j = 1; j < 8; ++j) { M0 = fmaxf(M0, st[j].x); M1 = fmaxf(M1, st[j].z); }
      M0 = __builtin_ceilf(M0); M1 = __builtin_ceilf(M1);
#pragma unroll
      for (int j = 0; j < 8; ++j) { st[j].x = ex2(st[j].x - M0); st[j].z = ex2(st[j].z - M1); }
      int ovs = (lane & 2) ? OOB : (2 * g + (lane & 1)) * BLK + (n16 >> 2) * 16;
      asm volatile("" : "+v"(ovs));
      const LAS unsigned char* vp = L + (4 * g + q4) * 256 + ((w ^ (4 * (g & 1) + q4)) * 32) + p4 * 8;
#pragma unroll
      for (int j = 0; j < 8; ++j) {
        const v4u pa = *(const LAS v4u*)(L + L_P + j * 2048 + lane * 16), pb = *(const LAS v4u*)(L + L_P + j * 2048 + 1024 + lane * 16);
        if (j < 7) lds_wait<2>(L, F_FULL, 13 + j); else lds_wait<0>(L, F_FULL, 20);
        const LAS unsigned char* vq = vp + ((4 + j) & 7) * 16384;
        const v4u va = trfrag(vq, 0, 4096), vb = trfrag(vq, 8192, 12288);
        if (j == 0) lds_signal<F_DV>(vsig); else if (j == 1) lds_signal<F_DV + 1>(vsig); else if (j == 2) lds_signal<F_DV + 2>(vsig); else if (j == 3) lds_signal<F_DV + 3>(vsig);
        f4 d = mfma32(pa, va, f4{0.f, 0.f, 0.f, 0.f});
        d = mfma32(pb, vb, d);
        o0 = fmaf(st[j].x, d.x + d.y, o0);
        o1 = fmaf(st[j].z, d.z + d.w, o1);
      }
      const bool odd = (lane & 1) != 0;
      const float rcv = dpp<XOR1>(odd ? o0 : o1);
      const unsigned gr = odd ? pk(rcv, o1) : pk(o0, rcv);
      const unsigned gn = dppu<XOR2>(gr);
      stc4(rws, ovs, WS_OP + ahkv * 8 * BLK + (w * 32 + sp) * 128 + ab * 64, v4u{gr, tag, gn, tag});
    }
    const LAS unsigned char* wp = L + (8 * g + q4) * 256 + ((w ^ (q4 | ((g & 1) << 2))) * 32) + p4 * 8;
    const int fvo = (g >> 1) * 2048 + (g & 1) * 32;
    {
      v4u A[4];
      lds_wait<1>(L, F_H, 1);
#pragma unroll
      for (int s = 0; s < 4; ++s) A[s] = *(const LAS v4u*)(L + L_HS + (lane & 1) * 272 + s * 64 + g * 16);
      lds_wait<2>(L, F_FULL, 22);
      f4 d = {0.f, 0.f, 0.f, 0.f};
#pragma unroll
      for (int s = 0; s < 4; ++s) d = mfma32(trfrag(wp + 4 * 16384, s * 8192, s * 8192 + 1024), A[s], d);
      stc4(rws2, n16 < 2 ? fvo + n16 * 16 : OOB, WS_MP + cgo * BLK + w * 4096 + ks2 * 64, tg4(d, ta, tb));
    }
    {
      const int ovo = ((n16 >> 2) & 1) * 2048 + g * 512 + (n16 >> 3) * 64 + (lane & 3) * 16, oso = WS_OP + ho * BLK + w * 4096;
      v4u G[4];
      lds_wait<0>(L, F_OG, 1);
#pragma unroll
      for (int j = 0; j < 4; ++j) G[j] = ldc4(rws, ovo + j * 128, oso);
      SB();
      lds_wait<0>(L, F_W, 1);
      v2u WF = *(const LAS v2u*)(L + L_WT + (lane & 3) * 32 + g * 8);
      float invl = ((const LAS float*)(L + L_WT + 128))[(lane >> 1) & 1];
      LAS unsigned char* osw = L + (n16 == g ? L_OS + g * 272 + w * 32 : L_QS + lane * 32);
      const LAS unsigned char* osr = L + L_OS + (lane & 3) * 272 + g * 16;
      const LAS unsigned char* wop = wp + 6 * 16384;
      int fv2 = (n16 & 13) == 0 ? fvo + (n16 >> 1) * 16 : OOB, fso = WS_FP + cgo * BLK + w * 4096 + ho * 64;
      unsigned vta = ta, vtb = tb, vtag = tag;
      asm volatile("" : "+v"(WF), "+v"(invl), "+v"(osw), "+v"(osr), "+v"(wop), "+v"(fv2), "+s"(fso), "+v"(vta), "+v"(vtb), "+v"(vtag));
      lds_wait<0>(L, F_FULL, 24);
      v4u Bo[4];
#pragma unroll
      for (int s = 0; s < 4; ++s) Bo[s] = trfrag(wop, s * 8192, s * 8192 + 1024);
      SB();
      const f4 z = {0.f, 0.f, 0.f, 0.f};
      f4 e0, e1, e2, e3;
#define COMBINE() do { \
        e0 = mfma16(v2u{plo(G[0].x, G[1].x), plo(G[2].x, G[3].x)}, WF, z); e1 = mfma16(v2u{phi(G[0].x, G[1].x), phi(G[2].x, G[3].x)}, WF, z); \
        e2 = mfma16(v2u{plo(G[0].z, G[1].z), plo(G[2].z, G[3].z)}, WF, z); e3 = mfma16(v2u{phi(G[0].z, G[1].z), phi(G[2].z, G[3].z)}, WF, z); } while (0)
#define OBAD(j, acc) xoror(G[j].y, vtag, xoror(G[j].w, vtag, acc))
      COMBINE();
      if (__builtin_expect(__any((OBAD(0, OBAD(1, 0u)) | OBAD(2, OBAD(3, 0u))) != 0u), 0)) {
#pragma nounroll
        for (unsigned spin = 0; spin < SPIN_CAP; ++spin) {
          const bool m0 = __any(OBAD(0, 0u) != 0u), m1 = __any(OBAD(1, 0u) != 0u), m2 = __any(OBAD(2, 0u) != 0u), m3 = __any(OBAD(3, 0u) != 0u);
          if (!(m0 || m1 || m2 || m3)) break;
          FENCE();
          if (m0) G[0] = ldc4(rws, ovo, oso);
          if (m1) G[1] = ldc4(rws, ovo + 128, oso);
          if (m2) G[2] = ldc4(rws, ovo + 256, oso);
          if (m3) G[3] = ldc4(rws, ovo + 384, oso);
        }
        COMBINE();
      }
      *(LAS v4u*)osw = v4u{pk(e0.x, e1.x), pk(e2.x, e3.x), pk(e0.y, e1.y), pk(e2.y, e3.y)};
      *(LAS v4u*)(osw + 16) = v4u{pk(e0.z, e1.z), pk(e2.z, e3.z), pk(e0.w, e1.w), pk(e2.w, e3.w)};
      LGKM0();
      __builtin_amdgcn_s_barrier();
      FENCE();
      v4u a0 = *(const LAS v4u*)osr, a1 = *(const LAS v4u*)(osr + 64), a2 = *(const LAS v4u*)(osr + 128), a3 = *(const LAS v4u*)(osr + 192);
      SB();
      f4 d = mfma32(Bo[0], a0, z), d2 = mfma32(Bo[1], a1, z);
      d = mfma32(Bo[2], a2, d); d2 = mfma32(Bo[3], a3, d2);
      d += d2;
      d.x += dpp<XOR1>(d.x); d.y += dpp<XOR1>(d.y); d.z += dpp<XOR1>(d.z); d.w += dpp<XOR1>(d.w);
      d *= invl;
      stc4(rws, fv2, fso, v4u{(asu(d.x) & 0xffffff00u) | vta, (asu(d.y) & 0xffffff00u) | vtb, (asu(d.z) & 0xffffff00u) | vta, (asu(d.w) & 0xffffff00u) | vtb});
    }
  } else if (wv == NWAVES - 1) {
    volatile LAS unsigned* fl = (volatile LAS unsigned*)(L + L_FL);
    const rsrc_t rws = mk(ws, WS_END), rws1 = mk(ws, has1 ? WS_END : 0u), rws2 = mk(ws, has2 ? WS_END : 0u), rout = mk(out, 4096);
    const unsigned tag = __builtin_amdgcn_readfirstlane(eraw) * 0x9E3779B1u + 0x7F4A7C15u;
    const unsigned ta = tag & 0xffu, tb = (tag >> 8) & 0xffu;
    fl[F_TAG] = tag;
    lds_signal<F_LN>(vsig);
    const unsigned b1p = __builtin_amdgcn_raw_buffer_load_b32(mk(b1, has1 ? 7168u : 0u), (n16 >> 2) * 8 + (lane & 1) * 4, c1 * 32, 0);
    const int col2 = ((lane >> 1) & 1) * 8 + ((g >> 1) | ((g & 1) << 1)) * 2;
    const int fo = (lane & 1) * 2048 + col2;
    const unsigned short xfin = __builtin_amdgcn_raw_buffer_load_b16(mk(x, c < 128 ? 4096u : 0u), fo, c * 16, 0);
    const unsigned short b2fin = __builtin_amdgcn_raw_buffer_load_b16(mk(b2, c < 128 ? 2048u : 0u), col2, c * 16, 0);
    FENCE();
    lds_wait<1>(L, F_FULL, 4);
    const unsigned t4 = (unsigned)__builtin_amdgcn_s_memrealtime();
    lds_wait<2>(L, F_FULL, S1GATE);
    lds_wait<2>(L, F_DQ, 4);
    lds_wait<2>(L, F_DH, 4);
    {
      const int fvo = (lane < 16 && !(lane & 1)) ? (lane >> 2) * 16 : OOB, fb = (lane >> 1) & 1;
      const unsigned t20 = tag & 0xfffffu;
      bool needq = true, needh = has1, needg = true;
#pragma nounroll
      for (unsigned spin = 0; (needq || needh || needg) && spin < SPIN_CAP; ++spin) {
        const bool tryg = needg && !needq && (unsigned)__builtin_amdgcn_readfirstlane(fl[F_FULL]) >= QGATE;
        if (!(needq || needh || tryg)) { __builtin_amdgcn_s_sleep(1); continue; }
        const v2u GQ = ldc2(rws, needq ? lane * 8 : OOB, WS_QP + (c >> 6) * BLK + (c & 63) * 512), GH = ldc2(rws1, needh ? lane * 8 : OOB, WS_HP + (c1 >> 6) * BLK + (c1 & 63) * 512);
        v4u Q[4];
#pragma unroll
        for (int i = 0; i < 4; ++i) Q[i] = ldc4(rws, tryg ? lane * 16 : OOB, WS_QF + grp * 4096 + i * 1024);
        if (needq && !__any((((GQ.x ^ ta) | (GQ.y ^ tb)) & 0xffu) != 0u)) {
          const unsigned gr = pk(x32_sum(x16_sum(untg(GQ.x))), x32_sum(x16_sum(untg(GQ.y)))), gn = dppu<XOR1>(gr), ty = (tag << 12) | (t4 & 0xfffu);
          stc4(rws, fvo + fb * 16384, WS_QF + c * 64, v4u{gr, ty, gn, ty});
          needq = false;
        }
        if (tryg) {
          unsigned bad = 0;
#pragma unroll
          for (int i = 0; i < 4; ++i) bad |= ((Q[i].y >> 12) ^ t20) | ((Q[i].w >> 12) ^ t20);
          if (!__any(bad != 0u)) {
#pragma unroll
            for (int i = 0; i < 4; ++i) *(LAS v2u*)(L + L_QS + (2 * i + (lane >> 5)) * 272 + (lane & 31) * 8) = v2u{Q[i].x, Q[i].z};
            lds_signal<F_Q>(vsig);
            int d = 0;
#pragma unroll
            for (int i = 0; i < 4; ++i) d += (int)((Q[i].y - t4) << 20) >> 20;
            fl[F_REF] = (t4 + (int)(wave_sum((float)d) * (1.0f / 256))) | 1u;
            needg = false;
          }
        }
        if (needh && !__any((((GH.x ^ ta) | (GH.y ^ tb)) & 0xffu) != 0u)) {
          float h0 = rb(rb(x32_sum(x16_sum(untg(GH.x)))) + blo(b1p)), h1 = rb(rb(x32_sum(x16_sum(untg(GH.y)))) + bhi(b1p));
          constexpr float GC = -2.0f * 0.7978845608028654f * 1.4426950408889634f;
          h0 = h0 * rcp(1.0f + ex2(GC * (h0 + 0.044715f * h0 * h0 * h0)));
          h1 = h1 * rcp(1.0f + ex2(GC * (h1 + 0.044715f * h1 * h1 * h1)));
          const unsigned gr = pk(h0, h1), gn = dppu<XOR1>(gr);
          stc4(rws, fvo + fb * 14336, WS_HF + c1 * 64, v4u{gr, tag, gn, tag});
          needh = false;
        }
      }
      if (needg) lds_signal<F_Q>(vsig);
    }
    lds_wait<1>(L, F_P, 8);
    {
      f2 sv[8];
#pragma unroll
      for (int j = 0; j < 8; ++j) sv[j] = *(const LAS f2*)(L + L_ST + (j * 8 + (lane & 7)) * 8);
      float M = sv[0].x, Ls = 0.f;
#pragma unroll
      for (int j = 1; j < 8; ++j) M = fmaxf(M, sv[j].x);
      M = __builtin_ceilf(M);
#pragma unroll
      for (int j = 0; j < 8; ++j) Ls = fmaf(ex2(sv[j].x - M), sv[j].y, Ls);
      stc4(rws, lane < 8 ? (lane >> 2) * 2048 + (lane & 3) * 16 : OOB, WS_ST + grp * 4096 + sp * 64, v4u{asu(M), tag, asu(Ls), tag});
      const unsigned tag2 = has2 ? tag : 0u;
      v4u H;
#pragma nounroll
      for (unsigned spin = 0; spin < SPIN_CAP; ++spin) {
        H = ldc4(rws2, (lane >> 5) * 14336 + (lane & 31) * 16, WS_HF + ks2 * 512);
        if (!__any(((H.y ^ tag2) | (H.w ^ tag2)) != 0u)) break;
        __builtin_amdgcn_s_sleep(2);
      }
      *(LAS v2u*)(L + L_HS + (lane >> 5) * 272 + (lane & 31) * 8) = v2u{H.x, H.z};
      lds_signal<F_H>(vsig);
    }
    lds_wait<2>(L, F_FULL, SGATE);
    {
      v4u SG;
#pragma nounroll
      for (unsigned spin = 0; spin < SPIN_CAP; ++spin) {
        SG = ldc4(rws, (lane >> 5) * 16384 + (lane & 31) * 64, WS_ST + (ho >> 2) * 2048 + (ho & 3) * 16);
        if (!__any(((SG.y ^ tag) | (SG.w ^ tag)) != 0u)) break;
        __builtin_amdgcn_s_sleep(2);
      }
      const float m = asf(SG.x), M = x16_max(row_max(m));
      const float e = rb(ex2(m - M)), Ls = x16_sum(row_sum(e * asf(SG.z)));
      ((LAS unsigned short*)(L + L_WT))[lane] = (unsigned short)(asu(e) >> 16);
      ((LAS float*)(L + L_WT + 128))[lane >> 5] = rcp(Ls) * (1.0f + 1.0f / 131072);
      lds_signal<F_W>(vsig);
    }
    {
      const rsrc_t rwf = mk(ws, c < 128 ? WS_END : 0u);
      const unsigned mlive = lane < 48 ? 0xffffffffu : 0u;
      __builtin_amdgcn_s_barrier();
      FENCE();
      if (c < 128) {
        int so = WS_MP + (c >> 4) * BLK + (c & 15) * 2048, ov = OOB, ev = OOB, pass = 0;
        unsigned live = mlive;
        float gt = 0.f;
        const float xv = blo(xfin), bv = blo(b2fin);
        asm volatile("" : "+s"(pass));
#pragma nounroll
        for (;;) {
          v4u A0 = ldc4(rwf, lane * 16, so), A1 = ldc4(rwf, lane * 16, so + 1024);
#pragma nounroll
          for (unsigned spin = 0; spin < SPIN_CAP; ++spin) {
            const bool m0 = __any(bad4(A0, ta, tb) != 0u), m1 = __any((bad4(A1, ta, tb) & live) != 0u);
            if (!(m0 || m1)) break;
            FENCE();
            if (m0) A0 = ldc4(rwf, lane * 16, so);
            if (m1) A1 = ldc4(rwf, lane * 16, so + 1024);
          }
          const float r = rb(red4(A0, A1 & live));
          const float o = xv + rb(r * gt);
          __builtin_amdgcn_raw_buffer_store_b16((unsigned short)pk(o, o), rout, ov, c * 16, AUX_SC1);
          stc4(rws, ev, WS_E, v4u{tag, tag, tag, tag});
          SB();
          if (pass) break;
          const float m = rb(r + bv);
          gt = rb(m * rcp(1.0f + ex2(-1.4426950408889634f * m)));
          pass = 1; so -= WS_MP - WS_FP; live = 0xffffffffu;
          ov = (lane & 12) == 0 ? fo : OOB; ev = (c == 0 && lane < 4) ? lane * 16 : OOB;
          asm volatile("" : "+s"(pass), "+s"(so), "+v"(live), "+v"(ov), "+v"(ev), "+v"(gt));
        }
      }
    }
  } else {
    const rsrc_t rq = mk(wq, WQB);
    const int r8 = lane >> 3, c8 = lane & 7, f0 = r8 & 2;
    volatile LAS unsigned* fl = (volatile LAS unsigned*)(L + L_FL);
    const unsigned lds0 = (unsigned)(size_t)smem;
    Ld st = {WIN - 16, 0, lds0 + L_FL + F_FULL * 4};
    unsigned w4 = WMID - 4, to = 0x7fffffffu;
    groups<8 * 8192>(st, rq, vq0, vq1, vq0, vq1, so, m0v, m0v + (32 - WIN) * 1024, w4, to);
    to = NMID;
    {
      const int v0 = r8 * 7168 + (c8 ^ f0) * 16;
      const rsrc_t r1 = mk(w1, has1 ? W1B : 0u);
#pragma nounroll
      for (int j = 0; j < 4; ++j) {
        const int qq = (j + sp) & 3;
        int so1 = (ks1 * 256 + qq * 64) * 7168 + cg1 * 128;
        unsigned m1 = lds0 + 32768 + qq * 8192;
        groups<8 * 7168>(st, r1, v0, v0 ^ 64, v0, v0 ^ 64, so1, m1, m1 + 8192, w4, to);
      }
    }
    const int kvb = grp * 4194304 + sp * 16384, rot = grp * 3 + (sp >> 2);
    {
      const int v0 = g * 256 + (n16 ^ g) * 16;
      stream<1024, 4, 8>(L, st, mk(kc, KVB), v0, v0 ^ 64, v0 ^ 128, v0 ^ 192, kvb, rot, lds0, w4, to);
    }
    {
      const int v0 = g * 256 + (n16 ^ (2 * g)) * 16;
      stream<1024, 12, 8>(L, st, mk(vc, KVB), v0, v0 ^ 128, v0, v0 ^ 128, kvb, rot, lds0, w4, to);
    }
    {
      const int v0 = g * 2048 + (n16 ^ (2 * g)) * 16;
      stream<8192, 20, 2>(L, st, mk(w2, has2 ? W1B : 0u), v0, v0, v0 ^ 128, v0 ^ 128, ks2 * 128 * 2048 + cgo * 256, 0, lds0, w4, to);
      stream<8192, 22, 2>(L, st, mk(wo, WQB), v0, v0, v0 ^ 128, v0 ^ 128, ho * 128 * 2048 + cgo * 256, 0, lds0, w4, to);
    }
    fl[F_OG] = 1;
    asm volatile("s_waitcnt vmcnt(32)" ::: "memory");
    if (st.pub < 22) fl[F_FULL] = 22;
    asm volatile("s_waitcnt vmcnt(16)" ::: "memory");
    fl[F_FULL] = 23;
    asm volatile("s_waitcnt vmcnt(0)" ::: "memory");
    fl[F_FULL] = 24;
  }
}

}

extern "C" void kernel_launch(void* const* d_in, const int* in_sizes, int n_in, void* d_out, int out_size, void* d_ws, size_t ws_size,
                              hipStream_t stream) {
  (void)in_sizes; (void)n_in; (void)out_size; (void)ws_size;
  void* ws = (void*)(((size_t)d_ws + WS_END - 1) & ~(size_t)(WS_END - 1));
  fused_decode<<<256, NT, 0, stream>>>((const unsigned short*)d_in[0], d_in[3], ws, d_in[6], d_in[1], d_in[2], d_in[8], d_in[4], d_in[7], d_in[9], d_out);
}
```
